```python
import torch

def _softmax(x, axis=-1):
    return torch.softmax(x, dim=axis)
def _gelu(x):
    return torch.nn.functional.gelu(x, approximate="tanh")
def _silu(x):
    return torch.nn.functional.silu(x)
def _sigmoid(x):
    return torch.sigmoid(x)
def _layernorm(x, axis=-1, eps=1e-5):
    m = x.mean(dim=axis, keepdim=True)
    v = x.var(dim=axis, keepdim=True, unbiased=False)
    return (x - m) / torch.sqrt(v + eps)
def _scatter_add(tgt, idx, src, axis=0):
    return tgt.clone().index_add_(axis, idx, src)
def _segment_sum(src, seg, n):
    out = torch.zeros((n,) + src.shape[1:], dtype=src.dtype)
    return out.index_add_(0, seg, src)
def _pad(x, pw):
    flat = tuple(v for pair in reversed(pw) for v in pair)
    return torch.nn.functional.pad(x, flat)

kernel_name = 'parallel_sum__sliding_block__dense_activation_relu__prompt'

def setup_inputs():

    _w = (lambda a: (lambda t: t.to(torch.bfloat16) if t.is_floating_point() else t)(torch.as_tensor(a)))
    def _A_setup():
        B, S, D = 1, 2048, 576
        return {
            "x": _w((lambda *s: torch.randn(*s, dtype=torch.bfloat16) * 0.05)(B, S, D)),
            "wqkv": _w((lambda *s: torch.randn(*s, dtype=torch.bfloat16) * 0.1)(D, 3 * D)),
            "wo": _w((lambda *s: torch.randn(*s, dtype=torch.bfloat16) * 0.05)(D, D)),
        }
    _A_in = _A_setup()

    def _B_setup():
        B, S, D, Dff = 1, 2048, 576, 4608
        return {
            "x": _w((lambda *s: torch.randn(*s, dtype=torch.bfloat16) * 0.05)(B, S, D)),
            "w1": _w((lambda *s: torch.randn(*s, dtype=torch.bfloat16) * 0.05)(D, Dff)),
            "w2": _w((lambda *s: torch.randn(*s, dtype=torch.bfloat16) * 0.05)(Dff, D)),
        }
    _B_in = _B_setup()

    return {
        "A_x": _A_in["x"],
        "A_wqkv": _A_in["wqkv"],
        "A_wo": _A_in["wo"],
        "B_x": _B_in["x"],
        "B_w1": _B_in["w1"],
        "B_w2": _B_in["w2"],
    }

def reference(A_x, A_wqkv, A_wo, B_x, B_w1, B_w2):
    def _A_ref(x, wqkv, wo):
        B, S, D = x.shape
        H = 32; Dh = D // H; W = 256
        qkv = (lambda x, *d: x.permute(*d))((x @ wqkv).reshape(B, S, 3, H, Dh), 2, 0, 3, 1, 4)
        q, k, v = qkv[0], qkv[1], qkv[2]
        scores = torch.einsum("bhqd,bhkd->bhqk", q, k) * (Dh ** -0.5)
        idx = torch.arange(S)
        mask = (idx[:, None] - idx[None, :] >= 0) & (idx[:, None] - idx[None, :] < W)
        scores = torch.where(mask[None, None], scores, -1e9)
        p = _softmax(scores, axis=-1)
        o = torch.einsum("bhqk,bhkd->bhqd", p, v)
        return (lambda x, *d: x.permute(*d))(o, 0, 2, 1, 3).reshape(B, S, D) @ wo

    def _B_ref(x, w1, w2):
        h = torch.einsum("bsd,df->bsf", x, w1)
        zero = torch.zeros_like(h)
        h = torch.maximum(h, zero)
        return torch.einsum("bsf,fd->bsd", h, w2)

    x = A_x
    xn = _layernorm(x, axis=-1)
    return x + _A_ref(xn, A_wqkv, A_wo) + _B_ref(xn, B_w1, B_w2)
```

```cpp
#include <hip/hip_runtime.h>
#include <hip/hip_bf16.h>
typedef unsigned short u16;
__device__ __forceinline__ float b2f(u16 v){ return __uint_as_float(((unsigned)v)<<16); }
__device__ __forceinline__ u16 f2b(float f){ unsigned u=__float_as_uint(f); u += 0x7fffu + ((u>>16)&1u); return (u16)(u>>16); }
constexpr int S=2048, D=576, F=4608, H=32, DH=18, W=256;

__global__ void k_ln(const u16* x, u16* xn){
  int r=blockIdx.x, t=threadIdx.x;
  float v[9]; float s=0;
  for(int i=0;i<9;i++){ v[i]=b2f(x[r*D+t+64*i]); s+=v[i]; }
  for(int o=32;o;o>>=1) s+=__shfl_xor(s,o);
  float m=s/D; float q=0;
  for(int i=0;i<9;i++){ float d=v[i]-m; q+=d*d; }
  for(int o=32;o;o>>=1) q+=__shfl_xor(q,o);
  float rs=rsqrtf(q/D+1e-5f);
  for(int i=0;i<9;i++) xn[r*D+t+64*i]=f2b((v[i]-m)*rs);
}
template<int MODE>
__global__ void k_gemm(const u16* A, const u16* B, int N, int K, u16* Cb, float* Cf, const u16* x){
  __shared__ float As[16][65], Bs[16][65];
  int bm=blockIdx.y*64, bn=blockIdx.x*64, t=threadIdx.x, tx=t&15, ty=t>>4;
  float acc[4][4]={};
  for(int k0=0;k0<K;k0+=16){
    for(int i=t;i<1024;i+=256){ int m=i>>4,k=i&15; As[k][m]=b2f(A[(size_t)(bm+m)*K+k0+k]); }
    for(int i=t;i<1024;i+=256){ int k=i>>6,n=i&63; Bs[k][n]=b2f(B[(size_t)(k0+k)*N+bn+n]); }
    __syncthreads();
    for(int k=0;k<16;k++){
      float a[4],b[4];
      for(int i=0;i<4;i++){a[i]=As[k][ty*4+i]; b[i]=Bs[k][tx*4+i];}
      for(int i=0;i<4;i++)for(int j=0;j<4;j++)acc[i][j]+=a[i]*b[j];
    }
    __syncthreads();
  }
  for(int i=0;i<4;i++)for(int j=0;j<4;j++){
    size_t o=(size_t)(bm+ty*4+i)*N+bn+tx*4+j;
    if(MODE==0) Cb[o]=f2b(acc[i][j]);
    if(MODE==1) Cb[o]=f2b(fmaxf(acc[i][j],0.f));
    if(MODE==2) Cf[o]=acc[i][j];
    if(MODE==3) Cb[o]=f2b(b2f(x[o])+acc[i][j]+Cf[o]);
  }
}
__global__ void k_attn(const u16* qkv, u16* o){
  int q=blockIdx.x*64+threadIdx.x, h=blockIdx.y;
  float qv[DH]; for(int d=0;d<DH;d++) qv[d]=b2f(qkv[(size_t)q*3*D+h*DH+d]);
  float m=-1e30f,l=0,acc[DH]={}; float sc=rsqrtf((float)DH);
  int k0=q-W+1; if(k0<0)k0=0;
  for(int k=k0;k<=q;k++){
    float s=0; for(int d=0;d<DH;d++) s+=qv[d]*b2f(qkv[(size_t)k*3*D+D+h*DH+d]);
    s*=sc; float mn=fmaxf(m,s); float a=__expf(m-mn), p=__expf(s-mn); l=l*a+p;
    for(int d=0;d<DH;d++) acc[d]=acc[d]*a+p*b2f(qkv[(size_t)k*3*D+2*D+h*DH+d]);
    m=mn;
  }
  for(int d=0;d<DH;d++) o[(size_t)q*D+h*DH+d]=f2b(acc[d]/l);
}
extern "C" void kernel_launch(void* const* d_in, const int* in_sizes, int n_in, void* d_out, int out_size, void* d_ws, size_t ws_size, hipStream_t stream){
  const u16* x=(const u16*)d_in[0]; const u16* wqkv=(const u16*)d_in[1]; const u16* wo=(const u16*)d_in[2];
  const u16* w1=(const u16*)d_in[4]; const u16* w2=(const u16*)d_in[5];
  char* ws=(char*)d_ws;
  u16* xn=(u16*)ws; u16* qkv=(u16*)(ws+(4<<20)); u16* o=(u16*)(ws+(16<<20)); u16* h=(u16*)(ws+(24<<20)); float* t=(float*)(ws+(48<<20));
  k_ln<<<S,64,0,stream>>>(x,xn);
  k_gemm<0><<<dim3(3*D/64,S/64),256,0,stream>>>(xn,wqkv,3*D,D,qkv,nullptr,nullptr);
  k_attn<<<dim3(S/64,H),64,0,stream>>>(qkv,o);
  k_gemm<1><<<dim3(F/64,S/64),256,0,stream>>>(xn,w1,F,D,h,nullptr,nullptr);
  k_gemm<2><<<dim3(D/64,S/64),256,0,stream>>>(o,wo,D,D,nullptr,t,nullptr);
  k_gemm<3><<<dim3(D/64,S/64),256,0,stream>>>(h,w2,D,F,(u16*)d_out,t,x);
}
```

```cpp
#include <hip/hip_runtime.h>
#include <hip/hip_bf16.h>

__global__ __launch_bounds__(512, 2) __attribute__((aligned(4096))) void parallel_sum__sliding_block__dense_activation_relu__prompt_kernel(const void* x, const void* wqkv, const void* wo, const void* w1,
                                                       const void* w2, void* out, void* ws) {
  __shared__ __attribute__((aligned(1024))) char lds[163840];
  const unsigned wid = __builtin_amdgcn_readfirstlane(threadIdx.x >> 6);
  asm volatile("" : : "s"((unsigned)(size_t)lds));
  asm volatile(
    "L_beg%=:\n"
    "s_load_dwordx8 s[4:11], %0, 0x0\n"
    "s_load_dwordx4 s[12:15], %0, 0x20\n"
    "s_load_dwordx2 s[16:17], %0, 0x30\n"
    "s_and_b32 s67, %1, 7\n"
    "s_lshr_b32 s71, %1, 3\n"
    "s_and_b32 s64, s67, 1\n"
    "s_lshl_b32 s64, s64, 3\n"
    "s_and_b32 s70, s71, 7\n"
    "s_add_u32 s64, s64, s70\n"
    "s_lshr_b32 s65, s67, 1\n"
    "s_lshl_b32 s65, s65, 2\n"
    "s_lshr_b32 s70, s71, 3\n"
    "s_add_u32 s65, s65, s70\n"
    "s_and_b32 s66, %2, 3\n"
    "s_lshl_b32 s69, s66, 10\n"
    "v_mbcnt_lo_u32_b32 v255, -1, 0\n"
    "v_mbcnt_hi_u32_b32 v255, -1, v255\n"
    "v_lshrrev_b32 v170, 3, v255\n"
    "v_and_b32 v171, 7, v255\n"
    "v_lshrrev_b32 v172, 4, v255\n"
    "v_xor_b32 v171, v171, v172\n"
    "v_mul_u32_u24 v170, 1152, v170\n"
    "v_lshl_add_u32 v170, v171, 4, v170\n"
    "v_xor_b32 v171, 64, v170\n"
    "v_add_u32 v171, 8192, v171\n"
    "s_lshl_b32 s67, s64, 2\n"
    "s_add_u32 s67, s67, s66\n"
    "s_mul_i32 s68, s67, 36864\n"
    "s_mul_i32 s70, s66, 18432\n"
    "s_add_u32 s70, s70, 15360\n"
    "s_mov_b32 s38, 0x240000\n"
    "s_mov_b32 s39, 0x20000\n"
    "s_cmp_ge_u32 %2, 4\n"
    "s_cbranch_scc1 L_aux%=\n"
    "s_setprio 1\n"
    "v_and_b32 v173, 15, v255\n"
    "v_bfe_u32 v174, v255, 1, 3\n"
    "v_xor_b32 v174, v174, v172\n"
    "v_lshlrev_b32 v174, 4, v174\n"
    "v_lshl_add_u32 v174, v173, 7, v174\n"
    "v_add_u32 v168, s70, v174\n"
    "v_xor_b32 v169, 64, v168\n"
    "v_mov_b32 v152, 0x3f803f80\n"
    "v_mov_b32 v153, 0x3f803f80\n"
    "v_mov_b32 v154, 0x3f803f80\n"
    "v_mov_b32 v155, 0x3f803f80\n"
    "v_and_b32 v174, 1, v255\n"
    "v_lshlrev_b32 v174, 4, v174\n"
    "v_mov_b32 v175, 0x3f80\n"
    "v_lshlrev_b32 v174, v174, v175\n"
    "v_lshrrev_b32 v175, 3, v173\n"
    "v_sub_u32 v175, v172, v175\n"
    "v_bfe_u32 v176, v255, 1, 2\n"
    "v_lshl_add_u32 v175, v175, 2, v176\n"
    "v_cmp_eq_u32 vcc, 0, v175\n"
    "v_cmp_eq_u32 s[76:77], 8, v175\n"
    "s_nop 0\n"
    "v_cndmask_b32 v156, 0, v174, vcc\n"
    "v_cndmask_b32 v160, 0, v174, s[76:77]\n"
    "v_cmp_eq_u32 vcc, 1, v175\n"
    "v_cmp_eq_u32 s[76:77], 9, v175\n"
    "s_nop 0\n"
    "v_cndmask_b32 v157, 0, v174, vcc\n"
    "v_cndmask_b32 v161, 0, v174, s[76:77]\n"
    "v_cmp_eq_u32 vcc, 2, v175\n"
    "v_cmp_eq_u32 s[76:77], 10, v175\n"
    "s_nop 0\n"
    "v_cndmask_b32 v158, 0, v174, vcc\n"
    "v_cndmask_b32 v162, 0, v174, s[76:77]\n"
    "v_cmp_eq_u32 vcc, 3, v175\n"
    "v_cmp_eq_u32 s[76:77], 11, v175\n"
    "s_nop 0\n"
    "v_cndmask_b32 v159, 0, v174, vcc\n"
    "v_cndmask_b32 v163, 0, v174, s[76:77]\n"
    "s_mov_b32 s40, 0x00f0000f\n"
    "s_mov_b32 s41, 0xf0000f00\n"
    "s_mov_b32 s76, 0x22222222\n"
    "s_mov_b32 s77, s76\n"
    "s_mov_b32 s78, 0x44444444\n"
    "s_mov_b32 s79, s78\n"
    "s_waitcnt lgkmcnt(0)\n"
    "s_mov_b32 s36, s4\n"
    "s_and_b32 s37, s5, 0xffff\n"
    "s_add_u32 m0, s70, 0\n"
    "s_add_u32 s92, s68, 0\n"
    "buffer_load_dwordx4 v170, s[36:39], s92 offen offset:0 lds\n"
    "buffer_load_dwordx4 v171, s[36:39], s92 offen offset:1024 lds\n"
    "s_add_u32 m0, s70, 2048\n"
    "s_add_u32 s92, s68, 128\n"
    "buffer_load_dwordx4 v170, s[36:39], s92 offen offset:0 lds\n"
    "buffer_load_dwordx4 v171, s[36:39], s92 offen offset:1024 lds\n"
    "s_add_u32 m0, s70, 4096\n"
    "s_add_u32 s92, s68, 256\n"
    "buffer_load_dwordx4 v170, s[36:39], s92 offen offset:0 lds\n"
    "buffer_load_dwordx4 v171, s[36:39], s92 offen offset:1024 lds\n"
    "s_mov_b32 s60, s16\n"
    "s_and_b32 s61, s17, 0xffff\n"
    "s_mov_b32 s62, 0x3000000\n"
    "s_mov_b32 s63, 0x20000\n"
    "s_mov_b32 s80, 0x88888888\n"
    "s_mov_b32 s81, s80\n"
    "s_movk_i32 s74, 0xff\n"
    "v_lshrrev_b32 v201, 2, v255\n"
    "v_and_b32 v202, 3, v255\n"
    "v_lshlrev_b32 v203, 5, v201\n"
    "v_lshlrev_b32 v201, 8, v201\n"
    "v_lshl_add_u32 v204, v202, 3, v203\n"
    "s_lshl_b32 s53, s64, 2\n"
    "s_add_u32 s53, s53, s66\n"
    "s_andn2_b32 s52, s65, 3\n"
    "s_lshr_b32 s50, s65, 3\n"
    "s_xor_b32 s51, s50, 1\n"
    "s_mul_i32 s50, s50, 3\n"
    "s_and_b32 s48, s65, 3\n"
    "s_add_u32 s49, s51, 0\n"
    "s_sub_u32 s51, s48, s51\n"
    "s_cmp_eq_u32 s48, s50\n"
    "s_cselect_b32 s48, 0, s51\n"
    "s_cselect_b32 s49, s50, s49\n"
    "s_add_u32 s50, s52, s49\n"
    "s_lshl_b32 s50, s50, 6\n"
    "s_add_u32 s50, s50, s53\n"
    "s_mul_i32 s50, s50, 2304\n"
    "s_mul_i32 s51, s48, 2304\n"
    "s_add_u32 s51, s51, s50\n"
    "s_add_u32 s51, s51, 0xff700\n"
    "s_cmp_eq_u32 s48, 0\n"
    "s_cselect_b32 s51, 0x600000, s51\n"
    "s_add_u32 s44, s50, s51\n"
    "s_and_b32 s49, s49, 1\n"
    "s_lshl_b32 s49, s49, 3\n"
    "s_add_u32 s49, s49, 0\n"
    "s_add_u32 m0, s70, 6144\n"
    "s_add_u32 s92, s68, 384\n"
    "buffer_load_dwordx4 v170, s[36:39], s92 offen offset:0 lds\n"
    "buffer_load_dwordx4 v171, s[36:39], s92 offen offset:1024 lds\n"
    "v_add_u32 v252, s49, v204\n"
    "v_add_u32 v253, 40, v252\n"
    "v_bfi_b32 v252, s74, v252, v201\n"
    "v_bfi_b32 v253, s74, v253, v201\n"
    "v_add3_u32 v251, v203, s49, 32\n"
    "s_lshr_b32 s50, s65, 3\n"
    "s_xor_b32 s51, s50, 1\n"
    "s_mul_i32 s50, s50, 3\n"
    "s_and_b32 s48, s65, 3\n"
    "s_add_u32 s49, s51, 1\n"
    "s_sub_u32 s51, s48, s51\n"
    "s_cmp_eq_u32 s48, s50\n"
    "s_cselect_b32 s48, 1, s51\n"
    "s_cselect_b32 s49, s50, s49\n"
    "s_add_u32 s50, s52, s49\n"
    "s_lshl_b32 s50, s50, 6\n"
    "s_add_u32 s50, s50, s53\n"
    "s_mul_i32 s50, s50, 2304\n"
    "s_mul_i32 s51, s48, 2304\n"
    "s_add_u32 s51, s51, s50\n"
    "s_add_u32 s51, s51, 0xff700\n"
    "s_cmp_eq_u32 s48, 0\n"
    "s_cselect_b32 s51, 0x600000, s51\n"
    "s_add_u32 s45, s50, s51\n"
    "s_and_b32 s49, s49, 1\n"
    "s_lshl_b32 s49, s49, 3\n"
    "s_add_u32 s49, s49, 80\n"
    "s_add_u32 m0, s70, 8192\n"
    "s_add_u32 s92, s68, 512\n"
    "buffer_load_dwordx4 v170, s[36:39], s92 offen offset:0 lds\n"
    "buffer_load_dwordx4 v171, s[36:39], s92 offen offset:1024 lds\n"
    "v_add_u32 v254, s49, v204\n"
    "v_add_u32 v248, 40, v254\n"
    "v_bfi_b32 v254, s74, v254, v201\n"
    "v_bfi_b32 v248, s74, v248, v201\n"
    "v_add3_u32 v205, v203, s49, 32\n"
    "v_cmp_le_u32 vcc, 1, v202\n"
    "s_nop 1\n"
    "v_cndmask_b32 v251, v251, v205, vcc\n"
    "s_lshr_b32 s50, s65, 3\n"
    "s_xor_b32 s51, s50, 1\n"
    "s_mul_i32 s50, s50, 3\n"
    "s_and_b32 s48, s65, 3\n"
    "s_add_u32 s49, s51, 2\n"
    "s_sub_u32 s51, s48, s51\n"
    "s_cmp_eq_u32 s48, s50\n"
    "s_cselect_b32 s48, 2, s51\n"
    "s_cselect_b32 s49, s50, s49\n"
    "s_add_u32 s50, s52, s49\n"
    "s_lshl_b32 s50, s50, 6\n"
    "s_add_u32 s50, s50, s53\n"
    "s_mul_i32 s50, s50, 2304\n"
    "s_mul_i32 s51, s48, 2304\n"
    "s_add_u32 s51, s51, s50\n"
    "s_add_u32 s51, s51, 0xff700\n"
    "s_cmp_eq_u32 s48, 0\n"
    "s_cselect_b32 s51, 0x600000, s51\n"
    "s_add_u32 s46, s50, s51\n"
    "s_and_b32 s49, s49, 1\n"
    "s_lshl_b32 s49, s49, 3\n"
    "s_add_u32 s49, s49, 160\n"
    "s_add_u32 m0, s70, 10240\n"
    "s_add_u32 s92, s68, 640\n"
    "buffer_load_dwordx4 v170, s[36:39], s92 offen offset:0 lds\n"
    "buffer_load_dwordx4 v171, s[36:39], s92 offen offset:1024 lds\n"
    "v_add_u32 v249, s49, v204\n"
    "v_add_u32 v250, 40, v249\n"
    "v_bfi_b32 v249, s74, v249, v201\n"
    "s_add_u32 m0, s70, 12288\n"
    "s_add_u32 s92, s68, 768\n"
    "buffer_load_dwordx4 v170, s[36:39], s92 offen offset:0 lds\n"
    "buffer_load_dwordx4 v171, s[36:39], s92 offen offset:1024 lds\n"
    "v_bfi_b32 v250, s74, v250, v201\n"
    "v_add3_u32 v205, v203, s49, 32\n"
    "v_cmp_le_u32 vcc, 2, v202\n"
    "s_nop 1\n"
    "v_cndmask_b32 v251, v251, v205, vcc\n"
    "v_bfi_b32 v251, s74, v251, v201\n"
    "s_lshl_b32 s47, s64, 4\n"
    "s_add_u32 s47, s47, s65\n"
    "s_lshl_b32 s47, s47, 2\n"
    "s_add_u32 s47, s47, s66\n"
    "s_lshl_b32 s47, s47, 8\n"
    "s_add_u32 s47, s47, 0xbf800\n"
    "s_lshl_b32 s67, s66, 11\n"
    "s_lshl_b32 s72, s64, 4\n"
    "s_add_u32 s72, s72, s65\n"
    "s_mul_i32 s72, s72, 81920\n"
    "s_add_u32 s72, s72, s67\n"
    "s_add_u32 s72, s72, s67\n"
    "s_add_u32 s72, s72, 0x900000\n"
    "s_and_b32 s93, s65, 3\n"
    "s_lshl_b32 s83, s64, 2\n"
    "s_add_u32 s83, s83, s66\n"
    "s_lshl_b32 s83, s83, 2\n"
    "s_add_u32 s73, s83, s93\n"
    "s_mul_i32 s73, s73, 40960\n"
    "s_lshr_b32 s84, s65, 2\n"
    "s_sub_u32 s42, s84, s66\n"
    "s_sub_u32 s94, s42, 1\n"
    "s_and_b32 s94, s94, 3\n"
    "s_lshl_b32 s94, s94, 10\n"
    "s_add_u32 s73, s73, s94\n"
    "s_add_u32 s73, s73, 0x2000000\n"
    "s_cmp_eq_u32 s42, 0\n"
    "s_cselect_b32 s73, 0x40000000, s73\n"
    "s_add_u32 m0, s70, 14336\n"
    "s_add_u32 s92, s68, 896\n"
    "buffer_load_dwordx4 v170, s[36:39], s92 offen offset:0 lds\n"
    "buffer_load_dwordx4 v171, s[36:39], s92 offen offset:1024 lds\n"
    "s_add_u32 s85, s73, 3072\n"
    "s_add_u32 s86, s73, 6144\n"
    "s_add_u32 s87, s73, 9216\n"
    "s_add_u32 s88, s73, 12288\n"
    "s_add_u32 s89, s73, 15360\n"
    "s_add_u32 s90, s73, 18432\n"
    "s_add_u32 s91, s73, 21504\n"
    "s_add_u32 s94, s73, 24576\n"
    "s_lshl_b32 s84, s84, 10\n"
    "s_add_u32 s84, s84, 0x2007000\n"
    "s_xor_b32 s95, s93, 1\n"
    "s_add_u32 s95, s95, s83\n"
    "s_mul_i32 s95, s95, 40960\n"
    "s_add_u32 s95, s95, s84\n"
    "s_add_u32 s84, s84, 4096\n"
    "s_xor_b32 s96, s93, 2\n"
    "s_add_u32 s96, s96, s83\n"
    "s_add_u32 m0, s70, 16384\n"
    "s_add_u32 s92, s68, 1024\n"
    "buffer_load_dwordx4 v170, s[36:39], s92 offen offset:0 lds\n"
    "buffer_load_dwordx4 v171, s[36:39], s92 offen offset:1024 lds\n"
    "s_mul_i32 s96, s96, 40960\n"
    "s_add_u32 s96, s96, s84\n"
    "s_add_u32 s84, s84, 4096\n"
    "s_xor_b32 s97, s93, 3\n"
    "s_add_u32 s97, s97, s83\n"
    "s_mul_i32 s97, s97, 40960\n"
    "s_add_u32 s97, s97, s84\n"
    "s_add_u32 s84, s84, 4096\n"
    "s_lshl_b32 s75, s64, 4\n"
    "s_add_u32 s75, s75, s65\n"
    "s_mul_i32 s75, s75, 40960\n"
    "s_mul_i32 s84, s66, 7168\n"
    "s_add_u32 s75, s75, s84\n"
    "s_add_u32 s75, s75, 0x2000000\n"
    "s_lshl_b32 s48, s64, 14\n"
    "s_cmp_eq_u32 s66, 0\n"
    "s_cselect_b32 s48, s48, 0x40000000\n"
    "s_cselect_b32 s53, 0xffff, 1\n"
    "s_lshl_b32 s50, 15, s52\n"
    "s_mov_b32 s51, 0\n"
    "s_mov_b32 s52, s53\n"
    "s_mov_b32 s53, 0\n"
    "s_lshl_b32 s49, s93, 3\n"
    "s_lshl_b32 s43, s65, 3\n"
    "s_add_u32 s68, s72, 16384\n"
    "s_add_u32 s70, s75, 4096\n"
    "s_waitcnt vmcnt(16)\n"
    "ds_read_b128 v[0:3], v168 offset:0\n"
    "ds_read_b128 v[8:11], v169 offset:0\n"
    "s_waitcnt vmcnt(14)\n"
    "ds_read_b128 v[16:19], v168 offset:2048\n"
    "ds_read_b128 v[24:27], v169 offset:2048\n"
    "s_waitcnt lgkmcnt(3)\n"
    "v_mfma_f32_16x16x32_bf16 v[144:147], v[152:155], v[0:3], 0\n"
    "v_mfma_f32_16x16x32_bf16 v[148:151], v[0:3], v[0:3], 0\n"
    "s_waitcnt lgkmcnt(2)\n"
    "v_mfma_f32_16x16x32_bf16 v[144:147], v[152:155], v[8:11], v[144:147]\n"
    "v_mfma_f32_16x16x32_bf16 v[148:151], v[8:11], v[8:11], v[148:151]\n"
    "s_waitcnt vmcnt(12)\n"
    "ds_read_b128 v[32:35], v168 offset:4096\n"
    "ds_read_b128 v[40:43], v169 offset:4096\n"
    "s_waitcnt lgkmcnt(3)\n"
    "v_mfma_f32_16x16x32_bf16 v[144:147], v[152:155], v[16:19], v[144:147]\n"
    "v_mfma_f32_16x16x32_bf16 v[148:151], v[16:19], v[16:19], v[148:151]\n"
    "s_waitcnt lgkmcnt(2)\n"
    "v_mfma_f32_16x16x32_bf16 v[144:147], v[152:155], v[24:27], v[144:147]\n"
    "v_mfma_f32_16x16x32_bf16 v[148:151], v[24:27], v[24:27], v[148:151]\n"
    "s_waitcnt vmcnt(10)\n"
    "ds_read_b128 v[48:51], v168 offset:6144\n"
    "ds_read_b128 v[56:59], v169 offset:6144\n"
    "s_waitcnt lgkmcnt(3)\n"
    "v_mfma_f32_16x16x32_bf16 v[144:147], v[152:155], v[32:35], v[144:147]\n"
    "v_mfma_f32_16x16x32_bf16 v[148:151], v[32:35], v[32:35], v[148:151]\n"
    "s_waitcnt lgkmcnt(2)\n"
    "v_mfma_f32_16x16x32_bf16 v[144:147], v[152:155], v[40:43], v[144:147]\n"
    "v_mfma_f32_16x16x32_bf16 v[148:151], v[40:43], v[40:43], v[148:151]\n"
    "s_waitcnt vmcnt(8)\n"
    "ds_read_b128 v[64:67], v168 offset:8192\n"
    "ds_read_b128 v[72:75], v169 offset:8192\n"
    "s_waitcnt lgkmcnt(3)\n"
    "v_mfma_f32_16x16x32_bf16 v[144:147], v[152:155], v[48:51], v[144:147]\n"
    "v_mfma_f32_16x16x32_bf16 v[148:151], v[48:51], v[48:51], v[148:151]\n"
    "s_waitcnt lgkmcnt(2)\n"
    "v_mfma_f32_16x16x32_bf16 v[144:147], v[152:155], v[56:59], v[144:147]\n"
    "v_mfma_f32_16x16x32_bf16 v[148:151], v[56:59], v[56:59], v[148:151]\n"
    "s_waitcnt vmcnt(6)\n"
    "ds_read_b128 v[80:83], v168 offset:10240\n"
    "ds_read_b128 v[88:91], v169 offset:10240\n"
    "s_waitcnt lgkmcnt(3)\n"
    "v_mfma_f32_16x16x32_bf16 v[144:147], v[152:155], v[64:67], v[144:147]\n"
    "v_mfma_f32_16x16x32_bf16 v[148:151], v[64:67], v[64:67], v[148:151]\n"
    "s_waitcnt lgkmcnt(2)\n"
    "v_mfma_f32_16x16x32_bf16 v[144:147], v[152:155], v[72:75], v[144:147]\n"
    "v_mfma_f32_16x16x32_bf16 v[148:151], v[72:75], v[72:75], v[148:151]\n"
    "s_waitcnt vmcnt(4)\n"
    "ds_read_b128 v[96:99], v168 offset:12288\n"
    "ds_read_b128 v[104:107], v169 offset:12288\n"
    "s_waitcnt lgkmcnt(3)\n"
    "v_mfma_f32_16x16x32_bf16 v[144:147], v[152:155], v[80:83], v[144:147]\n"
    "v_mfma_f32_16x16x32_bf16 v[148:151], v[80:83], v[80:83], v[148:151]\n"
    "s_waitcnt lgkmcnt(2)\n"
    "v_mfma_f32_16x16x32_bf16 v[144:147], v[152:155], v[88:91], v[144:147]\n"
    "v_mfma_f32_16x16x32_bf16 v[148:151], v[88:91], v[88:91], v[148:151]\n"
    "s_waitcnt vmcnt(2)\n"
    "ds_read_b128 v[112:115], v168 offset:14336\n"
    "ds_read_b128 v[120:123], v169 offset:14336\n"
    "s_waitcnt lgkmcnt(3)\n"
    "v_mfma_f32_16x16x32_bf16 v[144:147], v[152:155], v[96:99], v[144:147]\n"
    "v_mfma_f32_16x16x32_bf16 v[148:151], v[96:99], v[96:99], v[148:151]\n"
    "s_waitcnt lgkmcnt(2)\n"
    "v_mfma_f32_16x16x32_bf16 v[144:147], v[152:155], v[104:107], v[144:147]\n"
    "v_mfma_f32_16x16x32_bf16 v[148:151], v[104:107], v[104:107], v[148:151]\n"
    "s_waitcnt vmcnt(0)\n"
    "ds_read_b128 v[128:131], v168 offset:16384\n"
    "ds_read_b128 v[136:139], v169 offset:16384\n"
    "s_waitcnt lgkmcnt(3)\n"
    "v_mfma_f32_16x16x32_bf16 v[144:147], v[152:155], v[112:115], v[144:147]\n"
    "v_mfma_f32_16x16x32_bf16 v[148:151], v[112:115], v[112:115], v[148:151]\n"
    "s_waitcnt lgkmcnt(2)\n"
    "v_mfma_f32_16x16x32_bf16 v[144:147], v[152:155], v[120:123], v[144:147]\n"
    "v_mfma_f32_16x16x32_bf16 v[148:151], v[120:123], v[120:123], v[148:151]\n"
    "s_waitcnt lgkmcnt(1)\n"
    "v_mfma_f32_16x16x32_bf16 v[144:147], v[152:155], v[128:131], v[144:147]\n"
    "s_waitcnt lgkmcnt(0)\n"
    "v_mfma_f32_16x16x32_bf16 v[144:147], v[152:155], v[136:139], v[144:147]\n"
    "v_add_u32 v168, 73728, v168\n"
    "v_add_u32 v169, 73728, v169\n"
    "v_mfma_f32_16x16x32_bf16 v[148:151], v[128:131], v[128:131], v[148:151]\n"
    "v_mfma_f32_16x16x32_bf16 v[148:151], v[136:139], v[136:139], v[148:151]\n"
    "s_nop 3\n"
    "v_mul_f32 v164, 0xbae38e39, v144\n"
    "v_mul_f32 v165, 0xbae38e39, v145\n"
    "v_mul_f32 v166, 0xbae38e39, v146\n"
    "v_mul_f32 v167, 0xbae38e39, v147\n"
    "s_nop 1\n"
    "v_mfma_f32_16x16x32_bf16 v[208:211], v[156:159], v[0:3], v[164:167]\n"
    "v_mfma_f32_16x16x32_bf16 v[212:215], v[160:163], v[0:3], v[164:167]\n"
    "v_mfma_f32_16x16x32_bf16 v[216:219], v[156:159], v[8:11], v[164:167]\n"
    "v_mfma_f32_16x16x32_bf16 v[220:223], v[160:163], v[8:11], v[164:167]\n"
    "v_mfma_f32_16x16x32_bf16 v[224:227], v[156:159], v[16:19], v[164:167]\n"
    "s_nop 3\n"
    "v_cvt_pk_bf16_f32 v0, v208, v209\n"
    "v_cvt_pk_bf16_f32 v1, v210, v211\n"
    "v_mfma_f32_16x16x32_bf16 v[228:231], v[160:163], v[16:19], v[164:167]\n"
    "v_cvt_pk_bf16_f32 v2, v212, v213\n"
    "v_cvt_pk_bf16_f32 v3, v214, v215\n"
    "v_mfma_f32_16x16x32_bf16 v[208:211], v[156:159], v[24:27], v[164:167]\n"
    "v_cvt_pk_bf16_f32 v8, v216, v217\n"
    "v_cvt_pk_bf16_f32 v9, v218, v219\n"
    "v_mfma_f32_16x16x32_bf16 v[212:215], v[160:163], v[24:27], v[164:167]\n"
    "v_cvt_pk_bf16_f32 v10, v220, v221\n"
    "v_cvt_pk_bf16_f32 v11, v222, v223\n"
    "v_mfma_f32_16x16x32_bf16 v[216:219], v[156:159], v[32:35], v[164:167]\n"
    "v_cvt_pk_bf16_f32 v16, v224, v225\n"
    "v_cvt_pk_bf16_f32 v17, v226, v227\n"
    "v_mfma_f32_16x16x32_bf16 v[220:223], v[160:163], v[32:35], v[164:167]\n"
    "v_cvt_pk_bf16_f32 v18, v228, v229\n"
    "v_cvt_pk_bf16_f32 v19, v230, v231\n"
    "v_mfma_f32_16x16x32_bf16 v[224:227], v[156:159], v[40:43], v[164:167]\n"
    "v_cvt_pk_bf16_f32 v24, v208, v209\n"
    "v_cvt_pk_bf16_f32 v25, v210, v211\n"
    "v_mfma_f32_16x16x32_bf16 v[228:231], v[160:163], v[40:43], v[164:167]\n"
    "v_cvt_pk_bf16_f32 v26, v212, v213\n"
    "v_cvt_pk_bf16_f32 v27, v214, v215\n"
    "v_mfma_f32_16x16x32_bf16 v[208:211], v[156:159], v[48:51], v[164:167]\n"
    "v_cvt_pk_bf16_f32 v32, v216, v217\n"
    "v_cvt_pk_bf16_f32 v33, v218, v219\n"
    "v_mfma_f32_16x16x32_bf16 v[212:215], v[160:163], v[48:51], v[164:167]\n"
    "v_cvt_pk_bf16_f32 v34, v220, v221\n"
    "v_cvt_pk_bf16_f32 v35, v222, v223\n"
    "v_mfma_f32_16x16x32_bf16 v[216:219], v[156:159], v[56:59], v[164:167]\n"
    "v_cvt_pk_bf16_f32 v40, v224, v225\n"
    "v_cvt_pk_bf16_f32 v41, v226, v227\n"
    "v_mfma_f32_16x16x32_bf16 v[220:223], v[160:163], v[56:59], v[164:167]\n"
    "v_cvt_pk_bf16_f32 v42, v228, v229\n"
    "v_cvt_pk_bf16_f32 v43, v230, v231\n"
    "v_mfma_f32_16x16x32_bf16 v[224:227], v[156:159], v[64:67], v[164:167]\n"
    "v_cvt_pk_bf16_f32 v48, v208, v209\n"
    "v_cvt_pk_bf16_f32 v49, v210, v211\n"
    "v_mfma_f32_16x16x32_bf16 v[228:231], v[160:163], v[64:67], v[164:167]\n"
    "v_cvt_pk_bf16_f32 v50, v212, v213\n"
    "v_cvt_pk_bf16_f32 v51, v214, v215\n"
    "v_mfma_f32_16x16x32_bf16 v[208:211], v[156:159], v[72:75], v[164:167]\n"
    "v_cvt_pk_bf16_f32 v56, v216, v217\n"
    "v_cvt_pk_bf16_f32 v57, v218, v219\n"
    "v_mfma_f32_16x16x32_bf16 v[212:215], v[160:163], v[72:75], v[164:167]\n"
    "v_cvt_pk_bf16_f32 v58, v220, v221\n"
    "v_cvt_pk_bf16_f32 v59, v222, v223\n"
    "v_mfma_f32_16x16x32_bf16 v[216:219], v[156:159], v[80:83], v[164:167]\n"
    "v_cvt_pk_bf16_f32 v64, v224, v225\n"
    "v_cvt_pk_bf16_f32 v65, v226, v227\n"
    "v_mfma_f32_16x16x32_bf16 v[220:223], v[160:163], v[80:83], v[164:167]\n"
    "v_cvt_pk_bf16_f32 v66, v228, v229\n"
    "v_cvt_pk_bf16_f32 v67, v230, v231\n"
    "v_mfma_f32_16x16x32_bf16 v[224:227], v[156:159], v[88:91], v[164:167]\n"
    "v_cvt_pk_bf16_f32 v72, v208, v209\n"
    "v_cvt_pk_bf16_f32 v73, v210, v211\n"
    "v_mfma_f32_16x16x32_bf16 v[228:231], v[160:163], v[88:91], v[164:167]\n"
    "v_cvt_pk_bf16_f32 v74, v212, v213\n"
    "v_cvt_pk_bf16_f32 v75, v214, v215\n"
    "v_mfma_f32_16x16x32_bf16 v[208:211], v[156:159], v[96:99], v[164:167]\n"
    "v_cvt_pk_bf16_f32 v80, v216, v217\n"
    "v_cvt_pk_bf16_f32 v81, v218, v219\n"
    "v_mfma_f32_16x16x32_bf16 v[212:215], v[160:163], v[96:99], v[164:167]\n"
    "v_cvt_pk_bf16_f32 v82, v220, v221\n"
    "v_cvt_pk_bf16_f32 v83, v222, v223\n"
    "v_mfma_f32_16x16x32_bf16 v[216:219], v[156:159], v[104:107], v[164:167]\n"
    "v_cvt_pk_bf16_f32 v88, v224, v225\n"
    "v_cvt_pk_bf16_f32 v89, v226, v227\n"
    "v_mfma_f32_16x16x32_bf16 v[220:223], v[160:163], v[104:107], v[164:167]\n"
    "v_cvt_pk_bf16_f32 v90, v228, v229\n"
    "v_cvt_pk_bf16_f32 v91, v230, v231\n"
    "v_mfma_f32_16x16x32_bf16 v[224:227], v[156:159], v[112:115], v[164:167]\n"
    "v_cvt_pk_bf16_f32 v96, v208, v209\n"
    "v_cvt_pk_bf16_f32 v97, v210, v211\n"
    "v_mfma_f32_16x16x32_bf16 v[228:231], v[160:163], v[112:115], v[164:167]\n"
    "v_cvt_pk_bf16_f32 v98, v212, v213\n"
    "v_cvt_pk_bf16_f32 v99, v214, v215\n"
    "v_mfma_f32_16x16x32_bf16 v[208:211], v[156:159], v[120:123], v[164:167]\n"
    "v_cvt_pk_bf16_f32 v104, v216, v217\n"
    "v_cvt_pk_bf16_f32 v105, v218, v219\n"
    "v_mfma_f32_16x16x32_bf16 v[212:215], v[160:163], v[120:123], v[164:167]\n"
    "v_cvt_pk_bf16_f32 v106, v220, v221\n"
    "v_cvt_pk_bf16_f32 v107, v222, v223\n"
    "v_mfma_f32_16x16x32_bf16 v[216:219], v[156:159], v[128:131], v[164:167]\n"
    "v_cvt_pk_bf16_f32 v112, v224, v225\n"
    "v_cvt_pk_bf16_f32 v113, v226, v227\n"
    "v_mfma_f32_16x16x32_bf16 v[220:223], v[160:163], v[128:131], v[164:167]\n"
    "v_cvt_pk_bf16_f32 v114, v228, v229\n"
    "v_cvt_pk_bf16_f32 v115, v230, v231\n"
    "v_mfma_f32_16x16x32_bf16 v[224:227], v[156:159], v[136:139], v[164:167]\n"
    "v_cvt_pk_bf16_f32 v120, v208, v209\n"
    "v_cvt_pk_bf16_f32 v121, v210, v211\n"
    "v_mfma_f32_16x16x32_bf16 v[228:231], v[160:163], v[136:139], v[164:167]\n"
    "v_cvt_pk_bf16_f32 v122, v212, v213\n"
    "v_cvt_pk_bf16_f32 v123, v214, v215\n"
    "v_cvt_pk_bf16_f32 v128, v216, v217\n"
    "v_cvt_pk_bf16_f32 v129, v218, v219\n"
    "v_cvt_pk_bf16_f32 v130, v220, v221\n"
    "v_cvt_pk_bf16_f32 v131, v222, v223\n"
    "v_cvt_pk_bf16_f32 v136, v224, v225\n"
    "v_cvt_pk_bf16_f32 v137, v226, v227\n"
    "v_cvt_pk_bf16_f32 v138, v228, v229\n"
    "v_cvt_pk_bf16_f32 v139, v230, v231\n"
    "v_mov_b32 v171, v164\n"
    "s_barrier\n"
    "ds_read_b128 v[4:7], v168 offset:0\n"
    "ds_read_b128 v[12:15], v169 offset:0\n"
    "ds_read_b128 v[20:23], v168 offset:2048\n"
    "ds_read_b128 v[28:31], v169 offset:2048\n"
    "ds_read_b128 v[36:39], v168 offset:4096\n"
    "ds_read_b128 v[44:47], v169 offset:4096\n"
    "s_waitcnt lgkmcnt(5)\n"
    "v_mfma_f32_16x16x32_bf16 v[200:203], v[152:155], v[4:7], 0\n"
    "v_mfma_f32_16x16x32_bf16 v[172:175], v[4:7], v[4:7], 0\n"
    "s_waitcnt lgkmcnt(4)\n"
    "v_mfma_f32_16x16x32_bf16 v[200:203], v[152:155], v[12:15], v[200:203]\n"
    "v_mfma_f32_16x16x32_bf16 v[172:175], v[12:15], v[12:15], v[172:175]\n"
    "ds_read_b128 v[52:55], v168 offset:6144\n"
    "ds_read_b128 v[60:63], v169 offset:6144\n"
    "s_waitcnt lgkmcnt(5)\n"
    "v_mfma_f32_16x16x32_bf16 v[200:203], v[152:155], v[20:23], v[200:203]\n"
    "v_mfma_f32_16x16x32_bf16 v[172:175], v[20:23], v[20:23], v[172:175]\n"
    "s_waitcnt lgkmcnt(4)\n"
    "v_mfma_f32_16x16x32_bf16 v[200:203], v[152:155], v[28:31], v[200:203]\n"
    "v_mfma_f32_16x16x32_bf16 v[172:175], v[28:31], v[28:31], v[172:175]\n"
    "s_barrier\n"
    "ds_read_b128 v[68:71], v168 offset:8192\n"
    "ds_read_b128 v[76:79], v169 offset:8192\n"
    "s_waitcnt lgkmcnt(5)\n"
    "v_mfma_f32_16x16x32_bf16 v[200:203], v[152:155], v[36:39], v[200:203]\n"
    "v_mfma_f32_16x16x32_bf16 v[172:175], v[36:39], v[36:39], v[172:175]\n"
    "s_waitcnt lgkmcnt(4)\n"
    "v_mfma_f32_16x16x32_bf16 v[200:203], v[152:155], v[44:47], v[200:203]\n"
    "v_mfma_f32_16x16x32_bf16 v[172:175], v[44:47], v[44:47], v[172:175]\n"
    "ds_read_b128 v[84:87], v168 offset:10240\n"
    "ds_read_b128 v[92:95], v169 offset:10240\n"
    "s_waitcnt lgkmcnt(5)\n"
    "v_mfma_f32_16x16x32_bf16 v[200:203], v[152:155], v[52:55], v[200:203]\n"
    "v_mfma_f32_16x16x32_bf16 v[172:175], v[52:55], v[52:55], v[172:175]\n"
    "s_waitcnt lgkmcnt(4)\n"
    "v_mfma_f32_16x16x32_bf16 v[200:203], v[152:155], v[60:63], v[200:203]\n"
    "v_mfma_f32_16x16x32_bf16 v[172:175], v[60:63], v[60:63], v[172:175]\n"
    "s_waitcnt lgkmcnt(3)\n"
    "v_mfma_f32_16x16x32_bf16 v[200:203], v[152:155], v[68:71], v[200:203]\n"
    "v_mfma_f32_16x16x32_bf16 v[172:175], v[68:71], v[68:71], v[172:175]\n"
    "s_waitcnt lgkmcnt(2)\n"
    "v_mfma_f32_16x16x32_bf16 v[200:203], v[152:155], v[76:79], v[200:203]\n"
    "v_mfma_f32_16x16x32_bf16 v[172:175], v[76:79], v[76:79], v[172:175]\n"
    "s_barrier\n"
    "ds_read_b128 v[100:103], v168 offset:12288\n"
    "ds_read_b128 v[108:111], v169 offset:12288\n"
    "ds_read_b128 v[116:119], v168 offset:14336\n"
    "ds_read_b128 v[124:127], v169 offset:14336\n"
    "ds_read_b128 v[132:135], v168 offset:16384\n"
    "ds_read_b128 v[140:143], v169 offset:16384\n"
    "s_waitcnt lgkmcnt(7)\n"
    "v_mfma_f32_16x16x32_bf16 v[200:203], v[152:155], v[84:87], v[200:203]\n"
    "v_mfma_f32_16x16x32_bf16 v[172:175], v[84:87], v[84:87], v[172:175]\n"
    "s_waitcnt lgkmcnt(6)\n"
    "v_mfma_f32_16x16x32_bf16 v[200:203], v[152:155], v[92:95], v[200:203]\n"
    "v_mfma_f32_16x16x32_bf16 v[172:175], v[92:95], v[92:95], v[172:175]\n"
    "s_waitcnt lgkmcnt(5)\n"
    "v_mfma_f32_16x16x32_bf16 v[200:203], v[152:155], v[100:103], v[200:203]\n"
    "v_mfma_f32_16x16x32_bf16 v[172:175], v[100:103], v[100:103], v[172:175]\n"
    "s_waitcnt lgkmcnt(4)\n"
    "v_mfma_f32_16x16x32_bf16 v[200:203], v[152:155], v[108:111], v[200:203]\n"
    "v_mfma_f32_16x16x32_bf16 v[172:175], v[108:111], v[108:111], v[172:175]\n"
    "s_waitcnt lgkmcnt(3)\n"
    "v_mfma_f32_16x16x32_bf16 v[200:203], v[152:155], v[116:119], v[200:203]\n"
    "v_mfma_f32_16x16x32_bf16 v[172:175], v[116:119], v[116:119], v[172:175]\n"
    "s_waitcnt lgkmcnt(2)\n"
    "v_mfma_f32_16x16x32_bf16 v[200:203], v[152:155], v[124:127], v[200:203]\n"
    "v_mfma_f32_16x16x32_bf16 v[172:175], v[124:127], v[124:127], v[172:175]\n"
    "s_waitcnt lgkmcnt(1)\n"
    "v_mfma_f32_16x16x32_bf16 v[200:203], v[152:155], v[132:135], v[200:203]\n"
    "s_waitcnt lgkmcnt(0)\n"
    "v_mfma_f32_16x16x32_bf16 v[200:203], v[152:155], v[140:143], v[200:203]\n"
    "v_mfma_f32_16x16x32_bf16 v[172:175], v[132:135], v[132:135], v[172:175]\n"
    "v_mfma_f32_16x16x32_bf16 v[172:175], v[140:143], v[140:143], v[172:175]\n"
    "s_nop 5\n"
    "v_mul_f32 v164, 0xbae38e39, v200\n"
    "v_mul_f32 v165, 0xbae38e39, v201\n"
    "v_mul_f32 v166, 0xbae38e39, v202\n"
    "v_mul_f32 v167, 0xbae38e39, v203\n"
    "s_nop 1\n"
    "v_mfma_f32_16x16x32_bf16 v[208:211], v[156:159], v[4:7], v[164:167]\n"
    "v_mfma_f32_16x16x32_bf16 v[212:215], v[160:163], v[4:7], v[164:167]\n"
    "v_mfma_f32_16x16x32_bf16 v[216:219], v[156:159], v[12:15], v[164:167]\n"
    "v_mfma_f32_16x16x32_bf16 v[220:223], v[160:163], v[12:15], v[164:167]\n"
    "v_mfma_f32_16x16x32_bf16 v[224:227], v[156:159], v[20:23], v[164:167]\n"
    "s_nop 3\n"
    "v_cvt_pk_bf16_f32 v4, v208, v209\n"
    "v_cvt_pk_bf16_f32 v5, v210, v211\n"
    "v_mfma_f32_16x16x32_bf16 v[228:231], v[160:163], v[20:23], v[164:167]\n"
    "v_cvt_pk_bf16_f32 v6, v212, v213\n"
    "v_cvt_pk_bf16_f32 v7, v214, v215\n"
    "v_mfma_f32_16x16x32_bf16 v[208:211], v[156:159], v[28:31], v[164:167]\n"
    "v_cvt_pk_bf16_f32 v12, v216, v217\n"
    "v_cvt_pk_bf16_f32 v13, v218, v219\n"
    "v_mfma_f32_16x16x32_bf16 v[212:215], v[160:163], v[28:31], v[164:167]\n"
    "v_cvt_pk_bf16_f32 v14, v220, v221\n"
    "v_cvt_pk_bf16_f32 v15, v222, v223\n"
    "v_mfma_f32_16x16x32_bf16 v[216:219], v[156:159], v[36:39], v[164:167]\n"
    "v_cvt_pk_bf16_f32 v20, v224, v225\n"
    "v_cvt_pk_bf16_f32 v21, v226, v227\n"
    "v_mfma_f32_16x16x32_bf16 v[220:223], v[160:163], v[36:39], v[164:167]\n"
    "v_cvt_pk_bf16_f32 v22, v228, v229\n"
    "v_cvt_pk_bf16_f32 v23, v230, v231\n"
    "v_mfma_f32_16x16x32_bf16 v[224:227], v[156:159], v[44:47], v[164:167]\n"
    "v_cvt_pk_bf16_f32 v28, v208, v209\n"
    "v_cvt_pk_bf16_f32 v29, v210, v211\n"
    "v_mfma_f32_16x16x32_bf16 v[228:231], v[160:163], v[44:47], v[164:167]\n"
    "v_cvt_pk_bf16_f32 v30, v212, v213\n"
    "v_cvt_pk_bf16_f32 v31, v214, v215\n"
    "v_mfma_f32_16x16x32_bf16 v[208:211], v[156:159], v[52:55], v[164:167]\n"
    "v_cvt_pk_bf16_f32 v36, v216, v217\n"
    "v_cvt_pk_bf16_f32 v37, v218, v219\n"
    "v_mfma_f32_16x16x32_bf16 v[212:215], v[160:163], v[52:55], v[164:167]\n"
    "v_cvt_pk_bf16_f32 v38, v220, v221\n"
    "v_cvt_pk_bf16_f32 v39, v222, v223\n"
    "v_mfma_f32_16x16x32_bf16 v[216:219], v[156:159], v[60:63], v[164:167]\n"
    "v_cvt_pk_bf16_f32 v44, v224, v225\n"
    "v_cvt_pk_bf16_f32 v45, v226, v227\n"
    "v_mfma_f32_16x16x32_bf16 v[220:223], v[160:163], v[60:63], v[164:167]\n"
    "v_cvt_pk_bf16_f32 v46, v228, v229\n"
    "v_cvt_pk_bf16_f32 v47, v230, v231\n"
    "v_mfma_f32_16x16x32_bf16 v[224:227], v[156:159], v[68:71], v[164:167]\n"
    "v_cvt_pk_bf16_f32 v52, v208, v209\n"
    "v_cvt_pk_bf16_f32 v53, v210, v211\n"
    "v_mfma_f32_16x16x32_bf16 v[228:231], v[160:163], v[68:71], v[164:167]\n"
    "v_cvt_pk_bf16_f32 v54, v212, v213\n"
    "v_cvt_pk_bf16_f32 v55, v214, v215\n"
    "v_mfma_f32_16x16x32_bf16 v[208:211], v[156:159], v[76:79], v[164:167]\n"
    "v_cvt_pk_bf16_f32 v60, v216, v217\n"
    "v_cvt_pk_bf16_f32 v61, v218, v219\n"
    "v_mfma_f32_16x16x32_bf16 v[212:215], v[160:163], v[76:79], v[164:167]\n"
    "v_cvt_pk_bf16_f32 v62, v220, v221\n"
    "v_cvt_pk_bf16_f32 v63, v222, v223\n"
    "s_nop 0\n"
    "L_st1_%=:\n"
    "s_cbranch_execz L_bk%=\n"
    "v_mfma_f32_16x16x32_bf16 v[216:219], v[156:159], v[84:87], v[164:167]\n"
    "v_cvt_pk_bf16_f32 v68, v224, v225\n"
    "v_cvt_pk_bf16_f32 v69, v226, v227\n"
    "v_mfma_f32_16x16x32_bf16 v[220:223], v[160:163], v[84:87], v[164:167]\n"
    "v_cvt_pk_bf16_f32 v70, v228, v229\n"
    "v_cvt_pk_bf16_f32 v71, v230, v231\n"
    "v_mfma_f32_16x16x32_bf16 v[224:227], v[156:159], v[92:95], v[164:167]\n"
    "v_cvt_pk_bf16_f32 v76, v208, v209\n"
    "v_cvt_pk_bf16_f32 v77, v210, v211\n"
    "v_mfma_f32_16x16x32_bf16 v[228:231], v[160:163], v[92:95], v[164:167]\n"
    "v_cvt_pk_bf16_f32 v78, v212, v213\n"
    "v_cvt_pk_bf16_f32 v79, v214, v215\n"
    "v_mfma_f32_16x16x32_bf16 v[208:211], v[156:159], v[100:103], v[164:167]\n"
    "v_cvt_pk_bf16_f32 v84, v216, v217\n"
    "v_cvt_pk_bf16_f32 v85, v218, v219\n"
    "v_mfma_f32_16x16x32_bf16 v[212:215], v[160:163], v[100:103], v[164:167]\n"
    "v_cvt_pk_bf16_f32 v86, v220, v221\n"
    "v_cvt_pk_bf16_f32 v87, v222, v223\n"
    "v_mfma_f32_16x16x32_bf16 v[216:219], v[156:159], v[108:111], v[164:167]\n"
    "v_cvt_pk_bf16_f32 v92, v224, v225\n"
    "v_cvt_pk_bf16_f32 v93, v226, v227\n"
    "v_mfma_f32_16x16x32_bf16 v[220:223], v[160:163], v[108:111], v[164:167]\n"
    "v_cvt_pk_bf16_f32 v94, v228, v229\n"
    "v_cvt_pk_bf16_f32 v95, v230, v231\n"
    "v_mfma_f32_16x16x32_bf16 v[224:227], v[156:159], v[116:119], v[164:167]\n"
    "v_cvt_pk_bf16_f32 v100, v208, v209\n"
    "v_cvt_pk_bf16_f32 v101, v210, v211\n"
    "v_mfma_f32_16x16x32_bf16 v[228:231], v[160:163], v[116:119], v[164:167]\n"
    "v_cvt_pk_bf16_f32 v102, v212, v213\n"
    "v_cvt_pk_bf16_f32 v103, v214, v215\n"
    "v_mfma_f32_16x16x32_bf16 v[208:211], v[156:159], v[124:127], v[164:167]\n"
    "v_cvt_pk_bf16_f32 v108, v216, v217\n"
    "v_cvt_pk_bf16_f32 v109, v218, v219\n"
    "v_mfma_f32_16x16x32_bf16 v[212:215], v[160:163], v[124:127], v[164:167]\n"
    "v_cvt_pk_bf16_f32 v110, v220, v221\n"
    "v_cvt_pk_bf16_f32 v111, v222, v223\n"
    "v_mfma_f32_16x16x32_bf16 v[216:219], v[156:159], v[132:135], v[164:167]\n"
    "v_cvt_pk_bf16_f32 v116, v224, v225\n"
    "v_cvt_pk_bf16_f32 v117, v226, v227\n"
    "v_mfma_f32_16x16x32_bf16 v[220:223], v[160:163], v[132:135], v[164:167]\n"
    "v_cvt_pk_bf16_f32 v118, v228, v229\n"
    "v_cvt_pk_bf16_f32 v119, v230, v231\n"
    "s_barrier\n"
    "ds_read_b64_tr_b16 v[232:233], v252 offset:0\n"
    "ds_read_b64_tr_b16 v[234:235], v252 offset:4096\n"
    "ds_read_b64_tr_b16 v[236:237], v253 offset:0\n"
    "ds_read_b64_tr_b16 v[238:239], v253 offset:4096\n"
    "ds_read_b64_tr_b16 v[240:241], v254 offset:0\n"
    "ds_read_b64_tr_b16 v[242:243], v254 offset:4096\n"
    "v_mfma_f32_16x16x32_bf16 v[224:227], v[156:159], v[140:143], v[164:167]\n"
    "v_cvt_pk_bf16_f32 v124, v208, v209\n"
    "v_cvt_pk_bf16_f32 v125, v210, v211\n"
    "v_mfma_f32_16x16x32_bf16 v[228:231], v[160:163], v[140:143], v[164:167]\n"
    "v_cvt_pk_bf16_f32 v126, v212, v213\n"
    "v_cvt_pk_bf16_f32 v127, v214, v215\n"
    "v_cvt_pk_bf16_f32 v132, v216, v217\n"
    "v_cvt_pk_bf16_f32 v133, v218, v219\n"
    "v_cvt_pk_bf16_f32 v134, v220, v221\n"
    "v_cvt_pk_bf16_f32 v135, v222, v223\n"
    "v_cvt_pk_bf16_f32 v140, v224, v225\n"
    "v_cvt_pk_bf16_f32 v141, v226, v227\n"
    "v_cvt_pk_bf16_f32 v142, v228, v229\n"
    "v_cvt_pk_bf16_f32 v143, v230, v231\n"
    "s_waitcnt lgkmcnt(4)\n"
    "v_mfma_f32_16x16x32_bf16 v[176:179], v[232:235], v[0:3], 0\n"
    "ds_read_b64_tr_b16 v[244:245], v248 offset:0\n"
    "v_cndmask_b32 v153, v148, v149, s[76:77]\n"
    "v_mfma_f32_16x16x32_bf16 v[180:183], v[232:235], v[4:7], 0\n"
    "ds_read_b64_tr_b16 v[246:247], v248 offset:4096\n"
    "s_waitcnt lgkmcnt(4)\n"
    "v_mfma_f32_16x16x32_bf16 v[188:191], v[236:239], v[4:7], 0\n"
    "ds_read_b64_tr_b16 v[232:233], v249 offset:0\n"
    "v_cndmask_b32 v153, v153, v150, s[78:79]\n"
    "v_mfma_f32_16x16x32_bf16 v[184:187], v[236:239], v[0:3], 0\n"
    "ds_read_b64_tr_b16 v[234:235], v249 offset:4096\n"
    "s_waitcnt lgkmcnt(4)\n"
    "v_mfma_f32_16x16x32_bf16 v[192:195], v[240:243], v[0:3], 0\n"
    "ds_read_b64_tr_b16 v[236:237], v250 offset:0\n"
    "v_cndmask_b32 v153, v153, v151, s[80:81]\n"
    "v_mfma_f32_16x16x32_bf16 v[196:199], v[240:243], v[4:7], 0\n"
    "ds_read_b64_tr_b16 v[238:239], v250 offset:4096\n"
    "s_waitcnt lgkmcnt(4)\n"
    "v_mfma_f32_16x16x32_bf16 v[204:207], v[244:247], v[4:7], 0\n"
    "ds_read_b64_tr_b16 v[240:241], v251 offset:0\n"
    "v_cndmask_b32 v153, 0, v153, s[40:41]\n"
    "v_mfma_f32_16x16x32_bf16 v[200:203], v[244:247], v[0:3], 0\n"
    "ds_read_b64_tr_b16 v[242:243], v251 offset:4096\n"
    "s_waitcnt lgkmcnt(4)\n"
    "v_mfma_f32_16x16x32_bf16 v[208:211], v[232:235], v[0:3], 0\n"
    "ds_read_b64_tr_b16 v[244:245], v252 offset:8192\n"
    "v_mov_b32 v152, v153\n"
    "v_mfma_f32_16x16x32_bf16 v[212:215], v[232:235], v[4:7], 0\n"
    "ds_read_b64_tr_b16 v[246:247], v252 offset:12288\n"
    "s_waitcnt lgkmcnt(4)\n"
    "v_mfma_f32_16x16x32_bf16 v[220:223], v[236:239], v[4:7], 0\n"
    "ds_read_b64_tr_b16 v[232:233], v253 offset:8192\n"
    "v_permlane32_swap_b32 v152, v153\n"
    "v_mfma_f32_16x16x32_bf16 v[216:219], v[236:239], v[0:3], 0\n"
    "ds_read_b64_tr_b16 v[234:235], v253 offset:12288\n"
    "s_waitcnt lgkmcnt(4)\n"
    "v_mfma_f32_16x16x32_bf16 v[224:227], v[240:243], v[0:3], 0\n"
    "ds_read_b64_tr_b16 v[236:237], v254 offset:8192\n"
    "v_add_f32 v153, v152, v153\n"
    "v_mfma_f32_16x16x32_bf16 v[228:231], v[240:243], v[4:7], 0\n"
    "ds_read_b64_tr_b16 v[238:239], v254 offset:12288\n"
    "s_waitcnt lgkmcnt(4)\n"
    "v_mfma_f32_16x16x32_bf16 v[176:179], v[244:247], v[8:11], v[176:179]\n"
    "ds_read_b64_tr_b16 v[240:241], v248 offset:8192\n"
    "v_mov_b32 v152, v153\n"
    "v_mfma_f32_16x16x32_bf16 v[180:183], v[244:247], v[12:15], v[180:183]\n"
    "ds_read_b64_tr_b16 v[242:243], v248 offset:12288\n"
    "s_waitcnt lgkmcnt(4)\n"
    "v_mfma_f32_16x16x32_bf16 v[188:191], v[232:235], v[12:15], v[188:191]\n"
    "ds_read_b64_tr_b16 v[244:245], v249 offset:8192\n"
    "v_permlane16_swap_b32 v152, v153\n"
    "v_mfma_f32_16x16x32_bf16 v[184:187], v[232:235], v[8:11], v[184:187]\n"
    "ds_read_b64_tr_b16 v[246:247], v249 offset:12288\n"
    "s_waitcnt lgkmcnt(4)\n"
    "v_mfma_f32_16x16x32_bf16 v[192:195], v[236:239], v[8:11], v[192:195]\n"
    "ds_read_b64_tr_b16 v[232:233], v250 offset:8192\n"
    "v_add_f32 v153, v152, v153\n"
    "v_mfma_f32_16x16x32_bf16 v[196:199], v[236:239], v[12:15], v[196:199]\n"
    "ds_read_b64_tr_b16 v[234:235], v250 offset:12288\n"
    "s_waitcnt lgkmcnt(4)\n"
    "v_mfma_f32_16x16x32_bf16 v[204:207], v[240:243], v[12:15], v[204:207]\n"
    "ds_read_b64_tr_b16 v[236:237], v251 offset:8192\n"
    "v_mul_f32 v153, 0x3ae38e39, v153\n"
    "v_mfma_f32_16x16x32_bf16 v[200:203], v[240:243], v[8:11], v[200:203]\n"
    "ds_read_b64_tr_b16 v[238:239], v251 offset:12288\n"
    "s_waitcnt lgkmcnt(4)\n"
    "v_mfma_f32_16x16x32_bf16 v[208:211], v[244:247], v[8:11], v[208:211]\n"
    "ds_read_b64_tr_b16 v[240:241], v252 offset:16384\n"
    "v_fma_f32 v153, -v171, v171, v153\n"
    "v_mfma_f32_16x16x32_bf16 v[212:215], v[244:247], v[12:15], v[212:215]\n"
    "ds_read_b64_tr_b16 v[242:243], v252 offset:20480\n"
    "s_waitcnt lgkmcnt(4)\n"
    "v_mfma_f32_16x16x32_bf16 v[220:223], v[232:235], v[12:15], v[220:223]\n"
    "ds_read_b64_tr_b16 v[244:245], v253 offset:16384\n"
    "v_max_f32 v153, 0, v153\n"
    "v_mfma_f32_16x16x32_bf16 v[216:219], v[232:235], v[8:11], v[216:219]\n"
    "ds_read_b64_tr_b16 v[246:247], v253 offset:20480\n"
    "s_waitcnt lgkmcnt(4)\n"
    "v_mfma_f32_16x16x32_bf16 v[224:227], v[236:239], v[8:11], v[224:227]\n"
    "ds_read_b64_tr_b16 v[232:233], v254 offset:16384\n"
    "v_add_f32 v153, 0x3727c5ac, v153\n"
    "v_mfma_f32_16x16x32_bf16 v[228:231], v[236:239], v[12:15], v[228:231]\n"
    "ds_read_b64_tr_b16 v[234:235], v254 offset:20480\n"
    "s_waitcnt lgkmcnt(4)\n"
    "v_mfma_f32_16x16x32_bf16 v[176:179], v[240:243], v[16:19], v[176:179]\n"
    "ds_read_b64_tr_b16 v[236:237], v248 offset:16384\n"
    "v_rsq_f32 v153, v153\n"
    "v_mfma_f32_16x16x32_bf16 v[180:183], v[240:243], v[20:23], v[180:183]\n"
    "ds_read_b64_tr_b16 v[238:239], v248 offset:20480\n"
    "s_waitcnt lgkmcnt(4)\n"
    "v_mfma_f32_16x16x32_bf16 v[188:191], v[244:247], v[20:23], v[188:191]\n"
    "ds_read_b64_tr_b16 v[240:241], v249 offset:16384\n"
    "v_mul_f32 v154, 0x3f154857, v153\n"
    "v_mfma_f32_16x16x32_bf16 v[184:187], v[244:247], v[16:19], v[184:187]\n"
    "ds_read_b64_tr_b16 v[242:243], v249 offset:20480\n"
    "s_waitcnt lgkmcnt(4)\n"
    "v_mfma_f32_16x16x32_bf16 v[192:195], v[232:235], v[16:19], v[192:195]\n"
    "ds_read_b64_tr_b16 v[244:245], v250 offset:16384\n"
    "v_cndmask_b32 v153, v172, v173, s[76:77]\n"
    "v_mfma_f32_16x16x32_bf16 v[196:199], v[232:235], v[20:23], v[196:199]\n"
    "ds_read_b64_tr_b16 v[246:247], v250 offset:20480\n"
    "s_waitcnt lgkmcnt(4)\n"
    "v_mfma_f32_16x16x32_bf16 v[204:207], v[236:239], v[20:23], v[204:207]\n"
    "ds_read_b64_tr_b16 v[232:233], v251 offset:16384\n"
    "v_cndmask_b32 v153, v153, v174, s[78:79]\n"
    "v_mfma_f32_16x16x32_bf16 v[200:203], v[236:239], v[16:19], v[200:203]\n"
    "ds_read_b64_tr_b16 v[234:235], v251 offset:20480\n"
    "s_waitcnt lgkmcnt(4)\n"
    "v_mfma_f32_16x16x32_bf16 v[208:211], v[240:243], v[16:19], v[208:211]\n"
    "s_barrier\n"
    "ds_read_b64_tr_b16 v[236:237], v252 offset:24576\n"
    "v_mfma_f32_16x16x32_bf16 v[212:215], v[240:243], v[20:23], v[212:215]\n"
    "ds_read_b64_tr_b16 v[238:239], v252 offset:28672\n"
    "s_waitcnt lgkmcnt(4)\n"
    "v_mfma_f32_16x16x32_bf16 v[220:223], v[244:247], v[20:23], v[220:223]\n"
    "ds_read_b64_tr_b16 v[240:241], v253 offset:24576\n"
    "v_cndmask_b32 v153, v153, v175, s[80:81]\n"
    "v_mfma_f32_16x16x32_bf16 v[216:219], v[244:247], v[16:19], v[216:219]\n"
    "ds_read_b64_tr_b16 v[242:243], v253 offset:28672\n"
    "s_waitcnt lgkmcnt(4)\n"
    "v_mfma_f32_16x16x32_bf16 v[224:227], v[232:235], v[16:19], v[224:227]\n"
    "ds_read_b64_tr_b16 v[244:245], v254 offset:24576\n"
    "v_cndmask_b32 v153, 0, v153, s[40:41]\n"
    "v_mfma_f32_16x16x32_bf16 v[228:231], v[232:235], v[20:23], v[228:231]\n"
    "ds_read_b64_tr_b16 v[246:247], v254 offset:28672\n"
    "s_waitcnt lgkmcnt(4)\n"
    "v_mfma_f32_16x16x32_bf16 v[176:179], v[236:239], v[24:27], v[176:179]\n"
    "ds_read_b64_tr_b16 v[232:233], v248 offset:24576\n"
    "v_mov_b32 v152, v153\n"
    "v_mfma_f32_16x16x32_bf16 v[180:183], v[236:239], v[28:31], v[180:183]\n"
    "ds_read_b64_tr_b16 v[234:235], v248 offset:28672\n"
    "s_waitcnt lgkmcnt(4)\n"
    "v_mfma_f32_16x16x32_bf16 v[188:191], v[240:243], v[28:31], v[188:191]\n"
    "ds_read_b64_tr_b16 v[236:237], v249 offset:24576\n"
    "v_permlane32_swap_b32 v152, v153\n"
    "v_mfma_f32_16x16x32_bf16 v[184:187], v[240:243], v[24:27], v[184:187]\n"
    "ds_read_b64_tr_b16 v[238:239], v249 offset:28672\n"
    "s_waitcnt lgkmcnt(4)\n"
    "v_mfma_f32_16x16x32_bf16 v[192:195], v[244:247], v[24:27], v[192:195]\n"
    "ds_read_b64_tr_b16 v[240:241], v250 offset:24576\n"
    "v_add_f32 v153, v152, v153\n"
    "v_mfma_f32_16x16x32_bf16 v[196:199], v[244:247], v[28:31], v[196:199]\n"
    "ds_read_b64_tr_b16 v[242:243], v250 offset:28672\n"
    "s_waitcnt lgkmcnt(4)\n"
    "v_mfma_f32_16x16x32_bf16 v[204:207], v[232:235], v[28:31], v[204:207]\n"
    "ds_read_b64_tr_b16 v[244:245], v251 offset:24576\n"
    "v_mov_b32 v152, v153\n"
    "v_mfma_f32_16x16x32_bf16 v[200:203], v[232:235], v[24:27], v[200:203]\n"
    "ds_read_b64_tr_b16 v[246:247], v251 offset:28672\n"
    "s_waitcnt lgkmcnt(4)\n"
    "v_mfma_f32_16x16x32_bf16 v[208:211], v[236:239], v[24:27], v[208:211]\n"
    "ds_read_b64_tr_b16 v[232:233], v252 offset:32768\n"
    "v_permlane16_swap_b32 v152, v153\n"
    "v_mfma_f32_16x16x32_bf16 v[212:215], v[236:239], v[28:31], v[212:215]\n"
    "ds_read_b64_tr_b16 v[234:235], v252 offset:36864\n"
    "s_waitcnt lgkmcnt(4)\n"
    "v_mfma_f32_16x16x32_bf16 v[220:223], v[240:243], v[28:31], v[220:223]\n"
    "ds_read_b64_tr_b16 v[236:237], v253 offset:32768\n"
    "v_add_f32 v153, v152, v153\n"
    "v_mfma_f32_16x16x32_bf16 v[216:219], v[240:243], v[24:27], v[216:219]\n"
    "ds_read_b64_tr_b16 v[238:239], v253 offset:36864\n"
    "s_waitcnt lgkmcnt(4)\n"
    "v_mfma_f32_16x16x32_bf16 v[224:227], v[244:247], v[24:27], v[224:227]\n"
    "ds_read_b64_tr_b16 v[240:241], v254 offset:32768\n"
    "v_mul_f32 v153, 0x3ae38e39, v153\n"
    "v_mfma_f32_16x16x32_bf16 v[228:231], v[244:247], v[28:31], v[228:231]\n"
    "ds_read_b64_tr_b16 v[242:243], v254 offset:36864\n"
    "s_waitcnt lgkmcnt(4)\n"
    "v_mfma_f32_16x16x32_bf16 v[176:179], v[232:235], v[32:35], v[176:179]\n"
    "ds_read_b64_tr_b16 v[244:245], v248 offset:32768\n"
    "v_fma_f32 v153, -v164, v164, v153\n"
    "v_mfma_f32_16x16x32_bf16 v[180:183], v[232:235], v[36:39], v[180:183]\n"
    "ds_read_b64_tr_b16 v[246:247], v248 offset:36864\n"
    "s_waitcnt lgkmcnt(4)\n"
    "v_mfma_f32_16x16x32_bf16 v[188:191], v[236:239], v[36:39], v[188:191]\n"
    "ds_read_b64_tr_b16 v[232:233], v249 offset:32768\n"
    "v_max_f32 v153, 0, v153\n"
    "v_mfma_f32_16x16x32_bf16 v[184:187], v[236:239], v[32:35], v[184:187]\n"
    "ds_read_b64_tr_b16 v[234:235], v249 offset:36864\n"
    "s_waitcnt lgkmcnt(4)\n"
    "v_mfma_f32_16x16x32_bf16 v[192:195], v[240:243], v[32:35], v[192:195]\n"
    "s_barrier\n"
    "ds_read_b64_tr_b16 v[236:237], v250 offset:32768\n"
    "v_mfma_f32_16x16x32_bf16 v[196:199], v[240:243], v[36:39], v[196:199]\n"
    "ds_read_b64_tr_b16 v[238:239], v250 offset:36864\n"
    "s_waitcnt lgkmcnt(4)\n"
    "v_mfma_f32_16x16x32_bf16 v[204:207], v[244:247], v[36:39], v[204:207]\n"
    "ds_read_b64_tr_b16 v[240:241], v251 offset:32768\n"
    "v_add_f32 v153, 0x3727c5ac, v153\n"
    "v_mfma_f32_16x16x32_bf16 v[200:203], v[244:247], v[32:35], v[200:203]\n"
    "ds_read_b64_tr_b16 v[242:243], v251 offset:36864\n"
    "s_waitcnt lgkmcnt(4)\n"
    "v_mfma_f32_16x16x32_bf16 v[208:211], v[232:235], v[32:35], v[208:211]\n"
    "ds_read_b64_tr_b16 v[244:245], v252 offset:40960\n"
    "v_rsq_f32 v153, v153\n"
    "v_mfma_f32_16x16x32_bf16 v[212:215], v[232:235], v[36:39], v[212:215]\n"
    "ds_read_b64_tr_b16 v[246:247], v252 offset:45056\n"
    "s_waitcnt lgkmcnt(4)\n"
    "v_mfma_f32_16x16x32_bf16 v[220:223], v[236:239], v[36:39], v[220:223]\n"
    "ds_read_b64_tr_b16 v[232:233], v253 offset:40960\n"
    "v_mul_f32 v155, 0x3f154857, v153\n"
    "v_mfma_f32_16x16x32_bf16 v[216:219], v[236:239], v[32:35], v[216:219]\n"
    "ds_read_b64_tr_b16 v[234:235], v253 offset:45056\n"
    "s_waitcnt lgkmcnt(4)\n"
    "v_mfma_f32_16x16x32_bf16 v[224:227], v[240:243], v[32:35], v[224:227]\n"
    "ds_read_b64_tr_b16 v[236:237], v254 offset:40960\n"
    "v_mfma_f32_16x16x32_bf16 v[228:231], v[240:243], v[36:39], v[228:231]\n"
    "ds_read_b64_tr_b16 v[238:239], v254 offset:45056\n"
    "s_waitcnt lgkmcnt(4)\n"
    "v_mfma_f32_16x16x32_bf16 v[176:179], v[244:247], v[40:43], v[176:179]\n"
    "ds_read_b64_tr_b16 v[240:241], v248 offset:40960\n"
    "v_mfma_f32_16x16x32_bf16 v[180:183], v[244:247], v[44:47], v[180:183]\n"
    "ds_read_b64_tr_b16 v[242:243], v248 offset:45056\n"
    "s_waitcnt lgkmcnt(4)\n"
    "v_mfma_f32_16x16x32_bf16 v[188:191], v[232:235], v[44:47], v[188:191]\n"
    "ds_read_b64_tr_b16 v[244:245], v249 offset:40960\n"
    "v_mfma_f32_16x16x32_bf16 v[184:187], v[232:235], v[40:43], v[184:187]\n"
    "ds_read_b64_tr_b16 v[246:247], v249 offset:45056\n"
    "s_waitcnt lgkmcnt(4)\n"
    "v_mfma_f32_16x16x32_bf16 v[192:195], v[236:239], v[40:43], v[192:195]\n"
    "ds_read_b64_tr_b16 v[232:233], v250 offset:40960\n"
    "v_mfma_f32_16x16x32_bf16 v[196:199], v[236:239], v[44:47], v[196:199]\n"
    "ds_read_b64_tr_b16 v[234:235], v250 offset:45056\n"
    "s_waitcnt lgkmcnt(4)\n"
    "v_mfma_f32_16x16x32_bf16 v[204:207], v[240:243], v[44:47], v[204:207]\n"
    "ds_read_b64_tr_b16 v[236:237], v251 offset:40960\n"
    "v_mfma_f32_16x16x32_bf16 v[200:203], v[240:243], v[40:43], v[200:203]\n"
    "ds_read_b64_tr_b16 v[238:239], v251 offset:45056\n"
    "s_waitcnt lgkmcnt(4)\n"
    "v_mfma_f32_16x16x32_bf16 v[208:211], v[244:247], v[40:43], v[208:211]\n"
    "ds_read_b64_tr_b16 v[240:241], v252 offset:49152\n"
    "v_mfma_f32_16x16x32_bf16 v[212:215], v[244:247], v[44:47], v[212:215]\n"
    "ds_read_b64_tr_b16 v[242:243], v252 offset:53248\n"
    "s_waitcnt lgkmcnt(4)\n"
    "v_mfma_f32_16x16x32_bf16 v[220:223], v[232:235], v[44:47], v[220:223]\n"
    "ds_read_b64_tr_b16 v[244:245], v253 offset:49152\n"
    "v_lshrrev_b32 v156, 2, v255\n"
    "v_mfma_f32_16x16x32_bf16 v[216:219], v[232:235], v[40:43], v[216:219]\n"
    "ds_read_b64_tr_b16 v[246:247], v253 offset:53248\n"
    "s_waitcnt lgkmcnt(4)\n"
    "v_mfma_f32_16x16x32_bf16 v[224:227], v[236:239], v[40:43], v[224:227]\n"
    "ds_read_b64_tr_b16 v[232:233], v254 offset:49152\n"
    "v_lshlrev_b32 v156, 7, v156\n"
    "v_mfma_f32_16x16x32_bf16 v[228:231], v[236:239], v[44:47], v[228:231]\n"
    "ds_read_b64_tr_b16 v[234:235], v254 offset:53248\n"
    "s_waitcnt lgkmcnt(4)\n"
    "v_mfma_f32_16x16x32_bf16 v[176:179], v[240:243], v[48:51], v[176:179]\n"
    "s_barrier\n"
    "ds_read_b64_tr_b16 v[236:237], v248 offset:49152\n"
    "v_mfma_f32_16x16x32_bf16 v[180:183], v[240:243], v[52:55], v[180:183]\n"
    "ds_read_b64_tr_b16 v[238:239], v248 offset:53248\n"
    "s_waitcnt lgkmcnt(4)\n"
    "v_mfma_f32_16x16x32_bf16 v[188:191], v[244:247], v[52:55], v[188:191]\n"
    "ds_read_b64_tr_b16 v[240:241], v249 offset:49152\n"
    "v_bfe_u32 v159, v255, 3, 2\n"
    "v_mfma_f32_16x16x32_bf16 v[184:187], v[244:247], v[48:51], v[184:187]\n"
    "ds_read_b64_tr_b16 v[242:243], v249 offset:53248\n"
    "s_waitcnt lgkmcnt(4)\n"
    "v_mfma_f32_16x16x32_bf16 v[192:195], v[232:235], v[48:51], v[192:195]\n"
    "ds_read_b64_tr_b16 v[244:245], v250 offset:49152\n"
    "v_lshl_add_u32 v156, v159, 5, v156\n"
    "v_mfma_f32_16x16x32_bf16 v[196:199], v[232:235], v[52:55], v[196:199]\n"
    "ds_read_b64_tr_b16 v[246:247], v250 offset:53248\n"
    "s_waitcnt lgkmcnt(4)\n"
    "v_mfma_f32_16x16x32_bf16 v[204:207], v[236:239], v[52:55], v[204:207]\n"
    "ds_read_b64_tr_b16 v[232:233], v251 offset:49152\n"
    "v_and_b32 v159, 3, v255\n"
    "v_mfma_f32_16x16x32_bf16 v[200:203], v[236:239], v[48:51], v[200:203]\n"
    "ds_read_b64_tr_b16 v[234:235], v251 offset:53248\n"
    "s_waitcnt lgkmcnt(4)\n"
    "v_mfma_f32_16x16x32_bf16 v[208:211], v[240:243], v[48:51], v[208:211]\n"
    "ds_read_b64_tr_b16 v[236:237], v252 offset:57344\n"
    "v_lshl_add_u32 v156, v159, 3, v156\n"
    "v_mfma_f32_16x16x32_bf16 v[212:215], v[240:243], v[52:55], v[212:215]\n"
    "ds_read_b64_tr_b16 v[238:239], v252 offset:61440\n"
    "s_waitcnt lgkmcnt(4)\n"
    "v_mfma_f32_16x16x32_bf16 v[220:223], v[244:247], v[52:55], v[220:223]\n"
    "ds_read_b64_tr_b16 v[240:241], v253 offset:57344\n"
    "v_lshrrev_b32 v159, 1, v255\n"
    "v_mfma_f32_16x16x32_bf16 v[216:219], v[244:247], v[48:51], v[216:219]\n"
    "ds_read_b64_tr_b16 v[242:243], v253 offset:61440\n"
    "s_waitcnt lgkmcnt(4)\n"
    "v_mfma_f32_16x16x32_bf16 v[224:227], v[232:235], v[48:51], v[224:227]\n"
    "ds_read_b64_tr_b16 v[244:245], v254 offset:57344\n"
    "v_and_b32 v159, 8, v159\n"
    "v_mfma_f32_16x16x32_bf16 v[228:231], v[232:235], v[52:55], v[228:231]\n"
    "ds_read_b64_tr_b16 v[246:247], v254 offset:61440\n"
    "s_waitcnt lgkmcnt(4)\n"
    "v_mfma_f32_16x16x32_bf16 v[176:179], v[236:239], v[56:59], v[176:179]\n"
    "ds_read_b64_tr_b16 v[232:233], v248 offset:57344\n"
    "v_xor_b32 v157, v159, v255\n"
    "v_mfma_f32_16x16x32_bf16 v[180:183], v[236:239], v[60:63], v[180:183]\n"
    "ds_read_b64_tr_b16 v[234:235], v248 offset:61440\n"
    "s_waitcnt lgkmcnt(4)\n"
    "v_mfma_f32_16x16x32_bf16 v[188:191], v[240:243], v[60:63], v[188:191]\n"
    "ds_read_b64_tr_b16 v[236:237], v249 offset:57344\n"
    "v_add_u32 v252, 0x10000, v252\n"
    "v_mfma_f32_16x16x32_bf16 v[184:187], v[240:243], v[56:59], v[184:187]\n"
    "ds_read_b64_tr_b16 v[238:239], v249 offset:61440\n"
    "s_waitcnt lgkmcnt(4)\n"
    "v_mfma_f32_16x16x32_bf16 v[192:195], v[244:247], v[56:59], v[192:195]\n"
    "ds_read_b64_tr_b16 v[240:241], v250 offset:57344\n"
    "v_add_u32 v253, 0x10000, v253\n"
    "v_mfma_f32_16x16x32_bf16 v[196:199], v[244:247], v[60:63], v[196:199]\n"
    "ds_read_b64_tr_b16 v[242:243], v250 offset:61440\n"
    "s_waitcnt lgkmcnt(4)\n"
    "v_mfma_f32_16x16x32_bf16 v[204:207], v[232:235], v[60:63], v[204:207]\n"
    "ds_read_b64_tr_b16 v[244:245], v251 offset:57344\n"
    "v_add_u32 v254, 0x10000, v254\n"
    "v_mfma_f32_16x16x32_bf16 v[200:203], v[232:235], v[56:59], v[200:203]\n"
    "ds_read_b64_tr_b16 v[246:247], v251 offset:61440\n"
    "s_waitcnt lgkmcnt(4)\n"
    "v_mfma_f32_16x16x32_bf16 v[208:211], v[236:239], v[56:59], v[208:211]\n"
    "ds_read_b64_tr_b16 v[232:233], v252 offset:0\n"
    "v_add_u32 v248, 0x10000, v248\n"
    "v_mfma_f32_16x16x32_bf16 v[212:215], v[236:239], v[60:63], v[212:215]\n"
    "ds_read_b64_tr_b16 v[234:235], v252 offset:4096\n"
    "s_waitcnt lgkmcnt(4)\n"
    "v_mfma_f32_16x16x32_bf16 v[220:223], v[240:243], v[60:63], v[220:223]\n"
    "s_barrier\n"
    "ds_read_b64_tr_b16 v[236:237], v253 offset:0\n"
    "v_mfma_f32_16x16x32_bf16 v[216:219], v[240:243], v[56:59], v[216:219]\n"
    "ds_read_b64_tr_b16 v[238:239], v253 offset:4096\n"
    "s_waitcnt lgkmcnt(4)\n"
    "v_mfma_f32_16x16x32_bf16 v[224:227], v[244:247], v[56:59], v[224:227]\n"
    "ds_read_b64_tr_b16 v[240:241], v254 offset:0\n"
    "v_add_u32 v249, 0x10000, v249\n"
    "v_mfma_f32_16x16x32_bf16 v[228:231], v[244:247], v[60:63], v[228:231]\n"
    "ds_read_b64_tr_b16 v[242:243], v254 offset:4096\n"
    "s_waitcnt lgkmcnt(4)\n"
    "v_mfma_f32_16x16x32_bf16 v[176:179], v[232:235], v[64:67], v[176:179]\n"
    "ds_read_b64_tr_b16 v[244:245], v248 offset:0\n"
    "v_add_u32 v250, 0x10000, v250\n"
    "v_mfma_f32_16x16x32_bf16 v[180:183], v[232:235], v[68:71], v[180:183]\n"
    "ds_read_b64_tr_b16 v[246:247], v248 offset:4096\n"
    "s_waitcnt lgkmcnt(4)\n"
    "v_mfma_f32_16x16x32_bf16 v[188:191], v[236:239], v[68:71], v[188:191]\n"
    "ds_read_b64_tr_b16 v[232:233], v249 offset:0\n"
    "v_add_u32 v251, 0x10000, v251\n"
    "v_mfma_f32_16x16x32_bf16 v[184:187], v[236:239], v[64:67], v[184:187]\n"
    "ds_read_b64_tr_b16 v[234:235], v249 offset:4096\n"
    "s_waitcnt lgkmcnt(4)\n"
    "v_mfma_f32_16x16x32_bf16 v[192:195], v[240:243], v[64:67], v[192:195]\n"
    "ds_read_b64_tr_b16 v[236:237], v250 offset:0\n"
    "v_lshlrev_b32 v157, 4, v157\n"
    "v_mfma_f32_16x16x32_bf16 v[196:199], v[240:243], v[68:71], v[196:199]\n"
    "ds_read_b64_tr_b16 v[238:239], v250 offset:4096\n"
    "s_waitcnt lgkmcnt(4)\n"
    "v_mfma_f32_16x16x32_bf16 v[204:207], v[244:247], v[68:71], v[204:207]\n"
    "ds_read_b64_tr_b16 v[240:241], v251 offset:0\n"
    "v_mov_b32 v158, s44\n"
    "v_mfma_f32_16x16x32_bf16 v[200:203], v[244:247], v[64:67], v[200:203]\n"
    "ds_read_b64_tr_b16 v[242:243], v251 offset:4096\n"
    "s_waitcnt lgkmcnt(4)\n"
    "v_mfma_f32_16x16x32_bf16 v[208:211], v[232:235], v[64:67], v[208:211]\n"
    "ds_read_b64_tr_b16 v[244:245], v252 offset:8192\n"
    "v_mov_b32 v159, s45\n"
    "v_mfma_f32_16x16x32_bf16 v[212:215], v[232:235], v[68:71], v[212:215]\n"
    "ds_read_b64_tr_b16 v[246:247], v252 offset:12288\n"
    "s_waitcnt lgkmcnt(4)\n"
    "v_mfma_f32_16x16x32_bf16 v[220:223], v[236:239], v[68:71], v[220:223]\n"
    "ds_read_b64_tr_b16 v[232:233], v253 offset:8192\n"
    "s_mov_b32 vcc_lo, 0xffff0000\n"
    "v_mfma_f32_16x16x32_bf16 v[216:219], v[236:239], v[64:67], v[216:219]\n"
    "ds_read_b64_tr_b16 v[234:235], v253 offset:12288\n"
    "s_waitcnt lgkmcnt(4)\n"
    "v_mfma_f32_16x16x32_bf16 v[224:227], v[240:243], v[64:67], v[224:227]\n"
    "ds_read_b64_tr_b16 v[236:237], v254 offset:8192\n"
    "s_mov_b32 vcc_hi, 0\n"
    "v_mfma_f32_16x16x32_bf16 v[228:231], v[240:243], v[68:71], v[228:231]\n"
    "ds_read_b64_tr_b16 v[238:239], v254 offset:12288\n"
    "s_waitcnt lgkmcnt(4)\n"
    "v_mfma_f32_16x16x32_bf16 v[176:179], v[244:247], v[72:75], v[176:179]\n"
    "ds_read_b64_tr_b16 v[240:241], v248 offset:8192\n"
    "v_cndmask_b32 v158, v158, v159, vcc\n"
    "v_mfma_f32_16x16x32_bf16 v[180:183], v[244:247], v[76:79], v[180:183]\n"
    "ds_read_b64_tr_b16 v[242:243], v248 offset:12288\n"
    "s_waitcnt lgkmcnt(4)\n"
    "v_mfma_f32_16x16x32_bf16 v[188:191], v[232:235], v[76:79], v[188:191]\n"
    "ds_read_b64_tr_b16 v[244:245], v249 offset:8192\n"
    "v_mov_b32 v159, s46\n"
    "v_mfma_f32_16x16x32_bf16 v[184:187], v[232:235], v[72:75], v[184:187]\n"
    "ds_read_b64_tr_b16 v[246:247], v249 offset:12288\n"
    "s_waitcnt lgkmcnt(4)\n"
    "v_mfma_f32_16x16x32_bf16 v[192:195], v[236:239], v[72:75], v[192:195]\n"
    "ds_read_b64_tr_b16 v[232:233], v250 offset:8192\n"
    "s_mov_b32 vcc_lo, 0\n"
    "v_mfma_f32_16x16x32_bf16 v[196:199], v[236:239], v[76:79], v[196:199]\n"
    "ds_read_b64_tr_b16 v[234:235], v250 offset:12288\n"
    "s_waitcnt lgkmcnt(4)\n"
    "v_mfma_f32_16x16x32_bf16 v[204:207], v[240:243], v[76:79], v[204:207]\n"
    "s_barrier\n"
    "ds_read_b64_tr_b16 v[236:237], v251 offset:8192\n"
    "v_mfma_f32_16x16x32_bf16 v[200:203], v[240:243], v[72:75], v[200:203]\n"
    "ds_read_b64_tr_b16 v[238:239], v251 offset:12288\n"
    "s_waitcnt lgkmcnt(4)\n"
    "v_mfma_f32_16x16x32_bf16 v[208:211], v[244:247], v[72:75], v[208:211]\n"
    "ds_read_b64_tr_b16 v[240:241], v252 offset:16384\n"
    "s_mov_b32 vcc_hi, 0xffff\n"
    "v_mfma_f32_16x16x32_bf16 v[212:215], v[244:247], v[76:79], v[212:215]\n"
    "ds_read_b64_tr_b16 v[242:243], v252 offset:20480\n"
    "s_waitcnt lgkmcnt(4)\n"
    "v_mfma_f32_16x16x32_bf16 v[220:223], v[232:235], v[76:79], v[220:223]\n"
    "ds_read_b64_tr_b16 v[244:245], v253 offset:16384\n"
    "v_cndmask_b32 v158, v158, v159, vcc\n"
    "v_mfma_f32_16x16x32_bf16 v[216:219], v[232:235], v[72:75], v[216:219]\n"
    "ds_read_b64_tr_b16 v[246:247], v253 offset:20480\n"
    "s_waitcnt lgkmcnt(4)\n"
    "v_mfma_f32_16x16x32_bf16 v[224:227], v[236:239], v[72:75], v[224:227]\n"
    "ds_read_b64_tr_b16 v[232:233], v254 offset:16384\n"
    "v_mov_b32 v159, s47\n"
    "v_mfma_f32_16x16x32_bf16 v[228:231], v[236:239], v[76:79], v[228:231]\n"
    "ds_read_b64_tr_b16 v[234:235], v254 offset:20480\n"
    "s_waitcnt lgkmcnt(4)\n"
    "v_mfma_f32_16x16x32_bf16 v[176:179], v[240:243], v[80:83], v[176:179]\n"
    "ds_read_b64_tr_b16 v[236:237], v248 offset:16384\n"
    "s_mov_b32 vcc_hi, 0xffff0000\n"
    "v_mfma_f32_16x16x32_bf16 v[180:183], v[240:243], v[84:87], v[180:183]\n"
    "ds_read_b64_tr_b16 v[238:239], v248 offset:20480\n"
    "s_waitcnt lgkmcnt(4)\n"
    "v_mfma_f32_16x16x32_bf16 v[188:191], v[244:247], v[84:87], v[188:191]\n"
    "ds_read_b64_tr_b16 v[240:241], v249 offset:16384\n"
    "v_cndmask_b32 v158, v158, v159, vcc\n"
    "v_mfma_f32_16x16x32_bf16 v[184:187], v[244:247], v[80:83], v[184:187]\n"
    "ds_read_b64_tr_b16 v[242:243], v249 offset:20480\n"
    "s_waitcnt lgkmcnt(4)\n"
    "v_mfma_f32_16x16x32_bf16 v[192:195], v[232:235], v[80:83], v[192:195]\n"
    "ds_read_b64_tr_b16 v[244:245], v250 offset:16384\n"
    "v_and_b32 v159, 15, v255\n"
    "v_mfma_f32_16x16x32_bf16 v[196:199], v[232:235], v[84:87], v[196:199]\n"
    "ds_read_b64_tr_b16 v[246:247], v250 offset:20480\n"
    "s_waitcnt lgkmcnt(4)\n"
    "v_mfma_f32_16x16x32_bf16 v[204:207], v[236:239], v[84:87], v[204:207]\n"
    "ds_read_b64_tr_b16 v[232:233], v251 offset:16384\n"
    "v_lshl_add_u32 v158, v159, 4, v158\n"
    "v_mfma_f32_16x16x32_bf16 v[200:203], v[236:239], v[80:83], v[200:203]\n"
    "ds_read_b64_tr_b16 v[234:235], v251 offset:20480\n"
    "s_waitcnt lgkmcnt(4)\n"
    "v_mfma_f32_16x16x32_bf16 v[208:211], v[240:243], v[80:83], v[208:211]\n"
    "ds_read_b64_tr_b16 v[236:237], v252 offset:24576\n"
    "v_mfma_f32_16x16x32_bf16 v[212:215], v[240:243], v[84:87], v[212:215]\n"
    "ds_read_b64_tr_b16 v[238:239], v252 offset:28672\n"
    "s_waitcnt lgkmcnt(4)\n"
    "v_mfma_f32_16x16x32_bf16 v[220:223], v[244:247], v[84:87], v[220:223]\n"
    "ds_read_b64_tr_b16 v[240:241], v253 offset:24576\n"
    "v_mfma_f32_16x16x32_bf16 v[216:219], v[244:247], v[80:83], v[216:219]\n"
    "ds_read_b64_tr_b16 v[242:243], v253 offset:28672\n"
    "s_waitcnt lgkmcnt(4)\n"
    "v_mfma_f32_16x16x32_bf16 v[224:227], v[232:235], v[80:83], v[224:227]\n"
    "ds_read_b64_tr_b16 v[244:245], v254 offset:24576\n"
    "v_mfma_f32_16x16x32_bf16 v[228:231], v[232:235], v[84:87], v[228:231]\n"
    "ds_read_b64_tr_b16 v[246:247], v254 offset:28672\n"
    "s_waitcnt lgkmcnt(4)\n"
    "v_mfma_f32_16x16x32_bf16 v[176:179], v[236:239], v[88:91], v[176:179]\n"
    "ds_read_b64_tr_b16 v[232:233], v248 offset:24576\n"
    "v_mfma_f32_16x16x32_bf16 v[180:183], v[236:239], v[92:95], v[180:183]\n"
    "ds_read_b64_tr_b16 v[234:235], v248 offset:28672\n"
    "s_waitcnt lgkmcnt(4)\n"
    "v_mfma_f32_16x16x32_bf16 v[188:191], v[240:243], v[92:95], v[188:191]\n"
    "s_barrier\n"
    "ds_read_b64_tr_b16 v[236:237], v249 offset:24576\n"
    "v_mfma_f32_16x16x32_bf16 v[184:187], v[240:243], v[88:91], v[184:187]\n"
    "ds_read_b64_tr_b16 v[238:239], v249 offset:28672\n"
    "s_waitcnt lgkmcnt(4)\n"
    "v_mfma_f32_16x16x32_bf16 v[192:195], v[244:247], v[88:91], v[192:195]\n"
    "ds_read_b64_tr_b16 v[240:241], v250 offset:24576\n"
    "v_mfma_f32_16x16x32_bf16 v[196:199], v[244:247], v[92:95], v[196:199]\n"
    "ds_read_b64_tr_b16 v[242:243], v250 offset:28672\n"
    "s_waitcnt lgkmcnt(4)\n"
    "v_mfma_f32_16x16x32_bf16 v[204:207], v[232:235], v[92:95], v[204:207]\n"
    "ds_read_b64_tr_b16 v[244:245], v251 offset:24576\n"
    "v_mfma_f32_16x16x32_bf16 v[200:203], v[232:235], v[88:91], v[200:203]\n"
    "ds_read_b64_tr_b16 v[246:247], v251 offset:28672\n"
    "s_waitcnt lgkmcnt(4)\n"
    "v_mfma_f32_16x16x32_bf16 v[208:211], v[236:239], v[88:91], v[208:211]\n"
    "ds_read_b64_tr_b16 v[232:233], v252 offset:32768\n"
    "v_mfma_f32_16x16x32_bf16 v[212:215], v[236:239], v[92:95], v[212:215]\n"
    "ds_read_b64_tr_b16 v[234:235], v252 offset:36864\n"
    "s_waitcnt lgkmcnt(4)\n"
    "v_mfma_f32_16x16x32_bf16 v[220:223], v[240:243], v[92:95], v[220:223]\n"
    "ds_read_b64_tr_b16 v[236:237], v253 offset:32768\n"
    "v_mfma_f32_16x16x32_bf16 v[216:219], v[240:243], v[88:91], v[216:219]\n"
    "ds_read_b64_tr_b16 v[238:239], v253 offset:36864\n"
    "s_waitcnt lgkmcnt(4)\n"
    "v_mfma_f32_16x16x32_bf16 v[224:227], v[244:247], v[88:91], v[224:227]\n"
    "ds_read_b64_tr_b16 v[240:241], v254 offset:32768\n"
    "v_mfma_f32_16x16x32_bf16 v[228:231], v[244:247], v[92:95], v[228:231]\n"
    "ds_read_b64_tr_b16 v[242:243], v254 offset:36864\n"
    "s_waitcnt lgkmcnt(4)\n"
    "v_mfma_f32_16x16x32_bf16 v[176:179], v[232:235], v[96:99], v[176:179]\n"
    "ds_read_b64_tr_b16 v[244:245], v248 offset:32768\n"
    "v_mfma_f32_16x16x32_bf16 v[180:183], v[232:235], v[100:103], v[180:183]\n"
    "ds_read_b64_tr_b16 v[246:247], v248 offset:36864\n"
    "s_waitcnt lgkmcnt(4)\n"
    "v_mfma_f32_16x16x32_bf16 v[188:191], v[236:239], v[100:103], v[188:191]\n"
    "ds_read_b64_tr_b16 v[232:233], v249 offset:32768\n"
    "v_mfma_f32_16x16x32_bf16 v[184:187], v[236:239], v[96:99], v[184:187]\n"
    "ds_read_b64_tr_b16 v[234:235], v249 offset:36864\n"
    "s_waitcnt lgkmcnt(4)\n"
    "v_mfma_f32_16x16x32_bf16 v[192:195], v[240:243], v[96:99], v[192:195]\n"
    "ds_read_b64_tr_b16 v[236:237], v250 offset:32768\n"
    "v_mfma_f32_16x16x32_bf16 v[196:199], v[240:243], v[100:103], v[196:199]\n"
    "ds_read_b64_tr_b16 v[238:239], v250 offset:36864\n"
    "s_waitcnt lgkmcnt(4)\n"
    "v_mfma_f32_16x16x32_bf16 v[204:207], v[244:247], v[100:103], v[204:207]\n"
    "ds_read_b64_tr_b16 v[240:241], v251 offset:32768\n"
    "v_mfma_f32_16x16x32_bf16 v[200:203], v[244:247], v[96:99], v[200:203]\n"
    "ds_read_b64_tr_b16 v[242:243], v251 offset:36864\n"
    "s_waitcnt lgkmcnt(4)\n"
    "v_mfma_f32_16x16x32_bf16 v[208:211], v[232:235], v[96:99], v[208:211]\n"
    "ds_read_b64_tr_b16 v[244:245], v252 offset:40960\n"
    "v_mfma_f32_16x16x32_bf16 v[212:215], v[232:235], v[100:103], v[212:215]\n"
    "L_st2_%=:\n"
    "s_cbranch_execz L_bk%=\n"
    "ds_read_b64_tr_b16 v[246:247], v252 offset:45056\n"
    "s_waitcnt lgkmcnt(4)\n"
    "v_mfma_f32_16x16x32_bf16 v[220:223], v[236:239], v[100:103], v[220:223]\n"
    "ds_read_b64_tr_b16 v[232:233], v253 offset:40960\n"
    "v_mfma_f32_16x16x32_bf16 v[216:219], v[236:239], v[96:99], v[216:219]\n"
    "ds_read_b64_tr_b16 v[234:235], v253 offset:45056\n"
    "s_waitcnt lgkmcnt(4)\n"
    "v_mfma_f32_16x16x32_bf16 v[224:227], v[240:243], v[96:99], v[224:227]\n"
    "s_barrier\n"
    "ds_read_b64_tr_b16 v[236:237], v254 offset:40960\n"
    "v_mfma_f32_16x16x32_bf16 v[228:231], v[240:243], v[100:103], v[228:231]\n"
    "ds_read_b64_tr_b16 v[238:239], v254 offset:45056\n"
    "s_waitcnt lgkmcnt(4)\n"
    "v_mfma_f32_16x16x32_bf16 v[176:179], v[244:247], v[104:107], v[176:179]\n"
    "ds_read_b64_tr_b16 v[240:241], v248 offset:40960\n"
    "v_mfma_f32_16x16x32_bf16 v[180:183], v[244:247], v[108:111], v[180:183]\n"
    "ds_read_b64_tr_b16 v[242:243], v248 offset:45056\n"
    "s_waitcnt lgkmcnt(4)\n"
    "v_mfma_f32_16x16x32_bf16 v[188:191], v[232:235], v[108:111], v[188:191]\n"
    "ds_read_b64_tr_b16 v[244:245], v249 offset:40960\n"
    "v_mfma_f32_16x16x32_bf16 v[184:187], v[232:235], v[104:107], v[184:187]\n"
    "ds_read_b64_tr_b16 v[246:247], v249 offset:45056\n"
    "s_waitcnt lgkmcnt(4)\n"
    "v_mfma_f32_16x16x32_bf16 v[192:195], v[236:239], v[104:107], v[192:195]\n"
    "ds_read_b64_tr_b16 v[232:233], v250 offset:40960\n"
    "v_mfma_f32_16x16x32_bf16 v[196:199], v[236:239], v[108:111], v[196:199]\n"
    "ds_read_b64_tr_b16 v[234:235], v250 offset:45056\n"
    "s_waitcnt lgkmcnt(4)\n"
    "v_mfma_f32_16x16x32_bf16 v[204:207], v[240:243], v[108:111], v[204:207]\n"
    "ds_read_b64_tr_b16 v[236:237], v251 offset:40960\n"
    "v_mfma_f32_16x16x32_bf16 v[200:203], v[240:243], v[104:107], v[200:203]\n"
    "ds_read_b64_tr_b16 v[238:239], v251 offset:45056\n"
    "s_waitcnt lgkmcnt(4)\n"
    "v_mfma_f32_16x16x32_bf16 v[208:211], v[244:247], v[104:107], v[208:211]\n"
    "ds_read_b64_tr_b16 v[240:241], v252 offset:49152\n"
    "v_mfma_f32_16x16x32_bf16 v[212:215], v[244:247], v[108:111], v[212:215]\n"
    "ds_read_b64_tr_b16 v[242:243], v252 offset:53248\n"
    "s_waitcnt lgkmcnt(4)\n"
    "v_mfma_f32_16x16x32_bf16 v[220:223], v[232:235], v[108:111], v[220:223]\n"
    "ds_read_b64_tr_b16 v[244:245], v253 offset:49152\n"
    "v_mfma_f32_16x16x32_bf16 v[216:219], v[232:235], v[104:107], v[216:219]\n"
    "ds_read_b64_tr_b16 v[246:247], v253 offset:53248\n"
    "s_waitcnt lgkmcnt(4)\n"
    "v_mfma_f32_16x16x32_bf16 v[224:227], v[236:239], v[104:107], v[224:227]\n"
    "ds_read_b64_tr_b16 v[232:233], v254 offset:49152\n"
    "v_mfma_f32_16x16x32_bf16 v[228:231], v[236:239], v[108:111], v[228:231]\n"
    "ds_read_b64_tr_b16 v[234:235], v254 offset:53248\n"
    "s_waitcnt lgkmcnt(4)\n"
    "v_mfma_f32_16x16x32_bf16 v[176:179], v[240:243], v[112:115], v[176:179]\n"
    "ds_read_b64_tr_b16 v[236:237], v248 offset:49152\n"
    "v_mfma_f32_16x16x32_bf16 v[180:183], v[240:243], v[116:119], v[180:183]\n"
    "ds_read_b64_tr_b16 v[238:239], v248 offset:53248\n"
    "s_waitcnt lgkmcnt(4)\n"
    "v_mfma_f32_16x16x32_bf16 v[188:191], v[244:247], v[116:119], v[188:191]\n"
    "ds_read_b64_tr_b16 v[240:241], v249 offset:49152\n"
    "v_mfma_f32_16x16x32_bf16 v[184:187], v[244:247], v[112:115], v[184:187]\n"
    "ds_read_b64_tr_b16 v[242:243], v249 offset:53248\n"
    "s_waitcnt lgkmcnt(4)\n"
    "v_mfma_f32_16x16x32_bf16 v[192:195], v[232:235], v[112:115], v[192:195]\n"
    "ds_read_b64_tr_b16 v[244:245], v250 offset:49152\n"
    "v_mfma_f32_16x16x32_bf16 v[196:199], v[232:235], v[116:119], v[196:199]\n"
    "ds_read_b64_tr_b16 v[246:247], v250 offset:53248\n"
    "s_waitcnt lgkmcnt(4)\n"
    "v_mfma_f32_16x16x32_bf16 v[204:207], v[236:239], v[116:119], v[204:207]\n"
    "ds_read_b64_tr_b16 v[232:233], v251 offset:49152\n"
    "v_mfma_f32_16x16x32_bf16 v[200:203], v[236:239], v[112:115], v[200:203]\n"
    "ds_read_b64_tr_b16 v[234:235], v251 offset:53248\n"
    "s_waitcnt lgkmcnt(4)\n"
    "v_mfma_f32_16x16x32_bf16 v[208:211], v[240:243], v[112:115], v[208:211]\n"
    "s_barrier\n"
    "ds_read_b64_tr_b16 v[236:237], v252 offset:57344\n"
    "v_mfma_f32_16x16x32_bf16 v[212:215], v[240:243], v[116:119], v[212:215]\n"
    "ds_read_b64_tr_b16 v[238:239], v252 offset:61440\n"
    "s_waitcnt lgkmcnt(4)\n"
    "v_mfma_f32_16x16x32_bf16 v[220:223], v[244:247], v[116:119], v[220:223]\n"
    "ds_read_b64_tr_b16 v[240:241], v253 offset:57344\n"
    "v_mfma_f32_16x16x32_bf16 v[216:219], v[244:247], v[112:115], v[216:219]\n"
    "ds_read_b64_tr_b16 v[242:243], v253 offset:61440\n"
    "s_waitcnt lgkmcnt(4)\n"
    "v_mfma_f32_16x16x32_bf16 v[224:227], v[232:235], v[112:115], v[224:227]\n"
    "ds_read_b64_tr_b16 v[244:245], v254 offset:57344\n"
    "v_mfma_f32_16x16x32_bf16 v[228:231], v[232:235], v[116:119], v[228:231]\n"
    "ds_read_b64_tr_b16 v[246:247], v254 offset:61440\n"
    "s_waitcnt lgkmcnt(4)\n"
    "v_mfma_f32_16x16x32_bf16 v[176:179], v[236:239], v[120:123], v[176:179]\n"
    "ds_read_b64_tr_b16 v[232:233], v248 offset:57344\n"
    "v_mfma_f32_16x16x32_bf16 v[180:183], v[236:239], v[124:127], v[180:183]\n"
    "ds_read_b64_tr_b16 v[234:235], v248 offset:61440\n"
    "s_waitcnt lgkmcnt(4)\n"
    "v_mfma_f32_16x16x32_bf16 v[188:191], v[240:243], v[124:127], v[188:191]\n"
    "ds_read_b64_tr_b16 v[236:237], v249 offset:57344\n"
    "v_add_u32 v252, 0x8000, v252\n"
    "v_mfma_f32_16x16x32_bf16 v[184:187], v[240:243], v[120:123], v[184:187]\n"
    "ds_read_b64_tr_b16 v[238:239], v249 offset:61440\n"
    "s_waitcnt lgkmcnt(4)\n"
    "v_mfma_f32_16x16x32_bf16 v[192:195], v[244:247], v[120:123], v[192:195]\n"
    "ds_read_b64_tr_b16 v[240:241], v250 offset:57344\n"
    "v_add_u32 v253, 0x8000, v253\n"
    "v_mfma_f32_16x16x32_bf16 v[196:199], v[244:247], v[124:127], v[196:199]\n"
    "ds_read_b64_tr_b16 v[242:243], v250 offset:61440\n"
    "s_waitcnt lgkmcnt(4)\n"
    "v_mfma_f32_16x16x32_bf16 v[204:207], v[232:235], v[124:127], v[204:207]\n"
    "ds_read_b64_tr_b16 v[244:245], v251 offset:57344\n"
    "v_add_u32 v254, 0x8000, v254\n"
    "v_mfma_f32_16x16x32_bf16 v[200:203], v[232:235], v[120:123], v[200:203]\n"
    "ds_read_b64_tr_b16 v[246:247], v251 offset:61440\n"
    "s_waitcnt lgkmcnt(4)\n"
    "v_mfma_f32_16x16x32_bf16 v[208:211], v[236:239], v[120:123], v[208:211]\n"
    "ds_read_b64_tr_b16 v[232:233], v252 offset:32768\n"
    "v_add_u32 v248, 0x8000, v248\n"
    "v_mfma_f32_16x16x32_bf16 v[212:215], v[236:239], v[124:127], v[212:215]\n"
    "ds_read_b64_tr_b16 v[234:235], v252 offset:36864\n"
    "s_waitcnt lgkmcnt(4)\n"
    "v_mfma_f32_16x16x32_bf16 v[220:223], v[240:243], v[124:127], v[220:223]\n"
    "ds_read_b64_tr_b16 v[236:237], v253 offset:32768\n"
    "v_add_u32 v249, 0x8000, v249\n"
    "v_mfma_f32_16x16x32_bf16 v[216:219], v[240:243], v[120:123], v[216:219]\n"
    "ds_read_b64_tr_b16 v[238:239], v253 offset:36864\n"
    "s_waitcnt lgkmcnt(4)\n"
    "v_mfma_f32_16x16x32_bf16 v[224:227], v[244:247], v[120:123], v[224:227]\n"
    "ds_read_b64_tr_b16 v[240:241], v254 offset:32768\n"
    "v_add_u32 v250, 0x8000, v250\n"
    "v_mfma_f32_16x16x32_bf16 v[228:231], v[244:247], v[124:127], v[228:231]\n"
    "ds_read_b64_tr_b16 v[242:243], v254 offset:36864\n"
    "s_waitcnt lgkmcnt(4)\n"
    "v_mfma_f32_16x16x32_bf16 v[176:179], v[232:235], v[128:131], v[176:179]\n"
    "ds_read_b64_tr_b16 v[244:245], v248 offset:32768\n"
    "v_add_u32 v251, 0x8000, v251\n"
    "v_mfma_f32_16x16x32_bf16 v[180:183], v[232:235], v[132:135], v[180:183]\n"
    "ds_read_b64_tr_b16 v[246:247], v248 offset:36864\n"
    "s_waitcnt lgkmcnt(4)\n"
    "v_mfma_f32_16x16x32_bf16 v[188:191], v[236:239], v[132:135], v[188:191]\n"
    "ds_read_b64_tr_b16 v[232:233], v249 offset:32768\n"
    "v_mfma_f32_16x16x32_bf16 v[184:187], v[236:239], v[128:131], v[184:187]\n"
    "ds_read_b64_tr_b16 v[234:235], v249 offset:36864\n"
    "s_waitcnt lgkmcnt(4)\n"
    "v_mfma_f32_16x16x32_bf16 v[192:195], v[240:243], v[128:131], v[192:195]\n"
    "s_barrier\n"
    "ds_read_b64_tr_b16 v[236:237], v250 offset:32768\n"
    "v_mfma_f32_16x16x32_bf16 v[196:199], v[240:243], v[132:135], v[196:199]\n"
    "ds_read_b64_tr_b16 v[238:239], v250 offset:36864\n"
    "s_waitcnt lgkmcnt(4)\n"
    "v_mfma_f32_16x16x32_bf16 v[204:207], v[244:247], v[132:135], v[204:207]\n"
    "ds_read_b64_tr_b16 v[240:241], v251 offset:32768\n"
    "v_mfma_f32_16x16x32_bf16 v[200:203], v[244:247], v[128:131], v[200:203]\n"
    "ds_read_b64_tr_b16 v[242:243], v251 offset:36864\n"
    "s_waitcnt lgkmcnt(4)\n"
    "v_mfma_f32_16x16x32_bf16 v[208:211], v[232:235], v[128:131], v[208:211]\n"
    "ds_read_b64_tr_b16 v[244:245], v252 offset:40960\n"
    "v_mfma_f32_16x16x32_bf16 v[212:215], v[232:235], v[132:135], v[212:215]\n"
    "ds_read_b64_tr_b16 v[246:247], v252 offset:45056\n"
    "s_waitcnt lgkmcnt(4)\n"
    "v_mfma_f32_16x16x32_bf16 v[220:223], v[236:239], v[132:135], v[220:223]\n"
    "ds_read_b64_tr_b16 v[232:233], v253 offset:40960\n"
    "v_xor_b32 v252, 0, v156\n"
    "v_mfma_f32_16x16x32_bf16 v[216:219], v[236:239], v[128:131], v[216:219]\n"
    "ds_read_b64_tr_b16 v[234:235], v253 offset:45056\n"
    "s_waitcnt lgkmcnt(4)\n"
    "v_mfma_f32_16x16x32_bf16 v[224:227], v[240:243], v[128:131], v[224:227]\n"
    "ds_read_b64_tr_b16 v[236:237], v254 offset:40960\n"
    "v_xor_b32 v253, 32, v156\n"
    "v_mfma_f32_16x16x32_bf16 v[228:231], v[240:243], v[132:135], v[228:231]\n"
    "ds_read_b64_tr_b16 v[238:239], v254 offset:45056\n"
    "s_waitcnt lgkmcnt(4)\n"
    "v_mfma_f32_16x16x32_bf16 v[176:179], v[244:247], v[136:139], v[176:179]\n"
    "ds_read_b64_tr_b16 v[240:241], v248 offset:40960\n"
    "v_xor_b32 v254, 64, v156\n"
    "v_mfma_f32_16x16x32_bf16 v[180:183], v[244:247], v[140:143], v[180:183]\n"
    "ds_read_b64_tr_b16 v[242:243], v248 offset:45056\n"
    "s_waitcnt lgkmcnt(4)\n"
    "v_mfma_f32_16x16x32_bf16 v[188:191], v[232:235], v[140:143], v[188:191]\n"
    "ds_read_b64_tr_b16 v[244:245], v249 offset:40960\n"
    "v_add_u32 v252, 0x18000, v252\n"
    "v_mfma_f32_16x16x32_bf16 v[184:187], v[232:235], v[136:139], v[184:187]\n"
    "ds_read_b64_tr_b16 v[246:247], v249 offset:45056\n"
    "s_waitcnt lgkmcnt(4)\n"
    "v_mfma_f32_16x16x32_bf16 v[192:195], v[236:239], v[136:139], v[192:195]\n"
    "ds_read_b64_tr_b16 v[232:233], v250 offset:40960\n"
    "v_add_u32 v253, 0x18000, v253\n"
    "v_mfma_f32_16x16x32_bf16 v[196:199], v[236:239], v[140:143], v[196:199]\n"
    "ds_read_b64_tr_b16 v[234:235], v250 offset:45056\n"
    "s_waitcnt lgkmcnt(4)\n"
    "v_mfma_f32_16x16x32_bf16 v[204:207], v[240:243], v[140:143], v[204:207]\n"
    "ds_read_b64_tr_b16 v[236:237], v251 offset:40960\n"
    "v_mov_b32 v248, v157\n"
    "v_mfma_f32_16x16x32_bf16 v[200:203], v[240:243], v[136:139], v[200:203]\n"
    "ds_read_b64_tr_b16 v[238:239], v251 offset:45056\n"
    "s_waitcnt lgkmcnt(4)\n"
    "v_mfma_f32_16x16x32_bf16 v[208:211], v[244:247], v[136:139], v[208:211]\n"
    "ds_read_b64_tr_b16 v[240:241], v252 offset:49152\n"
    "v_mov_b32 v249, v158\n"
    "v_mfma_f32_16x16x32_bf16 v[212:215], v[244:247], v[140:143], v[212:215]\n"
    "ds_read_b64_tr_b16 v[242:243], v252 offset:51200\n"
    "s_waitcnt lgkmcnt(4)\n"
    "v_mfma_f32_16x16x32_bf16 v[220:223], v[232:235], v[140:143], v[220:223]\n"
    "ds_read_b64_tr_b16 v[244:245], v253 offset:49152\n"
    "v_lshlrev_b32 v250, 4, v255\n"
    "v_mfma_f32_16x16x32_bf16 v[216:219], v[232:235], v[136:139], v[216:219]\n"
    "ds_read_b64_tr_b16 v[246:247], v253 offset:51200\n"
    "s_waitcnt lgkmcnt(4)\n"
    "v_mfma_f32_16x16x32_bf16 v[224:227], v[236:239], v[136:139], v[224:227]\n"
    "ds_read_b64_tr_b16 v[232:233], v252 offset:53248\n"
    "v_xor_b32 v255, 96, v156\n"
    "v_mfma_f32_16x16x32_bf16 v[228:231], v[236:239], v[140:143], v[228:231]\n"
    "ds_read_b64_tr_b16 v[234:235], v252 offset:55296\n"
    "v_pk_mul_f32 v[176:177], v[176:177], v[154:155] op_sel:[0,0] op_sel_hi:[1,0]\n"
    "v_pk_mul_f32 v[178:179], v[178:179], v[154:155] op_sel:[0,0] op_sel_hi:[1,0]\n"
    "v_pk_mul_f32 v[180:181], v[180:181], v[154:155] op_sel:[0,1] op_sel_hi:[1,1]\n"
    "v_pk_mul_f32 v[182:183], v[182:183], v[154:155] op_sel:[0,1] op_sel_hi:[1,1]\n"
    "v_pk_mul_f32 v[184:185], v[184:185], v[154:155] op_sel:[0,0] op_sel_hi:[1,0]\n"
    "v_pk_mul_f32 v[186:187], v[186:187], v[154:155] op_sel:[0,0] op_sel_hi:[1,0]\n"
    "v_pk_mul_f32 v[188:189], v[188:189], v[154:155] op_sel:[0,1] op_sel_hi:[1,1]\n"
    "v_pk_mul_f32 v[190:191], v[190:191], v[154:155] op_sel:[0,1] op_sel_hi:[1,1]\n"
    "v_pk_mul_f32 v[192:193], v[192:193], v[154:155] op_sel:[0,0] op_sel_hi:[1,0]\n"
    "v_pk_mul_f32 v[194:195], v[194:195], v[154:155] op_sel:[0,0] op_sel_hi:[1,0]\n"
    "v_pk_mul_f32 v[196:197], v[196:197], v[154:155] op_sel:[0,1] op_sel_hi:[1,1]\n"
    "v_pk_mul_f32 v[198:199], v[198:199], v[154:155] op_sel:[0,1] op_sel_hi:[1,1]\n"
    "v_pk_mul_f32 v[200:201], v[200:201], v[154:155] op_sel:[0,0] op_sel_hi:[1,0]\n"
    "v_pk_mul_f32 v[202:203], v[202:203], v[154:155] op_sel:[0,0] op_sel_hi:[1,0]\n"
    "v_pk_mul_f32 v[204:205], v[204:205], v[154:155] op_sel:[0,1] op_sel_hi:[1,1]\n"
    "v_pk_mul_f32 v[206:207], v[206:207], v[154:155] op_sel:[0,1] op_sel_hi:[1,1]\n"
    "v_pk_mul_f32 v[208:209], v[208:209], v[154:155] op_sel:[0,0] op_sel_hi:[1,0]\n"
    "v_pk_mul_f32 v[210:211], v[210:211], v[154:155] op_sel:[0,0] op_sel_hi:[1,0]\n"
    "v_pk_mul_f32 v[212:213], v[212:213], v[154:155] op_sel:[0,1] op_sel_hi:[1,1]\n"
    "v_pk_mul_f32 v[214:215], v[214:215], v[154:155] op_sel:[0,1] op_sel_hi:[1,1]\n"
    "v_pk_mul_f32 v[216:217], v[216:217], v[154:155] op_sel:[0,0] op_sel_hi:[1,0]\n"
    "v_pk_mul_f32 v[218:219], v[218:219], v[154:155] op_sel:[0,0] op_sel_hi:[1,0]\n"
    "v_pk_mul_f32 v[220:221], v[220:221], v[154:155] op_sel:[0,1] op_sel_hi:[1,1]\n"
    "v_pk_mul_f32 v[222:223], v[222:223], v[154:155] op_sel:[0,1] op_sel_hi:[1,1]\n"
    "v_pk_mul_f32 v[224:225], v[224:225], v[154:155] op_sel:[0,0] op_sel_hi:[1,0]\n"
    "v_pk_mul_f32 v[226:227], v[226:227], v[154:155] op_sel:[0,0] op_sel_hi:[1,0]\n"
    "v_pk_mul_f32 v[228:229], v[228:229], v[154:155] op_sel:[0,1] op_sel_hi:[1,1]\n"
    "v_pk_mul_f32 v[230:231], v[230:231], v[154:155] op_sel:[0,1] op_sel_hi:[1,1]\n"
    "v_cvt_pk_bf16_f32 v224, v224, v225\n"
    "v_cvt_pk_bf16_f32 v225, v226, v227\n"
    "v_cvt_pk_bf16_f32 v226, v228, v229\n"
    "v_cvt_pk_bf16_f32 v227, v230, v231\n"
    "v_cndmask_b32 v224, v224, v154, vcc\n"
    "v_cndmask_b32 v225, v225, v155, vcc\n"
    "s_waitcnt lgkmcnt(4)\n"
    "v_mfma_f32_16x16x32_bf16 v[160:163], v[240:243], v[0:3], 0\n"
    "ds_read_b64_tr_b16 v[236:237], v253 offset:53248\n"
    "v_cvt_pk_bf16_f32 v176, v176, v177\n"
    "v_mfma_f32_16x16x32_bf16 v[164:167], v[240:243], v[4:7], 0\n"
    "ds_read_b64_tr_b16 v[238:239], v253 offset:55296\n"
    "s_waitcnt lgkmcnt(4)\n"
    "v_mfma_f32_16x16x32_bf16 v[172:175], v[244:247], v[4:7], 0\n"
    "ds_read_b64_tr_b16 v[240:241], v252 offset:57344\n"
    "v_cvt_pk_bf16_f32 v177, v178, v179\n"
    "v_mfma_f32_16x16x32_bf16 v[168:171], v[244:247], v[0:3], 0\n"
    "ds_read_b64_tr_b16 v[242:243], v252 offset:59392\n"
    "s_waitcnt lgkmcnt(4)\n"
    "v_mfma_f32_16x16x32_bf16 v[160:163], v[232:235], v[8:11], v[160:163]\n"
    "s_barrier\n"
    "ds_read_b64_tr_b16 v[244:245], v253 offset:57344\n"
    "v_mfma_f32_16x16x32_bf16 v[164:167], v[232:235], v[12:15], v[164:167]\n"
    "ds_read_b64_tr_b16 v[246:247], v253 offset:59392\n"
    "s_waitcnt lgkmcnt(4)\n"
    "v_mfma_f32_16x16x32_bf16 v[172:175], v[236:239], v[12:15], v[172:175]\n"
    "v_add_u32 v252, 0xfffe8000, v252\n"
    "ds_read_b64_tr_b16 v[232:233], v252 offset:0\n"
    "v_add_u32 v253, 0xfffe8000, v253\n"
    "v_mfma_f32_16x16x32_bf16 v[168:171], v[236:239], v[8:11], v[168:171]\n"
    "ds_read_b64_tr_b16 v[234:235], v252 offset:2048\n"
    "s_waitcnt lgkmcnt(4)\n"
    "v_mfma_f32_16x16x32_bf16 v[160:163], v[240:243], v[16:19], v[160:163]\n"
    "ds_read_b64_tr_b16 v[236:237], v253 offset:0\n"
    "v_cvt_pk_bf16_f32 v178, v180, v181\n"
    "v_mfma_f32_16x16x32_bf16 v[164:167], v[240:243], v[20:23], v[164:167]\n"
    "ds_read_b64_tr_b16 v[238:239], v253 offset:2048\n"
    "s_waitcnt lgkmcnt(4)\n"
    "v_mfma_f32_16x16x32_bf16 v[172:175], v[244:247], v[20:23], v[172:175]\n"
    "ds_read_b64_tr_b16 v[240:241], v252 offset:4096\n"
    "v_cvt_pk_bf16_f32 v179, v182, v183\n"
    "v_mfma_f32_16x16x32_bf16 v[168:171], v[244:247], v[16:19], v[168:171]\n"
    "ds_read_b64_tr_b16 v[242:243], v252 offset:6144\n"
    "s_waitcnt lgkmcnt(4)\n"
    "v_mfma_f32_16x16x32_bf16 v[160:163], v[232:235], v[24:27], v[160:163]\n"
    "ds_read_b64_tr_b16 v[244:245], v253 offset:4096\n"
    "v_add_u32 v254, 0x18000, v254\n"
    "v_mfma_f32_16x16x32_bf16 v[164:167], v[232:235], v[28:31], v[164:167]\n"
    "ds_read_b64_tr_b16 v[246:247], v253 offset:6144\n"
    "s_waitcnt lgkmcnt(4)\n"
    "v_mfma_f32_16x16x32_bf16 v[172:175], v[236:239], v[28:31], v[172:175]\n"
    "ds_read_b64_tr_b16 v[232:233], v252 offset:8192\n"
    "v_add_u32 v255, 0x18000, v255\n"
    "v_mfma_f32_16x16x32_bf16 v[168:171], v[236:239], v[24:27], v[168:171]\n"
    "ds_read_b64_tr_b16 v[234:235], v252 offset:10240\n"
    "s_waitcnt lgkmcnt(4)\n"
    "v_mfma_f32_16x16x32_bf16 v[160:163], v[240:243], v[32:35], v[160:163]\n"
    "ds_read_b64_tr_b16 v[236:237], v253 offset:8192\n"
    "buffer_store_dwordx4 v[176:179], v248, s[60:63], s44 offen offset:0 sc1\n"
    "v_mfma_f32_16x16x32_bf16 v[164:167], v[240:243], v[36:39], v[164:167]\n"
    "ds_read_b64_tr_b16 v[238:239], v253 offset:10240\n"
    "s_waitcnt lgkmcnt(4)\n"
    "v_mfma_f32_16x16x32_bf16 v[172:175], v[244:247], v[36:39], v[172:175]\n"
    "ds_read_b64_tr_b16 v[240:241], v254 offset:49152\n"
    "v_cvt_pk_bf16_f32 v184, v184, v185\n"
    "v_mfma_f32_16x16x32_bf16 v[168:171], v[244:247], v[32:35], v[168:171]\n"
    "ds_read_b64_tr_b16 v[242:243], v254 offset:51200\n"
    "s_waitcnt lgkmcnt(4)\n"
    "v_mfma_f32_16x16x32_bf16 v[160:163], v[232:235], v[40:43], v[160:163]\n"
    "ds_read_b64_tr_b16 v[244:245], v255 offset:49152\n"
    "v_cvt_pk_bf16_f32 v185, v186, v187\n"
    "v_mfma_f32_16x16x32_bf16 v[164:167], v[232:235], v[44:47], v[164:167]\n"
    "ds_read_b64_tr_b16 v[246:247], v255 offset:51200\n"
    "s_waitcnt lgkmcnt(4)\n"
    "v_mfma_f32_16x16x32_bf16 v[172:175], v[236:239], v[44:47], v[172:175]\n"
    "ds_read_b64_tr_b16 v[232:233], v252 offset:12288\n"
    "v_cvt_pk_bf16_f32 v186, v188, v189\n"
    "v_mfma_f32_16x16x32_bf16 v[168:171], v[236:239], v[40:43], v[168:171]\n"
    "ds_read_b64_tr_b16 v[234:235], v252 offset:14336\n"
    "s_waitcnt lgkmcnt(4)\n"
    "v_mfma_f32_16x16x32_bf16 v[144:147], v[240:243], v[0:3], 0\n"
    "ds_read_b64_tr_b16 v[236:237], v253 offset:12288\n"
    "v_cvt_pk_bf16_f32 v187, v190, v191\n"
    "v_mfma_f32_16x16x32_bf16 v[148:151], v[240:243], v[4:7], 0\n"
    "ds_read_b64_tr_b16 v[238:239], v253 offset:14336\n"
    "s_waitcnt lgkmcnt(4)\n"
    "v_mfma_f32_16x16x32_bf16 v[156:159], v[244:247], v[4:7], 0\n"
    "ds_read_b64_tr_b16 v[240:241], v254 offset:53248\n"
    "buffer_store_dwordx4 v[184:187], v248, s[60:63], s44 offen offset:1024 sc1\n"
    "v_mfma_f32_16x16x32_bf16 v[152:155], v[244:247], v[0:3], 0\n"
    "ds_read_b64_tr_b16 v[242:243], v254 offset:55296\n"
    "s_waitcnt lgkmcnt(4)\n"
    "v_mfma_f32_16x16x32_bf16 v[160:163], v[232:235], v[48:51], v[160:163]\n"
    "ds_read_b64_tr_b16 v[244:245], v255 offset:53248\n"
    "v_cvt_pk_bf16_f32 v192, v192, v193\n"
    "v_mfma_f32_16x16x32_bf16 v[164:167], v[232:235], v[52:55], v[164:167]\n"
    "ds_read_b64_tr_b16 v[246:247], v255 offset:55296\n"
    "s_waitcnt lgkmcnt(4)\n"
    "v_mfma_f32_16x16x32_bf16 v[172:175], v[236:239], v[52:55], v[172:175]\n"
    "ds_read_b64_tr_b16 v[232:233], v252 offset:16384\n"
    "v_cvt_pk_bf16_f32 v193, v194, v195\n"
    "v_mfma_f32_16x16x32_bf16 v[168:171], v[236:239], v[48:51], v[168:171]\n"
    "ds_read_b64_tr_b16 v[234:235], v252 offset:18432\n"
    "s_waitcnt lgkmcnt(4)\n"
    "v_mfma_f32_16x16x32_bf16 v[144:147], v[240:243], v[8:11], v[144:147]\n"
    "ds_read_b64_tr_b16 v[236:237], v253 offset:16384\n"
    "v_cvt_pk_bf16_f32 v194, v196, v197\n"
    "v_mfma_f32_16x16x32_bf16 v[148:151], v[240:243], v[12:15], v[148:151]\n"
    "ds_read_b64_tr_b16 v[238:239], v253 offset:18432\n"
    "s_waitcnt lgkmcnt(4)\n"
    "v_mfma_f32_16x16x32_bf16 v[156:159], v[244:247], v[12:15], v[156:159]\n"
    "ds_read_b64_tr_b16 v[240:241], v254 offset:57344\n"
    "v_cvt_pk_bf16_f32 v195, v198, v199\n"
    "v_mfma_f32_16x16x32_bf16 v[152:155], v[244:247], v[8:11], v[152:155]\n"
    "ds_read_b64_tr_b16 v[242:243], v254 offset:59392\n"
    "s_waitcnt lgkmcnt(4)\n"
    "v_mfma_f32_16x16x32_bf16 v[160:163], v[232:235], v[56:59], v[160:163]\n"
    "ds_read_b64_tr_b16 v[244:245], v255 offset:57344\n"
    "v_add_u32 v254, 0xfffe8000, v254\n"
    "v_mfma_f32_16x16x32_bf16 v[164:167], v[232:235], v[60:63], v[164:167]\n"
    "ds_read_b64_tr_b16 v[246:247], v255 offset:59392\n"
    "s_waitcnt lgkmcnt(4)\n"
    "v_mfma_f32_16x16x32_bf16 v[172:175], v[236:239], v[60:63], v[172:175]\n"
    "ds_read_b64_tr_b16 v[232:233], v252 offset:20480\n"
    "v_add_u32 v255, 0xfffe8000, v255\n"
    "v_mfma_f32_16x16x32_bf16 v[168:171], v[236:239], v[56:59], v[168:171]\n"
    "ds_read_b64_tr_b16 v[234:235], v252 offset:22528\n"
    "s_waitcnt lgkmcnt(4)\n"
    "v_mfma_f32_16x16x32_bf16 v[144:147], v[240:243], v[16:19], v[144:147]\n"
    "s_barrier\n"
    "ds_read_b64_tr_b16 v[236:237], v253 offset:20480\n"
    "v_mfma_f32_16x16x32_bf16 v[148:151], v[240:243], v[20:23], v[148:151]\n"
    "ds_read_b64_tr_b16 v[238:239], v253 offset:22528\n"
    "s_waitcnt lgkmcnt(4)\n"
    "v_mfma_f32_16x16x32_bf16 v[156:159], v[244:247], v[20:23], v[156:159]\n"
    "ds_read_b64_tr_b16 v[240:241], v254 offset:0\n"
    "buffer_store_dwordx4 v[192:195], v248, s[60:63], s45 offen offset:0 sc1\n"
    "v_mfma_f32_16x16x32_bf16 v[152:155], v[244:247], v[16:19], v[152:155]\n"
    "ds_read_b64_tr_b16 v[242:243], v254 offset:2048\n"
    "s_waitcnt lgkmcnt(4)\n"
    "v_mfma_f32_16x16x32_bf16 v[160:163], v[232:235], v[64:67], v[160:163]\n"
    "ds_read_b64_tr_b16 v[244:245], v255 offset:0\n"
    "v_cvt_pk_bf16_f32 v200, v200, v201\n"
    "v_mfma_f32_16x16x32_bf16 v[164:167], v[232:235], v[68:71], v[164:167]\n"
    "ds_read_b64_tr_b16 v[246:247], v255 offset:2048\n"
    "s_waitcnt lgkmcnt(4)\n"
    "v_mfma_f32_16x16x32_bf16 v[172:175], v[236:239], v[68:71], v[172:175]\n"
    "ds_read_b64_tr_b16 v[232:233], v252 offset:24576\n"
    "v_cvt_pk_bf16_f32 v201, v202, v203\n"
    "v_mfma_f32_16x16x32_bf16 v[168:171], v[236:239], v[64:67], v[168:171]\n"
    "ds_read_b64_tr_b16 v[234:235], v252 offset:26624\n"
    "s_waitcnt lgkmcnt(4)\n"
    "v_mfma_f32_16x16x32_bf16 v[144:147], v[240:243], v[24:27], v[144:147]\n"
    "ds_read_b64_tr_b16 v[236:237], v253 offset:24576\n"
    "v_cvt_pk_bf16_f32 v202, v204, v205\n"
    "v_mfma_f32_16x16x32_bf16 v[148:151], v[240:243], v[28:31], v[148:151]\n"
    "ds_read_b64_tr_b16 v[238:239], v253 offset:26624\n"
    "s_waitcnt lgkmcnt(4)\n"
    "v_mfma_f32_16x16x32_bf16 v[156:159], v[244:247], v[28:31], v[156:159]\n"
    "ds_read_b64_tr_b16 v[240:241], v254 offset:4096\n"
    "v_cvt_pk_bf16_f32 v203, v206, v207\n"
    "v_mfma_f32_16x16x32_bf16 v[152:155], v[244:247], v[24:27], v[152:155]\n"
    "ds_read_b64_tr_b16 v[242:243], v254 offset:6144\n"
    "s_waitcnt lgkmcnt(4)\n"
    "v_mfma_f32_16x16x32_bf16 v[160:163], v[232:235], v[72:75], v[160:163]\n"
    "ds_read_b64_tr_b16 v[244:245], v255 offset:4096\n"
    "buffer_store_dwordx4 v[200:203], v248, s[60:63], s45 offen offset:1024 sc1\n"
    "v_mfma_f32_16x16x32_bf16 v[164:167], v[232:235], v[76:79], v[164:167]\n"
    "ds_read_b64_tr_b16 v[246:247], v255 offset:6144\n"
    "s_waitcnt lgkmcnt(4)\n"
    "v_mfma_f32_16x16x32_bf16 v[172:175], v[236:239], v[76:79], v[172:175]\n"
    "ds_read_b64_tr_b16 v[232:233], v252 offset:28672\n"
    "v_cvt_pk_bf16_f32 v208, v208, v209\n"
    "v_mfma_f32_16x16x32_bf16 v[168:171], v[236:239], v[72:75], v[168:171]\n"
    "ds_read_b64_tr_b16 v[234:235], v252 offset:30720\n"
    "s_waitcnt lgkmcnt(4)\n"
    "v_mfma_f32_16x16x32_bf16 v[144:147], v[240:243], v[32:35], v[144:147]\n"
    "ds_read_b64_tr_b16 v[236:237], v253 offset:28672\n"
    "v_cvt_pk_bf16_f32 v209, v210, v211\n"
    "v_mfma_f32_16x16x32_bf16 v[148:151], v[240:243], v[36:39], v[148:151]\n"
    "ds_read_b64_tr_b16 v[238:239], v253 offset:30720\n"
    "s_waitcnt lgkmcnt(4)\n"
    "v_mfma_f32_16x16x32_bf16 v[156:159], v[244:247], v[36:39], v[156:159]\n"
    "ds_read_b64_tr_b16 v[240:241], v254 offset:8192\n"
    "v_cvt_pk_bf16_f32 v210, v212, v213\n"
    "v_mfma_f32_16x16x32_bf16 v[152:155], v[244:247], v[32:35], v[152:155]\n"
    "ds_read_b64_tr_b16 v[242:243], v254 offset:10240\n"
    "s_waitcnt lgkmcnt(4)\n"
    "v_mfma_f32_16x16x32_bf16 v[160:163], v[232:235], v[80:83], v[160:163]\n"
    "ds_read_b64_tr_b16 v[244:245], v255 offset:8192\n"
    "v_cvt_pk_bf16_f32 v211, v214, v215\n"
    "v_mfma_f32_16x16x32_bf16 v[164:167], v[232:235], v[84:87], v[164:167]\n"
    "ds_read_b64_tr_b16 v[246:247], v255 offset:10240\n"
    "s_waitcnt lgkmcnt(4)\n"
    "v_mfma_f32_16x16x32_bf16 v[172:175], v[236:239], v[84:87], v[172:175]\n"
    "ds_read_b64_tr_b16 v[232:233], v252 offset:32768\n"
    "buffer_store_dwordx4 v[208:211], v248, s[60:63], s46 offen offset:0 sc1\n"
    "v_mfma_f32_16x16x32_bf16 v[168:171], v[236:239], v[80:83], v[168:171]\n"
    "ds_read_b64_tr_b16 v[234:235], v252 offset:34816\n"
    "s_waitcnt lgkmcnt(4)\n"
    "v_mfma_f32_16x16x32_bf16 v[144:147], v[240:243], v[40:43], v[144:147]\n"
    "ds_read_b64_tr_b16 v[236:237], v253 offset:32768\n"
    "v_cvt_pk_bf16_f32 v216, v216, v217\n"
    "v_mfma_f32_16x16x32_bf16 v[148:151], v[240:243], v[44:47], v[148:151]\n"
    "ds_read_b64_tr_b16 v[238:239], v253 offset:34816\n"
    "s_waitcnt lgkmcnt(4)\n"
    "v_mfma_f32_16x16x32_bf16 v[156:159], v[244:247], v[44:47], v[156:159]\n"
    "ds_read_b64_tr_b16 v[240:241], v254 offset:12288\n"
    "v_cvt_pk_bf16_f32 v217, v218, v219\n"
    "v_mfma_f32_16x16x32_bf16 v[152:155], v[244:247], v[40:43], v[152:155]\n"
    "ds_read_b64_tr_b16 v[242:243], v254 offset:14336\n"
    "s_waitcnt lgkmcnt(4)\n"
    "v_mfma_f32_16x16x32_bf16 v[160:163], v[232:235], v[88:91], v[160:163]\n"
    "ds_read_b64_tr_b16 v[244:245], v255 offset:12288\n"
    "v_cvt_pk_bf16_f32 v218, v220, v221\n"
    "v_mfma_f32_16x16x32_bf16 v[164:167], v[232:235], v[92:95], v[164:167]\n"
    "ds_read_b64_tr_b16 v[246:247], v255 offset:14336\n"
    "s_waitcnt lgkmcnt(4)\n"
    "v_mfma_f32_16x16x32_bf16 v[172:175], v[236:239], v[92:95], v[172:175]\n"
    "ds_read_b64_tr_b16 v[232:233], v252 offset:36864\n"
    "v_cvt_pk_bf16_f32 v219, v222, v223\n"
    "v_mfma_f32_16x16x32_bf16 v[168:171], v[236:239], v[88:91], v[168:171]\n"
    "ds_read_b64_tr_b16 v[234:235], v252 offset:38912\n"
    "s_waitcnt lgkmcnt(4)\n"
    "v_mfma_f32_16x16x32_bf16 v[144:147], v[240:243], v[48:51], v[144:147]\n"
    "ds_read_b64_tr_b16 v[236:237], v253 offset:36864\n"
    "buffer_store_dwordx4 v[216:219], v248, s[60:63], s46 offen offset:1024 sc1\n"
    "v_mfma_f32_16x16x32_bf16 v[148:151], v[240:243], v[52:55], v[148:151]\n"
    "ds_read_b64_tr_b16 v[238:239], v253 offset:38912\n"
    "s_waitcnt lgkmcnt(4)\n"
    "v_mfma_f32_16x16x32_bf16 v[156:159], v[244:247], v[52:55], v[156:159]\n"
    "ds_read_b64_tr_b16 v[240:241], v254 offset:16384\n"
    "v_mov_b32 v228, 0xff81ff81\n"
    "v_mfma_f32_16x16x32_bf16 v[152:155], v[244:247], v[48:51], v[152:155]\n"
    "ds_read_b64_tr_b16 v[242:243], v254 offset:18432\n"
    "s_waitcnt lgkmcnt(4)\n"
    "v_mfma_f32_16x16x32_bf16 v[160:163], v[232:235], v[96:99], v[160:163]\n"
    "s_barrier\n"
    "ds_read_b64_tr_b16 v[244:245], v255 offset:16384\n"
    "v_mfma_f32_16x16x32_bf16 v[164:167], v[232:235], v[100:103], v[164:167]\n"
    "ds_read_b64_tr_b16 v[246:247], v255 offset:18432\n"
    "s_waitcnt lgkmcnt(4)\n"
    "v_mfma_f32_16x16x32_bf16 v[172:175], v[236:239], v[100:103], v[172:175]\n"
    "ds_read_b64_tr_b16 v[232:233], v252 offset:40960\n"
    "v_mov_b32 v229, 0xff81ff81\n"
    "v_mfma_f32_16x16x32_bf16 v[168:171], v[236:239], v[96:99], v[168:171]\n"
    "ds_read_b64_tr_b16 v[234:235], v252 offset:43008\n"
    "s_waitcnt lgkmcnt(4)\n"
    "v_mfma_f32_16x16x32_bf16 v[144:147], v[240:243], v[56:59], v[144:147]\n"
    "ds_read_b64_tr_b16 v[236:237], v253 offset:40960\n"
    "v_mov_b32 v230, 0xff81ff81\n"
    "v_mfma_f32_16x16x32_bf16 v[148:151], v[240:243], v[60:63], v[148:151]\n"
    "ds_read_b64_tr_b16 v[238:239], v253 offset:43008\n"
    "s_waitcnt lgkmcnt(4)\n"
    "v_mfma_f32_16x16x32_bf16 v[156:159], v[244:247], v[60:63], v[156:159]\n"
    "ds_read_b64_tr_b16 v[240:241], v254 offset:20480\n"
    "v_mov_b32 v231, 0xff81ff81\n"
    "v_mfma_f32_16x16x32_bf16 v[152:155], v[244:247], v[56:59], v[152:155]\n"
    "ds_read_b64_tr_b16 v[242:243], v254 offset:22528\n"
    "s_waitcnt lgkmcnt(4)\n"
    "v_mfma_f32_16x16x32_bf16 v[160:163], v[232:235], v[104:107], v[160:163]\n"
    "ds_read_b64_tr_b16 v[244:245], v255 offset:20480\n"
    "buffer_store_dwordx4 v[224:227], v249, s[60:63], 0 offen offset:2048 sc1\n"
    "v_mfma_f32_16x16x32_bf16 v[164:167], v[232:235], v[108:111], v[164:167]\n"
    "ds_read_b64_tr_b16 v[246:247], v255 offset:22528\n"
    "s_waitcnt lgkmcnt(4)\n"
    "v_mfma_f32_16x16x32_bf16 v[172:175], v[236:239], v[108:111], v[172:175]\n"
    "ds_read_b64_tr_b16 v[232:233], v252 offset:45056\n"
    "v_mfma_f32_16x16x32_bf16 v[168:171], v[236:239], v[104:107], v[168:171]\n"
    "ds_read_b64_tr_b16 v[234:235], v252 offset:47104\n"
    "s_waitcnt lgkmcnt(4)\n"
    "v_mfma_f32_16x16x32_bf16 v[144:147], v[240:243], v[64:67], v[144:147]\n"
    "ds_read_b64_tr_b16 v[236:237], v253 offset:45056\n"
    "v_mfma_f32_16x16x32_bf16 v[148:151], v[240:243], v[68:71], v[148:151]\n"
    "ds_read_b64_tr_b16 v[238:239], v253 offset:47104\n"
    "s_waitcnt lgkmcnt(4)\n"
    "v_mfma_f32_16x16x32_bf16 v[156:159], v[244:247], v[68:71], v[156:159]\n"
    "ds_read_b64_tr_b16 v[240:241], v254 offset:24576\n"
    "v_mfma_f32_16x16x32_bf16 v[152:155], v[244:247], v[64:67], v[152:155]\n"
    "ds_read_b64_tr_b16 v[242:243], v254 offset:26624\n"
    "s_waitcnt lgkmcnt(4)\n"
    "v_mfma_f32_16x16x32_bf16 v[160:163], v[232:235], v[112:115], v[160:163]\n"
    "ds_read_b64_tr_b16 v[244:245], v255 offset:24576\n"
    "v_mfma_f32_16x16x32_bf16 v[164:167], v[232:235], v[116:119], v[164:167]\n"
    "ds_read_b64_tr_b16 v[246:247], v255 offset:26624\n"
    "s_waitcnt lgkmcnt(4)\n"
    "v_mfma_f32_16x16x32_bf16 v[172:175], v[236:239], v[116:119], v[172:175]\n"
    "ds_read_b64_tr_b16 v[232:233], v252 offset:49152\n"
    "v_mfma_f32_16x16x32_bf16 v[168:171], v[236:239], v[112:115], v[168:171]\n"
    "ds_read_b64_tr_b16 v[234:235], v252 offset:51200\n"
    "s_waitcnt lgkmcnt(4)\n"
    "v_mfma_f32_16x16x32_bf16 v[144:147], v[240:243], v[72:75], v[144:147]\n"
    "ds_read_b64_tr_b16 v[236:237], v253 offset:49152\n"
    "v_mfma_f32_16x16x32_bf16 v[148:151], v[240:243], v[76:79], v[148:151]\n"
    "ds_read_b64_tr_b16 v[238:239], v253 offset:51200\n"
    "s_waitcnt lgkmcnt(4)\n"
    "v_mfma_f32_16x16x32_bf16 v[156:159], v[244:247], v[76:79], v[156:159]\n"
    "ds_read_b64_tr_b16 v[240:241], v254 offset:28672\n"
    "v_mfma_f32_16x16x32_bf16 v[152:155], v[244:247], v[72:75], v[152:155]\n"
    "ds_read_b64_tr_b16 v[242:243], v254 offset:30720\n"
    "s_waitcnt lgkmcnt(4)\n"
    "v_mfma_f32_16x16x32_bf16 v[160:163], v[232:235], v[120:123], v[160:163]\n"
    "ds_read_b64_tr_b16 v[244:245], v255 offset:28672\n"
    "v_mfma_f32_16x16x32_bf16 v[164:167], v[232:235], v[124:127], v[164:167]\n"
    "ds_read_b64_tr_b16 v[246:247], v255 offset:30720\n"
    "s_waitcnt lgkmcnt(4)\n"
    "v_mfma_f32_16x16x32_bf16 v[172:175], v[236:239], v[124:127], v[172:175]\n"
    "ds_read_b64_tr_b16 v[232:233], v252 offset:53248\n"
    "v_mfma_f32_16x16x32_bf16 v[168:171], v[236:239], v[120:123], v[168:171]\n"
    "ds_read_b64_tr_b16 v[234:235], v252 offset:55296\n"
    "s_waitcnt lgkmcnt(4)\n"
    "v_mfma_f32_16x16x32_bf16 v[144:147], v[240:243], v[80:83], v[144:147]\n"
    "ds_read_b64_tr_b16 v[236:237], v253 offset:53248\n"
    "v_mfma_f32_16x16x32_bf16 v[148:151], v[240:243], v[84:87], v[148:151]\n"
    "ds_read_b64_tr_b16 v[238:239], v253 offset:55296\n"
    "s_waitcnt lgkmcnt(4)\n"
    "v_mfma_f32_16x16x32_bf16 v[156:159], v[244:247], v[84:87], v[156:159]\n"
    "ds_read_b64_tr_b16 v[240:241], v254 offset:32768\n"
    "v_mfma_f32_16x16x32_bf16 v[152:155], v[244:247], v[80:83], v[152:155]\n"
    "ds_read_b64_tr_b16 v[242:243], v254 offset:34816\n"
    "s_waitcnt lgkmcnt(4)\n"
    "v_mfma_f32_16x16x32_bf16 v[160:163], v[232:235], v[128:131], v[160:163]\n"
    "ds_read_b64_tr_b16 v[244:245], v255 offset:32768\n"
    "v_mfma_f32_16x16x32_bf16 v[164:167], v[232:235], v[132:135], v[164:167]\n"
    "ds_read_b64_tr_b16 v[246:247], v255 offset:34816\n"
    "s_waitcnt lgkmcnt(4)\n"
    "v_mfma_f32_16x16x32_bf16 v[172:175], v[236:239], v[132:135], v[172:175]\n"
    "ds_read_b64_tr_b16 v[232:233], v252 offset:57344\n"
    "v_mfma_f32_16x16x32_bf16 v[168:171], v[236:239], v[128:131], v[168:171]\n"
    "ds_read_b64_tr_b16 v[234:235], v252 offset:59392\n"
    "s_waitcnt lgkmcnt(4)\n"
    "v_mfma_f32_16x16x32_bf16 v[144:147], v[240:243], v[88:91], v[144:147]\n"
    "s_barrier\n"
    "L_st3_%=:\n"
    "s_cbranch_execz L_bk%=\n"
    "ds_read_b64_tr_b16 v[236:237], v253 offset:57344\n"
    "v_mfma_f32_16x16x32_bf16 v[148:151], v[240:243], v[92:95], v[148:151]\n"
    "ds_read_b64_tr_b16 v[238:239], v253 offset:59392\n"
    "s_waitcnt lgkmcnt(4)\n"
    "v_mfma_f32_16x16x32_bf16 v[156:159], v[244:247], v[92:95], v[156:159]\n"
    "ds_read_b64_tr_b16 v[240:241], v254 offset:36864\n"
    "v_mfma_f32_16x16x32_bf16 v[152:155], v[244:247], v[88:91], v[152:155]\n"
    "ds_read_b64_tr_b16 v[242:243], v254 offset:38912\n"
    "s_waitcnt lgkmcnt(4)\n"
    "v_mfma_f32_16x16x32_bf16 v[160:163], v[232:235], v[136:139], v[160:163]\n"
    "ds_read_b64_tr_b16 v[244:245], v255 offset:36864\n"
    "v_mfma_f32_16x16x32_bf16 v[164:167], v[232:235], v[140:143], v[164:167]\n"
    "ds_read_b64_tr_b16 v[246:247], v255 offset:38912\n"
    "s_waitcnt lgkmcnt(4)\n"
    "v_mfma_f32_16x16x32_bf16 v[172:175], v[236:239], v[140:143], v[172:175]\n"
    "ds_read_b64_tr_b16 v[232:233], v254 offset:40960\n"
    "v_mfma_f32_16x16x32_bf16 v[168:171], v[236:239], v[136:139], v[168:171]\n"
    "ds_read_b64_tr_b16 v[234:235], v254 offset:43008\n"
    "s_waitcnt lgkmcnt(4)\n"
    "v_mfma_f32_16x16x32_bf16 v[144:147], v[240:243], v[96:99], v[144:147]\n"
    "ds_read_b64_tr_b16 v[236:237], v255 offset:40960\n"
    "v_mfma_f32_16x16x32_bf16 v[148:151], v[240:243], v[100:103], v[148:151]\n"
    "ds_read_b64_tr_b16 v[238:239], v255 offset:43008\n"
    "s_waitcnt lgkmcnt(4)\n"
    "v_mfma_f32_16x16x32_bf16 v[156:159], v[244:247], v[100:103], v[156:159]\n"
    "ds_read_b64_tr_b16 v[240:241], v254 offset:45056\n"
    "v_cvt_pk_bf16_f32 v176, v160, v161\n"
    "v_mfma_f32_16x16x32_bf16 v[152:155], v[244:247], v[96:99], v[152:155]\n"
    "ds_read_b64_tr_b16 v[242:243], v254 offset:47104\n"
    "s_waitcnt lgkmcnt(4)\n"
    "v_mfma_f32_16x16x32_bf16 v[144:147], v[232:235], v[104:107], v[144:147]\n"
    "ds_read_b64_tr_b16 v[244:245], v255 offset:45056\n"
    "v_cvt_pk_bf16_f32 v177, v162, v163\n"
    "v_mfma_f32_16x16x32_bf16 v[148:151], v[232:235], v[108:111], v[148:151]\n"
    "ds_read_b64_tr_b16 v[246:247], v255 offset:47104\n"
    "s_waitcnt lgkmcnt(4)\n"
    "v_mfma_f32_16x16x32_bf16 v[156:159], v[236:239], v[108:111], v[156:159]\n"
    "ds_read_b64_tr_b16 v[232:233], v254 offset:49152\n"
    "v_cvt_pk_bf16_f32 v178, v168, v169\n"
    "v_mfma_f32_16x16x32_bf16 v[152:155], v[236:239], v[104:107], v[152:155]\n"
    "ds_read_b64_tr_b16 v[234:235], v254 offset:51200\n"
    "s_waitcnt lgkmcnt(4)\n"
    "v_mfma_f32_16x16x32_bf16 v[144:147], v[240:243], v[112:115], v[144:147]\n"
    "ds_read_b64_tr_b16 v[236:237], v255 offset:49152\n"
    "v_cvt_pk_bf16_f32 v179, v170, v171\n"
    "v_mfma_f32_16x16x32_bf16 v[148:151], v[240:243], v[116:119], v[148:151]\n"
    "ds_read_b64_tr_b16 v[238:239], v255 offset:51200\n"
    "s_waitcnt lgkmcnt(4)\n"
    "v_mfma_f32_16x16x32_bf16 v[156:159], v[244:247], v[116:119], v[156:159]\n"
    "ds_read_b64_tr_b16 v[240:241], v254 offset:53248\n"
    "v_cvt_pk_bf16_f32 v180, v164, v165\n"
    "v_mfma_f32_16x16x32_bf16 v[152:155], v[244:247], v[112:115], v[152:155]\n"
    "ds_read_b64_tr_b16 v[242:243], v254 offset:55296\n"
    "s_waitcnt lgkmcnt(4)\n"
    "v_mfma_f32_16x16x32_bf16 v[144:147], v[232:235], v[120:123], v[144:147]\n"
    "ds_read_b64_tr_b16 v[244:245], v255 offset:53248\n"
    "v_cvt_pk_bf16_f32 v181, v166, v167\n"
    "v_mfma_f32_16x16x32_bf16 v[148:151], v[232:235], v[124:127], v[148:151]\n"
    "ds_read_b64_tr_b16 v[246:247], v255 offset:55296\n"
    "s_waitcnt lgkmcnt(4)\n"
    "v_mfma_f32_16x16x32_bf16 v[156:159], v[236:239], v[124:127], v[156:159]\n"
    "ds_read_b64_tr_b16 v[232:233], v254 offset:57344\n"
    "v_cvt_pk_bf16_f32 v182, v172, v173\n"
    "v_mfma_f32_16x16x32_bf16 v[152:155], v[236:239], v[120:123], v[152:155]\n"
    "ds_read_b64_tr_b16 v[234:235], v254 offset:59392\n"
    "s_waitcnt lgkmcnt(4)\n"
    "v_mfma_f32_16x16x32_bf16 v[144:147], v[240:243], v[128:131], v[144:147]\n"
    "ds_read_b64_tr_b16 v[236:237], v255 offset:57344\n"
    "v_cvt_pk_bf16_f32 v183, v174, v175\n"
    "v_mfma_f32_16x16x32_bf16 v[148:151], v[240:243], v[132:135], v[148:151]\n"
    "ds_read_b64_tr_b16 v[238:239], v255 offset:59392\n"
    "s_waitcnt lgkmcnt(4)\n"
    "v_mfma_f32_16x16x32_bf16 v[156:159], v[244:247], v[132:135], v[156:159]\n"
    "ds_read_b64_tr_b16 v[240:241], v252 offset:61440\n"
    "v_mfma_f32_16x16x32_bf16 v[152:155], v[244:247], v[128:131], v[152:155]\n"
    "ds_read_b64_tr_b16 v[242:243], v252 offset:63488\n"
    "s_waitcnt lgkmcnt(4)\n"
    "v_mfma_f32_16x16x32_bf16 v[144:147], v[232:235], v[136:139], v[144:147]\n"
    "ds_read_b64_tr_b16 v[244:245], v253 offset:61440\n"
    "v_add_u32 v252, 0x10000, v252\n"
    "v_mfma_f32_16x16x32_bf16 v[148:151], v[232:235], v[140:143], v[148:151]\n"
    "ds_read_b64_tr_b16 v[246:247], v253 offset:63488\n"
    "s_waitcnt lgkmcnt(4)\n"
    "v_mfma_f32_16x16x32_bf16 v[156:159], v[236:239], v[140:143], v[156:159]\n"
    "ds_read_b64_tr_b16 v[232:233], v252 offset:0\n"
    "v_add_u32 v253, 0x10000, v253\n"
    "v_mfma_f32_16x16x32_bf16 v[152:155], v[236:239], v[136:139], v[152:155]\n"
    "ds_read_b64_tr_b16 v[234:235], v252 offset:2048\n"
    "s_waitcnt lgkmcnt(4)\n"
    "v_mfma_f32_16x16x32_bf16 v[160:163], v[240:243], v[0:3], 0\n"
    "ds_read_b64_tr_b16 v[236:237], v253 offset:0\n"
    "v_mfma_f32_16x16x32_bf16 v[164:167], v[240:243], v[4:7], 0\n"
    "ds_read_b64_tr_b16 v[238:239], v253 offset:2048\n"
    "s_waitcnt lgkmcnt(4)\n"
    "v_mfma_f32_16x16x32_bf16 v[172:175], v[244:247], v[4:7], 0\n"
    "ds_read_b64_tr_b16 v[240:241], v252 offset:4096\n"
    "v_cvt_pk_bf16_f32 v184, v144, v145\n"
    "v_mfma_f32_16x16x32_bf16 v[168:171], v[244:247], v[0:3], 0\n"
    "ds_read_b64_tr_b16 v[242:243], v252 offset:6144\n"
    "s_waitcnt lgkmcnt(4)\n"
    "v_mfma_f32_16x16x32_bf16 v[160:163], v[232:235], v[8:11], v[160:163]\n"
    "s_waitcnt vmcnt(0)\n"
    "s_barrier\n"
    "ds_read_b64_tr_b16 v[244:245], v253 offset:4096\n"
    "v_mfma_f32_16x16x32_bf16 v[164:167], v[232:235], v[12:15], v[164:167]\n"
    "ds_read_b64_tr_b16 v[246:247], v253 offset:6144\n"
    "s_waitcnt lgkmcnt(4)\n"
    "v_mfma_f32_16x16x32_bf16 v[172:175], v[236:239], v[12:15], v[172:175]\n"
    "ds_read_b64_tr_b16 v[232:233], v252 offset:8192\n"
    "v_cvt_pk_bf16_f32 v185, v146, v147\n"
    "v_mfma_f32_16x16x32_bf16 v[168:171], v[236:239], v[8:11], v[168:171]\n"
    "ds_read_b64_tr_b16 v[234:235], v252 offset:10240\n"
    "s_waitcnt lgkmcnt(4)\n"
    "v_mfma_f32_16x16x32_bf16 v[160:163], v[240:243], v[16:19], v[160:163]\n"
    "ds_read_b64_tr_b16 v[236:237], v253 offset:8192\n"
    "v_cvt_pk_bf16_f32 v186, v152, v153\n"
    "v_mfma_f32_16x16x32_bf16 v[164:167], v[240:243], v[20:23], v[164:167]\n"
    "ds_read_b64_tr_b16 v[238:239], v253 offset:10240\n"
    "s_waitcnt lgkmcnt(4)\n"
    "v_mfma_f32_16x16x32_bf16 v[172:175], v[244:247], v[20:23], v[172:175]\n"
    "ds_read_b64_tr_b16 v[240:241], v252 offset:12288\n"
    "v_cvt_pk_bf16_f32 v187, v154, v155\n"
    "v_mfma_f32_16x16x32_bf16 v[168:171], v[244:247], v[16:19], v[168:171]\n"
    "ds_read_b64_tr_b16 v[242:243], v252 offset:14336\n"
    "s_waitcnt lgkmcnt(4)\n"
    "v_mfma_f32_16x16x32_bf16 v[160:163], v[232:235], v[24:27], v[160:163]\n"
    "ds_read_b64_tr_b16 v[244:245], v253 offset:12288\n"
    "v_cvt_pk_bf16_f32 v188, v148, v149\n"
    "v_mfma_f32_16x16x32_bf16 v[164:167], v[232:235], v[28:31], v[164:167]\n"
    "ds_read_b64_tr_b16 v[246:247], v253 offset:14336\n"
    "s_waitcnt lgkmcnt(4)\n"
    "v_mfma_f32_16x16x32_bf16 v[172:175], v[236:239], v[28:31], v[172:175]\n"
    "ds_read_b64_tr_b16 v[232:233], v252 offset:16384\n"
    "v_cvt_pk_bf16_f32 v189, v150, v151\n"
    "v_mfma_f32_16x16x32_bf16 v[168:171], v[236:239], v[24:27], v[168:171]\n"
    "ds_read_b64_tr_b16 v[234:235], v252 offset:18432\n"
    "s_waitcnt lgkmcnt(4)\n"
    "v_mfma_f32_16x16x32_bf16 v[160:163], v[240:243], v[32:35], v[160:163]\n"
    "ds_read_b64_tr_b16 v[236:237], v253 offset:16384\n"
    "v_cvt_pk_bf16_f32 v190, v156, v157\n"
    "v_mfma_f32_16x16x32_bf16 v[164:167], v[240:243], v[36:39], v[164:167]\n"
    "ds_read_b64_tr_b16 v[238:239], v253 offset:18432\n"
    "s_waitcnt lgkmcnt(4)\n"
    "v_mfma_f32_16x16x32_bf16 v[172:175], v[244:247], v[36:39], v[172:175]\n"
    "ds_read_b64_tr_b16 v[240:241], v254 offset:61440\n"
    "v_cvt_pk_bf16_f32 v191, v158, v159\n"
    "v_mfma_f32_16x16x32_bf16 v[168:171], v[244:247], v[32:35], v[168:171]\n"
    "ds_read_b64_tr_b16 v[242:243], v254 offset:63488\n"
    "s_waitcnt lgkmcnt(4)\n"
    "v_mfma_f32_16x16x32_bf16 v[160:163], v[232:235], v[40:43], v[160:163]\n"
    "ds_read_b64_tr_b16 v[244:245], v255 offset:61440\n"
    "v_add_u32 v254, 0x10000, v254\n"
    "v_mfma_f32_16x16x32_bf16 v[164:167], v[232:235], v[44:47], v[164:167]\n"
    "ds_read_b64_tr_b16 v[246:247], v255 offset:63488\n"
    "s_waitcnt lgkmcnt(4)\n"
    "v_mfma_f32_16x16x32_bf16 v[172:175], v[236:239], v[44:47], v[172:175]\n"
    "ds_read_b64_tr_b16 v[232:233], v252 offset:20480\n"
    "v_add_u32 v255, 0x10000, v255\n"
    "v_mfma_f32_16x16x32_bf16 v[168:171], v[236:239], v[40:43], v[168:171]\n"
    "ds_read_b64_tr_b16 v[234:235], v252 offset:22528\n"
    "s_waitcnt lgkmcnt(4)\n"
    "v_mfma_f32_16x16x32_bf16 v[144:147], v[240:243], v[0:3], 0\n"
    "ds_read_b64_tr_b16 v[236:237], v253 offset:20480\n"
    "v_pk_max_i16 v176, v176, 0\n"
    "v_mfma_f32_16x16x32_bf16 v[148:151], v[240:243], v[4:7], 0\n"
    "ds_read_b64_tr_b16 v[238:239], v253 offset:22528\n"
    "s_waitcnt lgkmcnt(4)\n"
    "v_mfma_f32_16x16x32_bf16 v[156:159], v[244:247], v[4:7], 0\n"
    "ds_read_b64_tr_b16 v[240:241], v254 offset:0\n"
    "v_pk_max_i16 v177, v177, 0\n"
    "v_mfma_f32_16x16x32_bf16 v[152:155], v[244:247], v[0:3], 0\n"
    "ds_read_b64_tr_b16 v[242:243], v254 offset:2048\n"
    "s_waitcnt lgkmcnt(4)\n"
    "v_mfma_f32_16x16x32_bf16 v[160:163], v[232:235], v[48:51], v[160:163]\n"
    "ds_read_b64_tr_b16 v[244:245], v255 offset:0\n"
    "v_pk_max_i16 v178, v178, 0\n"
    "v_mfma_f32_16x16x32_bf16 v[164:167], v[232:235], v[52:55], v[164:167]\n"
    "ds_read_b64_tr_b16 v[246:247], v255 offset:2048\n"
    "s_waitcnt lgkmcnt(4)\n"
    "v_mfma_f32_16x16x32_bf16 v[172:175], v[236:239], v[52:55], v[172:175]\n"
    "ds_read_b64_tr_b16 v[232:233], v252 offset:24576\n"
    "v_pk_max_i16 v179, v179, 0\n"
    "v_mfma_f32_16x16x32_bf16 v[168:171], v[236:239], v[48:51], v[168:171]\n"
    "ds_read_b64_tr_b16 v[234:235], v252 offset:26624\n"
    "s_waitcnt lgkmcnt(4)\n"
    "v_mfma_f32_16x16x32_bf16 v[144:147], v[240:243], v[8:11], v[144:147]\n"
    "ds_read_b64_tr_b16 v[236:237], v253 offset:24576\n"
    "v_pk_max_i16 v180, v180, 0\n"
    "v_mfma_f32_16x16x32_bf16 v[148:151], v[240:243], v[12:15], v[148:151]\n"
    "ds_read_b64_tr_b16 v[238:239], v253 offset:26624\n"
    "s_waitcnt lgkmcnt(4)\n"
    "v_mfma_f32_16x16x32_bf16 v[156:159], v[244:247], v[12:15], v[156:159]\n"
    "ds_read_b64_tr_b16 v[240:241], v254 offset:4096\n"
    "v_pk_max_i16 v181, v181, 0\n"
    "v_mfma_f32_16x16x32_bf16 v[152:155], v[244:247], v[8:11], v[152:155]\n"
    "ds_read_b64_tr_b16 v[242:243], v254 offset:6144\n"
    "s_waitcnt lgkmcnt(4)\n"
    "v_mfma_f32_16x16x32_bf16 v[160:163], v[232:235], v[56:59], v[160:163]\n"
    "ds_read_b64_tr_b16 v[244:245], v255 offset:4096\n"
    "v_pk_max_i16 v182, v182, 0\n"
    "v_mfma_f32_16x16x32_bf16 v[164:167], v[232:235], v[60:63], v[164:167]\n"
    "ds_read_b64_tr_b16 v[246:247], v255 offset:6144\n"
    "s_waitcnt lgkmcnt(4)\n"
    "v_mfma_f32_16x16x32_bf16 v[172:175], v[236:239], v[60:63], v[172:175]\n"
    "ds_read_b64_tr_b16 v[232:233], v252 offset:28672\n"
    "v_pk_max_i16 v183, v183, 0\n"
    "v_mfma_f32_16x16x32_bf16 v[168:171], v[236:239], v[56:59], v[168:171]\n"
    "ds_read_b64_tr_b16 v[234:235], v252 offset:30720\n"
    "s_waitcnt lgkmcnt(4)\n"
    "v_mfma_f32_16x16x32_bf16 v[144:147], v[240:243], v[16:19], v[144:147]\n"
    "s_barrier\n"
    "ds_read_b64_tr_b16 v[236:237], v253 offset:28672\n"
    "v_mfma_f32_16x16x32_bf16 v[148:151], v[240:243], v[20:23], v[148:151]\n"
    "ds_read_b64_tr_b16 v[238:239], v253 offset:30720\n"
    "s_waitcnt lgkmcnt(4)\n"
    "v_mfma_f32_16x16x32_bf16 v[156:159], v[244:247], v[20:23], v[156:159]\n"
    "ds_read_b64_tr_b16 v[240:241], v254 offset:8192\n"
    "v_pk_max_i16 v184, v184, 0\n"
    "v_mfma_f32_16x16x32_bf16 v[152:155], v[244:247], v[16:19], v[152:155]\n"
    "ds_read_b64_tr_b16 v[242:243], v254 offset:10240\n"
    "s_waitcnt lgkmcnt(4)\n"
    "v_mfma_f32_16x16x32_bf16 v[160:163], v[232:235], v[64:67], v[160:163]\n"
    "ds_read_b64_tr_b16 v[244:245], v255 offset:8192\n"
    "v_pk_max_i16 v185, v185, 0\n"
    "v_mfma_f32_16x16x32_bf16 v[164:167], v[232:235], v[68:71], v[164:167]\n"
    "ds_read_b64_tr_b16 v[246:247], v255 offset:10240\n"
    "s_waitcnt lgkmcnt(4)\n"
    "v_mfma_f32_16x16x32_bf16 v[172:175], v[236:239], v[68:71], v[172:175]\n"
    "ds_read_b64_tr_b16 v[232:233], v252 offset:32768\n"
    "v_pk_max_i16 v186, v186, 0\n"
    "v_mfma_f32_16x16x32_bf16 v[168:171], v[236:239], v[64:67], v[168:171]\n"
    "ds_read_b64_tr_b16 v[234:235], v252 offset:34816\n"
    "s_waitcnt lgkmcnt(4)\n"
    "v_mfma_f32_16x16x32_bf16 v[144:147], v[240:243], v[24:27], v[144:147]\n"
    "ds_read_b64_tr_b16 v[236:237], v253 offset:32768\n"
    "v_pk_max_i16 v187, v187, 0\n"
    "v_mfma_f32_16x16x32_bf16 v[148:151], v[240:243], v[28:31], v[148:151]\n"
    "ds_read_b64_tr_b16 v[238:239], v253 offset:34816\n"
    "s_waitcnt lgkmcnt(4)\n"
    "v_mfma_f32_16x16x32_bf16 v[156:159], v[244:247], v[28:31], v[156:159]\n"
    "ds_read_b64_tr_b16 v[240:241], v254 offset:12288\n"
    "v_pk_max_i16 v188, v188, 0\n"
    "v_mfma_f32_16x16x32_bf16 v[152:155], v[244:247], v[24:27], v[152:155]\n"
    "ds_read_b64_tr_b16 v[242:243], v254 offset:14336\n"
    "s_waitcnt lgkmcnt(4)\n"
    "v_mfma_f32_16x16x32_bf16 v[160:163], v[232:235], v[72:75], v[160:163]\n"
    "ds_read_b64_tr_b16 v[244:245], v255 offset:12288\n"
    "v_pk_max_i16 v189, v189, 0\n"
    "v_mfma_f32_16x16x32_bf16 v[164:167], v[232:235], v[76:79], v[164:167]\n"
    "ds_read_b64_tr_b16 v[246:247], v255 offset:14336\n"
    "s_waitcnt lgkmcnt(4)\n"
    "v_mfma_f32_16x16x32_bf16 v[172:175], v[236:239], v[76:79], v[172:175]\n"
    "ds_read_b64_tr_b16 v[232:233], v252 offset:36864\n"
    "v_pk_max_i16 v190, v190, 0\n"
    "v_mfma_f32_16x16x32_bf16 v[168:171], v[236:239], v[72:75], v[168:171]\n"
    "ds_read_b64_tr_b16 v[234:235], v252 offset:38912\n"
    "s_waitcnt lgkmcnt(4)\n"
    "v_mfma_f32_16x16x32_bf16 v[144:147], v[240:243], v[32:35], v[144:147]\n"
    "ds_read_b64_tr_b16 v[236:237], v253 offset:36864\n"
    "v_pk_max_i16 v191, v191, 0\n"
    "v_mfma_f32_16x16x32_bf16 v[148:151], v[240:243], v[36:39], v[148:151]\n"
    "ds_read_b64_tr_b16 v[238:239], v253 offset:38912\n"
    "s_waitcnt lgkmcnt(4)\n"
    "v_mfma_f32_16x16x32_bf16 v[156:159], v[244:247], v[36:39], v[156:159]\n"
    "ds_read_b64_tr_b16 v[240:241], v254 offset:16384\n"
    "v_mfma_f32_16x16x32_bf16 v[152:155], v[244:247], v[32:35], v[152:155]\n"
    "ds_read_b64_tr_b16 v[242:243], v254 offset:18432\n"
    "s_waitcnt lgkmcnt(4)\n"
    "v_mfma_f32_16x16x32_bf16 v[160:163], v[232:235], v[80:83], v[160:163]\n"
    "ds_read_b64_tr_b16 v[244:245], v255 offset:16384\n"
    "v_mfma_f32_16x16x32_bf16 v[164:167], v[232:235], v[84:87], v[164:167]\n"
    "ds_read_b64_tr_b16 v[246:247], v255 offset:18432\n"
    "s_waitcnt lgkmcnt(4)\n"
    "v_mfma_f32_16x16x32_bf16 v[172:175], v[236:239], v[84:87], v[172:175]\n"
    "ds_read_b64_tr_b16 v[232:233], v252 offset:40960\n"
    "v_mfma_f32_16x16x32_bf16 v[168:171], v[236:239], v[80:83], v[168:171]\n"
    "ds_read_b64_tr_b16 v[234:235], v252 offset:43008\n"
    "s_waitcnt lgkmcnt(4)\n"
    "v_mfma_f32_16x16x32_bf16 v[144:147], v[240:243], v[40:43], v[144:147]\n"
    "ds_read_b64_tr_b16 v[236:237], v253 offset:40960\n"
    "v_mfma_f32_16x16x32_bf16 v[148:151], v[240:243], v[44:47], v[148:151]\n"
    "ds_read_b64_tr_b16 v[238:239], v253 offset:43008\n"
    "s_waitcnt lgkmcnt(4)\n"
    "v_mfma_f32_16x16x32_bf16 v[156:159], v[244:247], v[44:47], v[156:159]\n"
    "ds_read_b64_tr_b16 v[240:241], v254 offset:20480\n"
    "buffer_store_dwordx4 v[176:179], v250, s[60:63], s72 offen offset:0 sc1\n"
    "v_mfma_f32_16x16x32_bf16 v[152:155], v[244:247], v[40:43], v[152:155]\n"
    "ds_read_b64_tr_b16 v[242:243], v254 offset:22528\n"
    "s_waitcnt lgkmcnt(4)\n"
    "v_mfma_f32_16x16x32_bf16 v[160:163], v[232:235], v[88:91], v[160:163]\n"
    "ds_read_b64_tr_b16 v[244:245], v255 offset:20480\n"
    "buffer_store_dwordx4 v[180:183], v250, s[60:63], s72 offen offset:1024 sc1\n"
    "v_mfma_f32_16x16x32_bf16 v[164:167], v[232:235], v[92:95], v[164:167]\n"
    "ds_read_b64_tr_b16 v[246:247], v255 offset:22528\n"
    "s_waitcnt lgkmcnt(4)\n"
    "v_mfma_f32_16x16x32_bf16 v[172:175], v[236:239], v[92:95], v[172:175]\n"
    "ds_read_b64_tr_b16 v[232:233], v252 offset:45056\n"
    "buffer_store_dwordx4 v[184:187], v250, s[60:63], s72 offen offset:2048 sc1\n"
    "v_mfma_f32_16x16x32_bf16 v[168:171], v[236:239], v[88:91], v[168:171]\n"
    "ds_read_b64_tr_b16 v[234:235], v252 offset:47104\n"
    "s_waitcnt lgkmcnt(4)\n"
    "v_mfma_f32_16x16x32_bf16 v[144:147], v[240:243], v[48:51], v[144:147]\n"
    "ds_read_b64_tr_b16 v[236:237], v253 offset:45056\n"
    "buffer_store_dwordx4 v[188:191], v250, s[60:63], s72 offen offset:3072 sc1\n"
    "v_mfma_f32_16x16x32_bf16 v[148:151], v[240:243], v[52:55], v[148:151]\n"
    "ds_read_b64_tr_b16 v[238:239], v253 offset:47104\n"
    "s_waitcnt lgkmcnt(4)\n"
    "v_mfma_f32_16x16x32_bf16 v[156:159], v[244:247], v[52:55], v[156:159]\n"
    "ds_read_b64_tr_b16 v[240:241], v254 offset:24576\n"
    "v_mfma_f32_16x16x32_bf16 v[152:155], v[244:247], v[48:51], v[152:155]\n"
    "ds_read_b64_tr_b16 v[242:243], v254 offset:26624\n"
    "s_waitcnt lgkmcnt(4)\n"
    "v_mfma_f32_16x16x32_bf16 v[160:163], v[232:235], v[96:99], v[160:163]\n"
    "s_barrier\n"
    "ds_read_b64_tr_b16 v[244:245], v255 offset:24576\n"
    "v_mfma_f32_16x16x32_bf16 v[164:167], v[232:235], v[100:103], v[164:167]\n"
    "ds_read_b64_tr_b16 v[246:247], v255 offset:26624\n"
    "s_waitcnt lgkmcnt(4)\n"
    "v_mfma_f32_16x16x32_bf16 v[172:175], v[236:239], v[100:103], v[172:175]\n"
    "ds_read_b64_tr_b16 v[232:233], v252 offset:49152\n"
    "v_mfma_f32_16x16x32_bf16 v[168:171], v[236:239], v[96:99], v[168:171]\n"
    "ds_read_b64_tr_b16 v[234:235], v252 offset:51200\n"
    "s_waitcnt lgkmcnt(4)\n"
    "v_mfma_f32_16x16x32_bf16 v[144:147], v[240:243], v[56:59], v[144:147]\n"
    "ds_read_b64_tr_b16 v[236:237], v253 offset:49152\n"
    "v_mfma_f32_16x16x32_bf16 v[148:151], v[240:243], v[60:63], v[148:151]\n"
    "ds_read_b64_tr_b16 v[238:239], v253 offset:51200\n"
    "s_waitcnt lgkmcnt(4)\n"
    "v_mfma_f32_16x16x32_bf16 v[156:159], v[244:247], v[60:63], v[156:159]\n"
    "ds_read_b64_tr_b16 v[240:241], v254 offset:28672\n"
    "v_mfma_f32_16x16x32_bf16 v[152:155], v[244:247], v[56:59], v[152:155]\n"
    "ds_read_b64_tr_b16 v[242:243], v254 offset:30720\n"
    "s_waitcnt lgkmcnt(4)\n"
    "v_mfma_f32_16x16x32_bf16 v[160:163], v[232:235], v[104:107], v[160:163]\n"
    "ds_read_b64_tr_b16 v[244:245], v255 offset:28672\n"
    "v_mfma_f32_16x16x32_bf16 v[164:167], v[232:235], v[108:111], v[164:167]\n"
    "ds_read_b64_tr_b16 v[246:247], v255 offset:30720\n"
    "s_waitcnt lgkmcnt(4)\n"
    "v_mfma_f32_16x16x32_bf16 v[172:175], v[236:239], v[108:111], v[172:175]\n"
    "ds_read_b64_tr_b16 v[232:233], v252 offset:53248\n"
    "v_mfma_f32_16x16x32_bf16 v[168:171], v[236:239], v[104:107], v[168:171]\n"
    "ds_read_b64_tr_b16 v[234:235], v252 offset:55296\n"
    "s_waitcnt lgkmcnt(4)\n"
    "v_mfma_f32_16x16x32_bf16 v[144:147], v[240:243], v[64:67], v[144:147]\n"
    "ds_read_b64_tr_b16 v[236:237], v253 offset:53248\n"
    "v_mfma_f32_16x16x32_bf16 v[148:151], v[240:243], v[68:71], v[148:151]\n"
    "ds_read_b64_tr_b16 v[238:239], v253 offset:55296\n"
    "s_waitcnt lgkmcnt(4)\n"
    "v_mfma_f32_16x16x32_bf16 v[156:159], v[244:247], v[68:71], v[156:159]\n"
    "ds_read_b64_tr_b16 v[240:241], v254 offset:32768\n"
    "v_mfma_f32_16x16x32_bf16 v[152:155], v[244:247], v[64:67], v[152:155]\n"
    "ds_read_b64_tr_b16 v[242:243], v254 offset:34816\n"
    "s_waitcnt lgkmcnt(4)\n"
    "v_mfma_f32_16x16x32_bf16 v[160:163], v[232:235], v[112:115], v[160:163]\n"
    "ds_read_b64_tr_b16 v[244:245], v255 offset:32768\n"
    "v_mfma_f32_16x16x32_bf16 v[164:167], v[232:235], v[116:119], v[164:167]\n"
    "ds_read_b64_tr_b16 v[246:247], v255 offset:34816\n"
    "s_waitcnt lgkmcnt(4)\n"
    "v_mfma_f32_16x16x32_bf16 v[172:175], v[236:239], v[116:119], v[172:175]\n"
    "ds_read_b64_tr_b16 v[232:233], v252 offset:57344\n"
    "v_mfma_f32_16x16x32_bf16 v[168:171], v[236:239], v[112:115], v[168:171]\n"
    "ds_read_b64_tr_b16 v[234:235], v252 offset:59392\n"
    "s_waitcnt lgkmcnt(4)\n"
    "v_mfma_f32_16x16x32_bf16 v[144:147], v[240:243], v[72:75], v[144:147]\n"
    "ds_read_b64_tr_b16 v[236:237], v253 offset:57344\n"
    "v_mfma_f32_16x16x32_bf16 v[148:151], v[240:243], v[76:79], v[148:151]\n"
    "ds_read_b64_tr_b16 v[238:239], v253 offset:59392\n"
    "s_waitcnt lgkmcnt(4)\n"
    "v_mfma_f32_16x16x32_bf16 v[156:159], v[244:247], v[76:79], v[156:159]\n"
    "ds_read_b64_tr_b16 v[240:241], v254 offset:36864\n"
    "v_mfma_f32_16x16x32_bf16 v[152:155], v[244:247], v[72:75], v[152:155]\n"
    "ds_read_b64_tr_b16 v[242:243], v254 offset:38912\n"
    "s_waitcnt lgkmcnt(4)\n"
    "v_mfma_f32_16x16x32_bf16 v[160:163], v[232:235], v[120:123], v[160:163]\n"
    "ds_read_b64_tr_b16 v[244:245], v255 offset:36864\n"
    "v_mfma_f32_16x16x32_bf16 v[164:167], v[232:235], v[124:127], v[164:167]\n"
    "ds_read_b64_tr_b16 v[246:247], v255 offset:38912\n"
    "s_waitcnt lgkmcnt(4)\n"
    "v_mfma_f32_16x16x32_bf16 v[172:175], v[236:239], v[124:127], v[172:175]\n"
    "ds_read_b64_tr_b16 v[232:233], v252 offset:61440\n"
    "v_mfma_f32_16x16x32_bf16 v[168:171], v[236:239], v[120:123], v[168:171]\n"
    "ds_read_b64_tr_b16 v[234:235], v252 offset:63488\n"
    "s_waitcnt lgkmcnt(4)\n"
    "v_mfma_f32_16x16x32_bf16 v[144:147], v[240:243], v[80:83], v[144:147]\n"
    "ds_read_b64_tr_b16 v[236:237], v253 offset:61440\n"
    "v_add_u32 v252, 0x8000, v252\n"
    "v_mfma_f32_16x16x32_bf16 v[148:151], v[240:243], v[84:87], v[148:151]\n"
    "ds_read_b64_tr_b16 v[238:239], v253 offset:63488\n"
    "s_waitcnt lgkmcnt(4)\n"
    "v_mfma_f32_16x16x32_bf16 v[156:159], v[244:247], v[84:87], v[156:159]\n"
    "ds_read_b64_tr_b16 v[240:241], v254 offset:40960\n"
    "v_add_u32 v253, 0x8000, v253\n"
    "v_mfma_f32_16x16x32_bf16 v[152:155], v[244:247], v[80:83], v[152:155]\n"
    "ds_read_b64_tr_b16 v[242:243], v254 offset:43008\n"
    "s_waitcnt lgkmcnt(4)\n"
    "v_mfma_f32_16x16x32_bf16 v[160:163], v[232:235], v[128:131], v[160:163]\n"
    "ds_read_b64_tr_b16 v[244:245], v255 offset:40960\n"
    "v_mfma_f32_16x16x32_bf16 v[164:167], v[232:235], v[132:135], v[164:167]\n"
    "ds_read_b64_tr_b16 v[246:247], v255 offset:43008\n"
    "s_waitcnt lgkmcnt(4)\n"
    "v_mfma_f32_16x16x32_bf16 v[172:175], v[236:239], v[132:135], v[172:175]\n"
    "ds_read_b64_tr_b16 v[232:233], v252 offset:32768\n"
    "v_mfma_f32_16x16x32_bf16 v[168:171], v[236:239], v[128:131], v[168:171]\n"
    "ds_read_b64_tr_b16 v[234:235], v252 offset:34816\n"
    "s_waitcnt lgkmcnt(4)\n"
    "v_mfma_f32_16x16x32_bf16 v[144:147], v[240:243], v[88:91], v[144:147]\n"
    "s_barrier\n"
    "ds_read_b64_tr_b16 v[236:237], v253 offset:32768\n"
    "v_mfma_f32_16x16x32_bf16 v[148:151], v[240:243], v[92:95], v[148:151]\n"
    "ds_read_b64_tr_b16 v[238:239], v253 offset:34816\n"
    "s_waitcnt lgkmcnt(4)\n"
    "v_mfma_f32_16x16x32_bf16 v[156:159], v[244:247], v[92:95], v[156:159]\n"
    "ds_read_b64_tr_b16 v[240:241], v254 offset:45056\n"
    "v_mfma_f32_16x16x32_bf16 v[152:155], v[244:247], v[88:91], v[152:155]\n"
    "ds_read_b64_tr_b16 v[242:243], v254 offset:47104\n"
    "s_waitcnt lgkmcnt(4)\n"
    "v_mfma_f32_16x16x32_bf16 v[160:163], v[232:235], v[136:139], v[160:163]\n"
    "ds_read_b64_tr_b16 v[244:245], v255 offset:45056\n"
    "v_mfma_f32_16x16x32_bf16 v[164:167], v[232:235], v[140:143], v[164:167]\n"
    "ds_read_b64_tr_b16 v[246:247], v255 offset:47104\n"
    "s_waitcnt lgkmcnt(4)\n"
    "v_mfma_f32_16x16x32_bf16 v[172:175], v[236:239], v[140:143], v[172:175]\n"
    "ds_read_b64_tr_b16 v[232:233], v254 offset:49152\n"
    "v_mfma_f32_16x16x32_bf16 v[168:171], v[236:239], v[136:139], v[168:171]\n"
    "ds_read_b64_tr_b16 v[234:235], v254 offset:51200\n"
    "s_waitcnt lgkmcnt(4)\n"
    "v_mfma_f32_16x16x32_bf16 v[144:147], v[240:243], v[96:99], v[144:147]\n"
    "ds_read_b64_tr_b16 v[236:237], v255 offset:49152\n"
    "v_mfma_f32_16x16x32_bf16 v[148:151], v[240:243], v[100:103], v[148:151]\n"
    "ds_read_b64_tr_b16 v[238:239], v255 offset:51200\n"
    "s_waitcnt lgkmcnt(4)\n"
    "v_mfma_f32_16x16x32_bf16 v[156:159], v[244:247], v[100:103], v[156:159]\n"
    "ds_read_b64_tr_b16 v[240:241], v254 offset:53248\n"
    "v_cvt_pk_bf16_f32 v192, v160, v161\n"
    "v_mfma_f32_16x16x32_bf16 v[152:155], v[244:247], v[96:99], v[152:155]\n"
    "ds_read_b64_tr_b16 v[242:243], v254 offset:55296\n"
    "s_waitcnt lgkmcnt(4)\n"
    "v_mfma_f32_16x16x32_bf16 v[144:147], v[232:235], v[104:107], v[144:147]\n"
    "ds_read_b64_tr_b16 v[244:245], v255 offset:53248\n"
    "v_cvt_pk_bf16_f32 v193, v162, v163\n"
    "v_mfma_f32_16x16x32_bf16 v[148:151], v[232:235], v[108:111], v[148:151]\n"
    "ds_read_b64_tr_b16 v[246:247], v255 offset:55296\n"
    "s_waitcnt lgkmcnt(4)\n"
    "v_mfma_f32_16x16x32_bf16 v[156:159], v[236:239], v[108:111], v[156:159]\n"
    "ds_read_b64_tr_b16 v[232:233], v254 offset:57344\n"
    "v_cvt_pk_bf16_f32 v194, v168, v169\n"
    "v_mfma_f32_16x16x32_bf16 v[152:155], v[236:239], v[104:107], v[152:155]\n"
    "ds_read_b64_tr_b16 v[234:235], v254 offset:59392\n"
    "s_waitcnt lgkmcnt(4)\n"
    "v_mfma_f32_16x16x32_bf16 v[144:147], v[240:243], v[112:115], v[144:147]\n"
    "ds_read_b64_tr_b16 v[236:237], v255 offset:57344\n"
    "v_cvt_pk_bf16_f32 v195, v170, v171\n"
    "v_mfma_f32_16x16x32_bf16 v[148:151], v[240:243], v[116:119], v[148:151]\n"
    "ds_read_b64_tr_b16 v[238:239], v255 offset:59392\n"
    "s_waitcnt lgkmcnt(4)\n"
    "v_mfma_f32_16x16x32_bf16 v[156:159], v[244:247], v[116:119], v[156:159]\n"
    "ds_read_b64_tr_b16 v[240:241], v254 offset:61440\n"
    "v_cvt_pk_bf16_f32 v196, v164, v165\n"
    "v_mfma_f32_16x16x32_bf16 v[152:155], v[244:247], v[112:115], v[152:155]\n"
    "ds_read_b64_tr_b16 v[242:243], v254 offset:63488\n"
    "s_waitcnt lgkmcnt(4)\n"
    "v_mfma_f32_16x16x32_bf16 v[144:147], v[232:235], v[120:123], v[144:147]\n"
    "ds_read_b64_tr_b16 v[244:245], v255 offset:61440\n"
    "v_add_u32 v254, 0x8000, v254\n"
    "v_mfma_f32_16x16x32_bf16 v[148:151], v[232:235], v[124:127], v[148:151]\n"
    "ds_read_b64_tr_b16 v[246:247], v255 offset:63488\n"
    "s_waitcnt lgkmcnt(4)\n"
    "v_mfma_f32_16x16x32_bf16 v[156:159], v[236:239], v[124:127], v[156:159]\n"
    "ds_read_b64_tr_b16 v[232:233], v254 offset:32768\n"
    "v_add_u32 v255, 0x8000, v255\n"
    "v_mfma_f32_16x16x32_bf16 v[152:155], v[236:239], v[120:123], v[152:155]\n"
    "ds_read_b64_tr_b16 v[234:235], v254 offset:34816\n"
    "s_waitcnt lgkmcnt(4)\n"
    "v_mfma_f32_16x16x32_bf16 v[144:147], v[240:243], v[128:131], v[144:147]\n"
    "ds_read_b64_tr_b16 v[236:237], v255 offset:32768\n"
    "v_add_u32 v252, 0xfffe8000, v252\n"
    "v_mfma_f32_16x16x32_bf16 v[148:151], v[240:243], v[132:135], v[148:151]\n"
    "ds_read_b64_tr_b16 v[238:239], v255 offset:34816\n"
    "s_waitcnt lgkmcnt(4)\n"
    "v_mfma_f32_16x16x32_bf16 v[156:159], v[244:247], v[132:135], v[156:159]\n"
    "ds_read_b64_tr_b16 v[240:241], v252 offset:0\n"
    "v_add_u32 v253, 0xfffe8000, v253\n"
    "v_mfma_f32_16x16x32_bf16 v[152:155], v[244:247], v[128:131], v[152:155]\n"
    "ds_read_b64_tr_b16 v[242:243], v252 offset:2048\n"
    "s_waitcnt lgkmcnt(4)\n"
    "v_mfma_f32_16x16x32_bf16 v[144:147], v[232:235], v[136:139], v[144:147]\n"
    "ds_read_b64_tr_b16 v[244:245], v253 offset:0\n"
    "v_cvt_pk_bf16_f32 v197, v166, v167\n"
    "v_mfma_f32_16x16x32_bf16 v[148:151], v[232:235], v[140:143], v[148:151]\n"
    "ds_read_b64_tr_b16 v[246:247], v253 offset:2048\n"
    "s_waitcnt lgkmcnt(4)\n"
    "v_mfma_f32_16x16x32_bf16 v[156:159], v[236:239], v[140:143], v[156:159]\n"
    "ds_read_b64_tr_b16 v[232:233], v252 offset:4096\n"
    "v_cvt_pk_bf16_f32 v198, v172, v173\n"
    "v_mfma_f32_16x16x32_bf16 v[152:155], v[236:239], v[136:139], v[152:155]\n"
    "ds_read_b64_tr_b16 v[234:235], v252 offset:6144\n"
    "v_cvt_pk_bf16_f32 v199, v174, v175\n"
    "s_waitcnt lgkmcnt(4)\n"
    "v_mfma_f32_16x16x32_bf16 v[160:163], v[240:243], v[0:3], 0\n"
    "ds_read_b64_tr_b16 v[236:237], v253 offset:4096\n"
    "v_mfma_f32_16x16x32_bf16 v[164:167], v[240:243], v[4:7], 0\n"
    "ds_read_b64_tr_b16 v[238:239], v253 offset:6144\n"
    "s_waitcnt lgkmcnt(4)\n"
    "v_mfma_f32_16x16x32_bf16 v[172:175], v[244:247], v[4:7], 0\n"
    "ds_read_b64_tr_b16 v[240:241], v252 offset:8192\n"
    "v_cvt_pk_bf16_f32 v200, v144, v145\n"
    "v_mfma_f32_16x16x32_bf16 v[168:171], v[244:247], v[0:3], 0\n"
    "ds_read_b64_tr_b16 v[242:243], v252 offset:10240\n"
    "s_waitcnt lgkmcnt(4)\n"
    "v_mfma_f32_16x16x32_bf16 v[160:163], v[232:235], v[8:11], v[160:163]\n"
    "s_barrier\n"
    "ds_read_b64_tr_b16 v[244:245], v253 offset:8192\n"
    "v_mfma_f32_16x16x32_bf16 v[164:167], v[232:235], v[12:15], v[164:167]\n"
    "ds_read_b64_tr_b16 v[246:247], v253 offset:10240\n"
    "s_waitcnt lgkmcnt(4)\n"
    "v_mfma_f32_16x16x32_bf16 v[172:175], v[236:239], v[12:15], v[172:175]\n"
    "ds_read_b64_tr_b16 v[232:233], v252 offset:12288\n"
    "v_cvt_pk_bf16_f32 v201, v146, v147\n"
    "v_mfma_f32_16x16x32_bf16 v[168:171], v[236:239], v[8:11], v[168:171]\n"
    "ds_read_b64_tr_b16 v[234:235], v252 offset:14336\n"
    "s_waitcnt lgkmcnt(4)\n"
    "v_mfma_f32_16x16x32_bf16 v[160:163], v[240:243], v[16:19], v[160:163]\n"
    "ds_read_b64_tr_b16 v[236:237], v253 offset:12288\n"
    "v_cvt_pk_bf16_f32 v202, v152, v153\n"
    "v_mfma_f32_16x16x32_bf16 v[164:167], v[240:243], v[20:23], v[164:167]\n"
    "ds_read_b64_tr_b16 v[238:239], v253 offset:14336\n"
    "s_waitcnt lgkmcnt(4)\n"
    "v_mfma_f32_16x16x32_bf16 v[172:175], v[244:247], v[20:23], v[172:175]\n"
    "ds_read_b64_tr_b16 v[240:241], v252 offset:16384\n"
    "v_cvt_pk_bf16_f32 v203, v154, v155\n"
    "v_mfma_f32_16x16x32_bf16 v[168:171], v[244:247], v[16:19], v[168:171]\n"
    "ds_read_b64_tr_b16 v[242:243], v252 offset:18432\n"
    "s_waitcnt lgkmcnt(4)\n"
    "v_mfma_f32_16x16x32_bf16 v[160:163], v[232:235], v[24:27], v[160:163]\n"
    "ds_read_b64_tr_b16 v[244:245], v253 offset:16384\n"
    "v_add_u32 v254, 0xfffe8000, v254\n"
    "v_mfma_f32_16x16x32_bf16 v[164:167], v[232:235], v[28:31], v[164:167]\n"
    "ds_read_b64_tr_b16 v[246:247], v253 offset:18432\n"
    "s_waitcnt lgkmcnt(4)\n"
    "v_mfma_f32_16x16x32_bf16 v[172:175], v[236:239], v[28:31], v[172:175]\n"
    "ds_read_b64_tr_b16 v[232:233], v252 offset:20480\n"
    "v_add_u32 v255, 0xfffe8000, v255\n"
    "v_mfma_f32_16x16x32_bf16 v[168:171], v[236:239], v[24:27], v[168:171]\n"
    "ds_read_b64_tr_b16 v[234:235], v252 offset:22528\n"
    "s_waitcnt lgkmcnt(4)\n"
    "v_mfma_f32_16x16x32_bf16 v[160:163], v[240:243], v[32:35], v[160:163]\n"
    "ds_read_b64_tr_b16 v[236:237], v253 offset:20480\n"
    "v_cvt_pk_bf16_f32 v204, v148, v149\n"
    "v_mfma_f32_16x16x32_bf16 v[164:167], v[240:243], v[36:39], v[164:167]\n"
    "ds_read_b64_tr_b16 v[238:239], v253 offset:22528\n"
    "s_waitcnt lgkmcnt(4)\n"
    "v_mfma_f32_16x16x32_bf16 v[172:175], v[244:247], v[36:39], v[172:175]\n"
    "ds_read_b64_tr_b16 v[240:241], v254 offset:0\n"
    "v_cvt_pk_bf16_f32 v205, v150, v151\n"
    "v_mfma_f32_16x16x32_bf16 v[168:171], v[244:247], v[32:35], v[168:171]\n"
    "ds_read_b64_tr_b16 v[242:243], v254 offset:2048\n"
    "s_waitcnt lgkmcnt(4)\n"
    "v_mfma_f32_16x16x32_bf16 v[160:163], v[232:235], v[40:43], v[160:163]\n"
    "ds_read_b64_tr_b16 v[244:245], v255 offset:0\n"
    "v_cvt_pk_bf16_f32 v206, v156, v157\n"
    "v_mfma_f32_16x16x32_bf16 v[164:167], v[232:235], v[44:47], v[164:167]\n"
    "ds_read_b64_tr_b16 v[246:247], v255 offset:2048\n"
    "s_waitcnt lgkmcnt(4)\n"
    "v_mfma_f32_16x16x32_bf16 v[172:175], v[236:239], v[44:47], v[172:175]\n"
    "L_st4_%=:\n"
    "s_cbranch_execz L_bk%=\n"
    "ds_read_b64_tr_b16 v[232:233], v252 offset:24576\n"
    "v_cvt_pk_bf16_f32 v207, v158, v159\n"
    "v_mfma_f32_16x16x32_bf16 v[168:171], v[236:239], v[40:43], v[168:171]\n"
    "ds_read_b64_tr_b16 v[234:235], v252 offset:26624\n"
    "s_waitcnt lgkmcnt(4)\n"
    "v_mfma_f32_16x16x32_bf16 v[144:147], v[240:243], v[0:3], 0\n"
    "ds_read_b64_tr_b16 v[236:237], v253 offset:24576\n"
    "v_pk_max_i16 v192, v192, 0\n"
    "v_mfma_f32_16x16x32_bf16 v[148:151], v[240:243], v[4:7], 0\n"
    "ds_read_b64_tr_b16 v[238:239], v253 offset:26624\n"
    "s_waitcnt lgkmcnt(4)\n"
    "v_mfma_f32_16x16x32_bf16 v[156:159], v[244:247], v[4:7], 0\n"
    "ds_read_b64_tr_b16 v[240:241], v254 offset:4096\n"
    "v_pk_max_i16 v193, v193, 0\n"
    "v_mfma_f32_16x16x32_bf16 v[152:155], v[244:247], v[0:3], 0\n"
    "ds_read_b64_tr_b16 v[242:243], v254 offset:6144\n"
    "s_waitcnt lgkmcnt(4)\n"
    "v_mfma_f32_16x16x32_bf16 v[160:163], v[232:235], v[48:51], v[160:163]\n"
    "ds_read_b64_tr_b16 v[244:245], v255 offset:4096\n"
    "v_pk_max_i16 v194, v194, 0\n"
    "v_mfma_f32_16x16x32_bf16 v[164:167], v[232:235], v[52:55], v[164:167]\n"
    "ds_read_b64_tr_b16 v[246:247], v255 offset:6144\n"
    "s_waitcnt lgkmcnt(4)\n"
    "v_mfma_f32_16x16x32_bf16 v[172:175], v[236:239], v[52:55], v[172:175]\n"
    "ds_read_b64_tr_b16 v[232:233], v252 offset:28672\n"
    "v_pk_max_i16 v195, v195, 0\n"
    "v_mfma_f32_16x16x32_bf16 v[168:171], v[236:239], v[48:51], v[168:171]\n"
    "ds_read_b64_tr_b16 v[234:235], v252 offset:30720\n"
    "s_waitcnt lgkmcnt(4)\n"
    "v_mfma_f32_16x16x32_bf16 v[144:147], v[240:243], v[8:11], v[144:147]\n"
    "ds_read_b64_tr_b16 v[236:237], v253 offset:28672\n"
    "v_pk_max_i16 v196, v196, 0\n"
    "v_mfma_f32_16x16x32_bf16 v[148:151], v[240:243], v[12:15], v[148:151]\n"
    "ds_read_b64_tr_b16 v[238:239], v253 offset:30720\n"
    "s_waitcnt lgkmcnt(4)\n"
    "v_mfma_f32_16x16x32_bf16 v[156:159], v[244:247], v[12:15], v[156:159]\n"
    "ds_read_b64_tr_b16 v[240:241], v254 offset:8192\n"
    "v_pk_max_i16 v197, v197, 0\n"
    "v_mfma_f32_16x16x32_bf16 v[152:155], v[244:247], v[8:11], v[152:155]\n"
    "ds_read_b64_tr_b16 v[242:243], v254 offset:10240\n"
    "s_waitcnt lgkmcnt(4)\n"
    "v_mfma_f32_16x16x32_bf16 v[160:163], v[232:235], v[56:59], v[160:163]\n"
    "ds_read_b64_tr_b16 v[244:245], v255 offset:8192\n"
    "buffer_store_dwordx4 v[228:231], v250, s[60:63], s75 offen offset:0 sc1\n"
    "v_mfma_f32_16x16x32_bf16 v[164:167], v[232:235], v[60:63], v[164:167]\n"
    "ds_read_b64_tr_b16 v[246:247], v255 offset:10240\n"
    "s_waitcnt lgkmcnt(4)\n"
    "v_mfma_f32_16x16x32_bf16 v[172:175], v[236:239], v[60:63], v[172:175]\n"
    "ds_read_b64_tr_b16 v[232:233], v252 offset:32768\n"
    "v_pk_max_i16 v198, v198, 0\n"
    "v_mfma_f32_16x16x32_bf16 v[168:171], v[236:239], v[56:59], v[168:171]\n"
    "ds_read_b64_tr_b16 v[234:235], v252 offset:34816\n"
    "s_waitcnt lgkmcnt(4)\n"
    "v_mfma_f32_16x16x32_bf16 v[144:147], v[240:243], v[16:19], v[144:147]\n"
    "s_barrier\n"
    "ds_read_b64_tr_b16 v[236:237], v253 offset:32768\n"
    "v_mfma_f32_16x16x32_bf16 v[148:151], v[240:243], v[20:23], v[148:151]\n"
    "ds_read_b64_tr_b16 v[238:239], v253 offset:34816\n"
    "s_waitcnt lgkmcnt(4)\n"
    "v_mfma_f32_16x16x32_bf16 v[156:159], v[244:247], v[20:23], v[156:159]\n"
    "ds_read_b64_tr_b16 v[240:241], v254 offset:12288\n"
    "v_pk_max_i16 v199, v199, 0\n"
    "v_mfma_f32_16x16x32_bf16 v[152:155], v[244:247], v[16:19], v[152:155]\n"
    "ds_read_b64_tr_b16 v[242:243], v254 offset:14336\n"
    "s_waitcnt lgkmcnt(4)\n"
    "v_mfma_f32_16x16x32_bf16 v[160:163], v[232:235], v[64:67], v[160:163]\n"
    "ds_read_b64_tr_b16 v[244:245], v255 offset:12288\n"
    "v_pk_max_i16 v200, v200, 0\n"
    "v_mfma_f32_16x16x32_bf16 v[164:167], v[232:235], v[68:71], v[164:167]\n"
    "ds_read_b64_tr_b16 v[246:247], v255 offset:14336\n"
    "s_waitcnt lgkmcnt(4)\n"
    "v_mfma_f32_16x16x32_bf16 v[172:175], v[236:239], v[68:71], v[172:175]\n"
    "ds_read_b64_tr_b16 v[232:233], v252 offset:36864\n"
    "v_pk_max_i16 v201, v201, 0\n"
    "v_mfma_f32_16x16x32_bf16 v[168:171], v[236:239], v[64:67], v[168:171]\n"
    "ds_read_b64_tr_b16 v[234:235], v252 offset:38912\n"
    "s_waitcnt lgkmcnt(4)\n"
    "v_mfma_f32_16x16x32_bf16 v[144:147], v[240:243], v[24:27], v[144:147]\n"
    "ds_read_b64_tr_b16 v[236:237], v253 offset:36864\n"
    "v_pk_max_i16 v202, v202, 0\n"
    "v_mfma_f32_16x16x32_bf16 v[148:151], v[240:243], v[28:31], v[148:151]\n"
    "ds_read_b64_tr_b16 v[238:239], v253 offset:38912\n"
    "s_waitcnt lgkmcnt(4)\n"
    "v_mfma_f32_16x16x32_bf16 v[156:159], v[244:247], v[28:31], v[156:159]\n"
    "ds_read_b64_tr_b16 v[240:241], v254 offset:16384\n"
    "v_pk_max_i16 v203, v203, 0\n"
    "v_mfma_f32_16x16x32_bf16 v[152:155], v[244:247], v[24:27], v[152:155]\n"
    "ds_read_b64_tr_b16 v[242:243], v254 offset:18432\n"
    "s_waitcnt lgkmcnt(4)\n"
    "v_mfma_f32_16x16x32_bf16 v[160:163], v[232:235], v[72:75], v[160:163]\n"
    "ds_read_b64_tr_b16 v[244:245], v255 offset:16384\n"
    "v_pk_max_i16 v204, v204, 0\n"
    "v_mfma_f32_16x16x32_bf16 v[164:167], v[232:235], v[76:79], v[164:167]\n"
    "ds_read_b64_tr_b16 v[246:247], v255 offset:18432\n"
    "s_waitcnt lgkmcnt(4)\n"
    "v_mfma_f32_16x16x32_bf16 v[172:175], v[236:239], v[76:79], v[172:175]\n"
    "ds_read_b64_tr_b16 v[232:233], v252 offset:40960\n"
    "v_pk_max_i16 v205, v205, 0\n"
    "v_mfma_f32_16x16x32_bf16 v[168:171], v[236:239], v[72:75], v[168:171]\n"
    "ds_read_b64_tr_b16 v[234:235], v252 offset:43008\n"
    "s_waitcnt lgkmcnt(4)\n"
    "v_mfma_f32_16x16x32_bf16 v[144:147], v[240:243], v[32:35], v[144:147]\n"
    "ds_read_b64_tr_b16 v[236:237], v253 offset:40960\n"
    "v_pk_max_i16 v206, v206, 0\n"
    "v_mfma_f32_16x16x32_bf16 v[148:151], v[240:243], v[36:39], v[148:151]\n"
    "ds_read_b64_tr_b16 v[238:239], v253 offset:43008\n"
    "s_waitcnt lgkmcnt(4)\n"
    "v_mfma_f32_16x16x32_bf16 v[156:159], v[244:247], v[36:39], v[156:159]\n"
    "ds_read_b64_tr_b16 v[240:241], v254 offset:20480\n"
    "v_pk_max_i16 v207, v207, 0\n"
    "v_mfma_f32_16x16x32_bf16 v[152:155], v[244:247], v[32:35], v[152:155]\n"
    "ds_read_b64_tr_b16 v[242:243], v254 offset:22528\n"
    "s_waitcnt lgkmcnt(4)\n"
    "v_mfma_f32_16x16x32_bf16 v[160:163], v[232:235], v[80:83], v[160:163]\n"
    "ds_read_b64_tr_b16 v[244:245], v255 offset:20480\n"
    "s_add_u32 s72, s72, 32768\n"
    "v_mfma_f32_16x16x32_bf16 v[164:167], v[232:235], v[84:87], v[164:167]\n"
    "ds_read_b64_tr_b16 v[246:247], v255 offset:22528\n"
    "s_waitcnt lgkmcnt(4)\n"
    "v_mfma_f32_16x16x32_bf16 v[172:175], v[236:239], v[84:87], v[172:175]\n"
    "ds_read_b64_tr_b16 v[232:233], v252 offset:45056\n"
    "v_lshrrev_b32 v248, 6, v250\n"
    "v_mfma_f32_16x16x32_bf16 v[168:171], v[236:239], v[80:83], v[168:171]\n"
    "ds_read_b64_tr_b16 v[234:235], v252 offset:47104\n"
    "s_waitcnt lgkmcnt(4)\n"
    "v_mfma_f32_16x16x32_bf16 v[144:147], v[240:243], v[40:43], v[144:147]\n"
    "ds_read_b64_tr_b16 v[236:237], v253 offset:45056\n"
    "v_lshlrev_b32 v248, 6, v248\n"
    "v_mfma_f32_16x16x32_bf16 v[148:151], v[240:243], v[44:47], v[148:151]\n"
    "ds_read_b64_tr_b16 v[238:239], v253 offset:47104\n"
    "s_waitcnt lgkmcnt(4)\n"
    "v_mfma_f32_16x16x32_bf16 v[156:159], v[244:247], v[44:47], v[156:159]\n"
    "ds_read_b64_tr_b16 v[240:241], v254 offset:24576\n"
    "buffer_store_dwordx4 v[192:195], v250, s[60:63], s68 offen offset:0 sc1\n"
    "v_mfma_f32_16x16x32_bf16 v[152:155], v[244:247], v[40:43], v[152:155]\n"
    "ds_read_b64_tr_b16 v[242:243], v254 offset:26624\n"
    "s_waitcnt lgkmcnt(4)\n"
    "v_mfma_f32_16x16x32_bf16 v[160:163], v[232:235], v[88:91], v[160:163]\n"
    "ds_read_b64_tr_b16 v[244:245], v255 offset:24576\n"
    "buffer_store_dwordx4 v[196:199], v250, s[60:63], s68 offen offset:1024 sc1\n"
    "v_mfma_f32_16x16x32_bf16 v[164:167], v[232:235], v[92:95], v[164:167]\n"
    "ds_read_b64_tr_b16 v[246:247], v255 offset:26624\n"
    "s_waitcnt lgkmcnt(4)\n"
    "v_mfma_f32_16x16x32_bf16 v[172:175], v[236:239], v[92:95], v[172:175]\n"
    "ds_read_b64_tr_b16 v[232:233], v252 offset:49152\n"
    "buffer_store_dwordx4 v[200:203], v250, s[60:63], s68 offen offset:2048 sc1\n"
    "v_mfma_f32_16x16x32_bf16 v[168:171], v[236:239], v[88:91], v[168:171]\n"
    "ds_read_b64_tr_b16 v[234:235], v252 offset:51200\n"
    "s_waitcnt lgkmcnt(4)\n"
    "v_mfma_f32_16x16x32_bf16 v[144:147], v[240:243], v[48:51], v[144:147]\n"
    "ds_read_b64_tr_b16 v[236:237], v253 offset:49152\n"
    "buffer_store_dwordx4 v[204:207], v250, s[60:63], s68 offen offset:3072 sc1\n"
    "buffer_store_dwordx4 v[228:231], v250, s[60:63], s75 offen offset:1024 sc1\n"
    "v_mfma_f32_16x16x32_bf16 v[148:151], v[240:243], v[52:55], v[148:151]\n"
    "ds_read_b64_tr_b16 v[238:239], v253 offset:51200\n"
    "s_waitcnt lgkmcnt(4)\n"
    "v_mfma_f32_16x16x32_bf16 v[156:159], v[244:247], v[52:55], v[156:159]\n"
    "ds_read_b64_tr_b16 v[240:241], v254 offset:28672\n"
    "v_bfe_u32 v249, v250, 4, 2\n"
    "v_mfma_f32_16x16x32_bf16 v[152:155], v[244:247], v[48:51], v[152:155]\n"
    "ds_read_b64_tr_b16 v[242:243], v254 offset:30720\n"
    "s_waitcnt lgkmcnt(4)\n"
    "v_mfma_f32_16x16x32_bf16 v[160:163], v[232:235], v[96:99], v[160:163]\n"
    "s_barrier\n"
    "ds_read_b64_tr_b16 v[244:245], v255 offset:28672\n"
    "v_mfma_f32_16x16x32_bf16 v[164:167], v[232:235], v[100:103], v[164:167]\n"
    "ds_read_b64_tr_b16 v[246:247], v255 offset:30720\n"
    "s_waitcnt lgkmcnt(4)\n"
    "v_mfma_f32_16x16x32_bf16 v[172:175], v[236:239], v[100:103], v[172:175]\n"
    "ds_read_b64_tr_b16 v[232:233], v252 offset:53248\n"
    "v_lshl_add_u32 v248, v249, 3, v248\n"
    "v_mfma_f32_16x16x32_bf16 v[168:171], v[236:239], v[96:99], v[168:171]\n"
    "ds_read_b64_tr_b16 v[234:235], v252 offset:55296\n"
    "s_waitcnt lgkmcnt(4)\n"
    "v_mfma_f32_16x16x32_bf16 v[144:147], v[240:243], v[56:59], v[144:147]\n"
    "ds_read_b64_tr_b16 v[236:237], v253 offset:53248\n"
    "v_bfe_u32 v249, v250, 8, 1\n"
    "v_mfma_f32_16x16x32_bf16 v[148:151], v[240:243], v[60:63], v[148:151]\n"
    "ds_read_b64_tr_b16 v[238:239], v253 offset:55296\n"
    "s_waitcnt lgkmcnt(4)\n"
    "v_mfma_f32_16x16x32_bf16 v[156:159], v[244:247], v[60:63], v[156:159]\n"
    "ds_read_b64_tr_b16 v[240:241], v254 offset:32768\n"
    "v_lshl_add_u32 v248, v249, 5, v248\n"
    "v_mfma_f32_16x16x32_bf16 v[152:155], v[244:247], v[56:59], v[152:155]\n"
    "ds_read_b64_tr_b16 v[242:243], v254 offset:34816\n"
    "s_waitcnt lgkmcnt(4)\n"
    "v_mfma_f32_16x16x32_bf16 v[160:163], v[232:235], v[104:107], v[160:163]\n"
    "ds_read_b64_tr_b16 v[244:245], v255 offset:32768\n"
    "v_xor_b32 v249, 32, v248\n"
    "v_mfma_f32_16x16x32_bf16 v[164:167], v[232:235], v[108:111], v[164:167]\n"
    "ds_read_b64_tr_b16 v[246:247], v255 offset:34816\n"
    "s_waitcnt lgkmcnt(4)\n"
    "v_mfma_f32_16x16x32_bf16 v[172:175], v[236:239], v[108:111], v[172:175]\n"
    "ds_read_b64_tr_b16 v[232:233], v252 offset:57344\n"
    "v_mfma_f32_16x16x32_bf16 v[168:171], v[236:239], v[104:107], v[168:171]\n"
    "ds_read_b64_tr_b16 v[234:235], v252 offset:59392\n"
    "s_waitcnt lgkmcnt(4)\n"
    "v_mfma_f32_16x16x32_bf16 v[144:147], v[240:243], v[64:67], v[144:147]\n"
    "ds_read_b64_tr_b16 v[236:237], v253 offset:57344\n"
    "v_mfma_f32_16x16x32_bf16 v[148:151], v[240:243], v[68:71], v[148:151]\n"
    "ds_read_b64_tr_b16 v[238:239], v253 offset:59392\n"
    "s_waitcnt lgkmcnt(4)\n"
    "v_mfma_f32_16x16x32_bf16 v[156:159], v[244:247], v[68:71], v[156:159]\n"
    "ds_read_b64_tr_b16 v[240:241], v254 offset:36864\n"
    "v_mfma_f32_16x16x32_bf16 v[152:155], v[244:247], v[64:67], v[152:155]\n"
    "ds_read_b64_tr_b16 v[242:243], v254 offset:38912\n"
    "s_waitcnt lgkmcnt(4)\n"
    "v_mfma_f32_16x16x32_bf16 v[160:163], v[232:235], v[112:115], v[160:163]\n"
    "ds_read_b64_tr_b16 v[244:245], v255 offset:36864\n"
    "v_mfma_f32_16x16x32_bf16 v[164:167], v[232:235], v[116:119], v[164:167]\n"
    "ds_read_b64_tr_b16 v[246:247], v255 offset:38912\n"
    "s_waitcnt lgkmcnt(4)\n"
    "v_mfma_f32_16x16x32_bf16 v[172:175], v[236:239], v[116:119], v[172:175]\n"
    "ds_read_b64_tr_b16 v[232:233], v252 offset:61440\n"
    "v_mfma_f32_16x16x32_bf16 v[168:171], v[236:239], v[112:115], v[168:171]\n"
    "ds_read_b64_tr_b16 v[234:235], v252 offset:63488\n"
    "s_waitcnt lgkmcnt(4)\n"
    "v_mfma_f32_16x16x32_bf16 v[144:147], v[240:243], v[72:75], v[144:147]\n"
    "ds_read_b64_tr_b16 v[236:237], v253 offset:61440\n"
    "v_add_u32 v252, 0x10000, v252\n"
    "v_mfma_f32_16x16x32_bf16 v[148:151], v[240:243], v[76:79], v[148:151]\n"
    "ds_read_b64_tr_b16 v[238:239], v253 offset:63488\n"
    "s_waitcnt lgkmcnt(4)\n"
    "v_mfma_f32_16x16x32_bf16 v[156:159], v[244:247], v[76:79], v[156:159]\n"
    "ds_read_b64_tr_b16 v[240:241], v254 offset:40960\n"
    "v_add_u32 v253, 0x10000, v253\n"
    "v_mfma_f32_16x16x32_bf16 v[152:155], v[244:247], v[72:75], v[152:155]\n"
    "ds_read_b64_tr_b16 v[242:243], v254 offset:43008\n"
    "s_waitcnt lgkmcnt(4)\n"
    "v_mfma_f32_16x16x32_bf16 v[160:163], v[232:235], v[120:123], v[160:163]\n"
    "ds_read_b64_tr_b16 v[244:245], v255 offset:40960\n"
    "v_mfma_f32_16x16x32_bf16 v[164:167], v[232:235], v[124:127], v[164:167]\n"
    "ds_read_b64_tr_b16 v[246:247], v255 offset:43008\n"
    "s_waitcnt lgkmcnt(4)\n"
    "v_mfma_f32_16x16x32_bf16 v[172:175], v[236:239], v[124:127], v[172:175]\n"
    "ds_read_b64_tr_b16 v[232:233], v252 offset:0\n"
    "v_mfma_f32_16x16x32_bf16 v[168:171], v[236:239], v[120:123], v[168:171]\n"
    "ds_read_b64_tr_b16 v[234:235], v252 offset:2048\n"
    "s_waitcnt lgkmcnt(4)\n"
    "v_mfma_f32_16x16x32_bf16 v[144:147], v[240:243], v[80:83], v[144:147]\n"
    "ds_read_b64_tr_b16 v[236:237], v253 offset:0\n"
    "v_mfma_f32_16x16x32_bf16 v[148:151], v[240:243], v[84:87], v[148:151]\n"
    "ds_read_b64_tr_b16 v[238:239], v253 offset:2048\n"
    "s_waitcnt lgkmcnt(4)\n"
    "v_mfma_f32_16x16x32_bf16 v[156:159], v[244:247], v[84:87], v[156:159]\n"
    "ds_read_b64_tr_b16 v[240:241], v254 offset:45056\n"
    "v_mfma_f32_16x16x32_bf16 v[152:155], v[244:247], v[80:83], v[152:155]\n"
    "ds_read_b64_tr_b16 v[242:243], v254 offset:47104\n"
    "s_waitcnt lgkmcnt(4)\n"
    "v_mfma_f32_16x16x32_bf16 v[160:163], v[232:235], v[128:131], v[160:163]\n"
    "ds_read_b64_tr_b16 v[244:245], v255 offset:45056\n"
    "buffer_store_dwordx4 v[228:231], v250, s[60:63], s75 offen offset:2048 sc1\n"
    "v_mfma_f32_16x16x32_bf16 v[164:167], v[232:235], v[132:135], v[164:167]\n"
    "ds_read_b64_tr_b16 v[246:247], v255 offset:47104\n"
    "s_waitcnt lgkmcnt(4)\n"
    "v_mfma_f32_16x16x32_bf16 v[172:175], v[236:239], v[132:135], v[172:175]\n"
    "ds_read_b64_tr_b16 v[232:233], v252 offset:4096\n"
    "v_mfma_f32_16x16x32_bf16 v[168:171], v[236:239], v[128:131], v[168:171]\n"
    "ds_read_b64_tr_b16 v[234:235], v252 offset:6144\n"
    "s_waitcnt lgkmcnt(4)\n"
    "v_mfma_f32_16x16x32_bf16 v[144:147], v[240:243], v[88:91], v[144:147]\n"
    "s_barrier\n"
    "ds_read_b64_tr_b16 v[236:237], v253 offset:4096\n"
    "v_mfma_f32_16x16x32_bf16 v[148:151], v[240:243], v[92:95], v[148:151]\n"
    "ds_read_b64_tr_b16 v[238:239], v253 offset:6144\n"
    "s_waitcnt lgkmcnt(4)\n"
    "v_mfma_f32_16x16x32_bf16 v[156:159], v[244:247], v[92:95], v[156:159]\n"
    "ds_read_b64_tr_b16 v[240:241], v254 offset:49152\n"
    "v_mfma_f32_16x16x32_bf16 v[152:155], v[244:247], v[88:91], v[152:155]\n"
    "ds_read_b64_tr_b16 v[242:243], v254 offset:51200\n"
    "s_waitcnt lgkmcnt(4)\n"
    "v_mfma_f32_16x16x32_bf16 v[160:163], v[232:235], v[136:139], v[160:163]\n"
    "ds_read_b64_tr_b16 v[244:245], v255 offset:49152\n"
    "v_mfma_f32_16x16x32_bf16 v[164:167], v[232:235], v[140:143], v[164:167]\n"
    "ds_read_b64_tr_b16 v[246:247], v255 offset:51200\n"
    "s_waitcnt lgkmcnt(4)\n"
    "v_mfma_f32_16x16x32_bf16 v[172:175], v[236:239], v[140:143], v[172:175]\n"
    "ds_read_b64_tr_b16 v[232:233], v254 offset:53248\n"
    "v_mfma_f32_16x16x32_bf16 v[168:171], v[236:239], v[136:139], v[168:171]\n"
    "ds_read_b64_tr_b16 v[234:235], v254 offset:55296\n"
    "s_waitcnt lgkmcnt(4)\n"
    "v_mfma_f32_16x16x32_bf16 v[144:147], v[240:243], v[96:99], v[144:147]\n"
    "ds_read_b64_tr_b16 v[236:237], v255 offset:53248\n"
    "v_mfma_f32_16x16x32_bf16 v[148:151], v[240:243], v[100:103], v[148:151]\n"
    "ds_read_b64_tr_b16 v[238:239], v255 offset:55296\n"
    "s_waitcnt lgkmcnt(4)\n"
    "v_mfma_f32_16x16x32_bf16 v[156:159], v[244:247], v[100:103], v[156:159]\n"
    "ds_read_b64_tr_b16 v[240:241], v254 offset:57344\n"
    "v_cvt_pk_bf16_f32 v208, v160, v161\n"
    "v_mfma_f32_16x16x32_bf16 v[152:155], v[244:247], v[96:99], v[152:155]\n"
    "ds_read_b64_tr_b16 v[242:243], v254 offset:59392\n"
    "s_waitcnt lgkmcnt(4)\n"
    "v_mfma_f32_16x16x32_bf16 v[144:147], v[232:235], v[104:107], v[144:147]\n"
    "ds_read_b64_tr_b16 v[244:245], v255 offset:57344\n"
    "v_cvt_pk_bf16_f32 v209, v162, v163\n"
    "v_mfma_f32_16x16x32_bf16 v[148:151], v[232:235], v[108:111], v[148:151]\n"
    "ds_read_b64_tr_b16 v[246:247], v255 offset:59392\n"
    "s_waitcnt lgkmcnt(4)\n"
    "v_mfma_f32_16x16x32_bf16 v[156:159], v[236:239], v[108:111], v[156:159]\n"
    "ds_read_b64_tr_b16 v[232:233], v254 offset:61440\n"
    "v_cvt_pk_bf16_f32 v210, v168, v169\n"
    "v_mfma_f32_16x16x32_bf16 v[152:155], v[236:239], v[104:107], v[152:155]\n"
    "ds_read_b64_tr_b16 v[234:235], v254 offset:63488\n"
    "s_waitcnt lgkmcnt(4)\n"
    "v_mfma_f32_16x16x32_bf16 v[144:147], v[240:243], v[112:115], v[144:147]\n"
    "ds_read_b64_tr_b16 v[236:237], v255 offset:61440\n"
    "v_add_u32 v254, 0x10000, v254\n"
    "v_mfma_f32_16x16x32_bf16 v[148:151], v[240:243], v[116:119], v[148:151]\n"
    "ds_read_b64_tr_b16 v[238:239], v255 offset:63488\n"
    "s_waitcnt lgkmcnt(4)\n"
    "v_mfma_f32_16x16x32_bf16 v[156:159], v[244:247], v[116:119], v[156:159]\n"
    "ds_read_b64_tr_b16 v[240:241], v254 offset:0\n"
    "v_add_u32 v255, 0x10000, v255\n"
    "v_mfma_f32_16x16x32_bf16 v[152:155], v[244:247], v[112:115], v[152:155]\n"
    "ds_read_b64_tr_b16 v[242:243], v254 offset:2048\n"
    "s_waitcnt lgkmcnt(4)\n"
    "v_mfma_f32_16x16x32_bf16 v[144:147], v[232:235], v[120:123], v[144:147]\n"
    "ds_read_b64_tr_b16 v[244:245], v255 offset:0\n"
    "v_cvt_pk_bf16_f32 v211, v170, v171\n"
    "v_mfma_f32_16x16x32_bf16 v[148:151], v[232:235], v[124:127], v[148:151]\n"
    "ds_read_b64_tr_b16 v[246:247], v255 offset:2048\n"
    "s_waitcnt lgkmcnt(4)\n"
    "v_mfma_f32_16x16x32_bf16 v[156:159], v[236:239], v[124:127], v[156:159]\n"
    "ds_read_b64_tr_b16 v[232:233], v254 offset:4096\n"
    "v_cvt_pk_bf16_f32 v212, v164, v165\n"
    "v_mfma_f32_16x16x32_bf16 v[152:155], v[236:239], v[120:123], v[152:155]\n"
    "ds_read_b64_tr_b16 v[234:235], v254 offset:6144\n"
    "s_waitcnt lgkmcnt(4)\n"
    "v_mfma_f32_16x16x32_bf16 v[144:147], v[240:243], v[128:131], v[144:147]\n"
    "ds_read_b64_tr_b16 v[236:237], v255 offset:4096\n"
    "v_cvt_pk_bf16_f32 v213, v166, v167\n"
    "v_mfma_f32_16x16x32_bf16 v[148:151], v[240:243], v[132:135], v[148:151]\n"
    "ds_read_b64_tr_b16 v[238:239], v255 offset:6144\n"
    "s_waitcnt lgkmcnt(4)\n"
    "v_mfma_f32_16x16x32_bf16 v[156:159], v[244:247], v[132:135], v[156:159]\n"
    "ds_read_b64_tr_b16 v[240:241], v252 offset:8192\n"
    "v_cvt_pk_bf16_f32 v214, v172, v173\n"
    "v_mfma_f32_16x16x32_bf16 v[152:155], v[244:247], v[128:131], v[152:155]\n"
    "ds_read_b64_tr_b16 v[242:243], v252 offset:10240\n"
    "s_waitcnt lgkmcnt(4)\n"
    "v_mfma_f32_16x16x32_bf16 v[144:147], v[232:235], v[136:139], v[144:147]\n"
    "ds_read_b64_tr_b16 v[244:245], v253 offset:8192\n"
    "v_cvt_pk_bf16_f32 v215, v174, v175\n"
    "v_mfma_f32_16x16x32_bf16 v[148:151], v[232:235], v[140:143], v[148:151]\n"
    "ds_read_b64_tr_b16 v[246:247], v253 offset:10240\n"
    "s_waitcnt lgkmcnt(4)\n"
    "v_mfma_f32_16x16x32_bf16 v[156:159], v[236:239], v[140:143], v[156:159]\n"
    "ds_read_b64_tr_b16 v[232:233], v252 offset:12288\n"
    "v_mfma_f32_16x16x32_bf16 v[152:155], v[236:239], v[136:139], v[152:155]\n"
    "ds_read_b64_tr_b16 v[234:235], v252 offset:14336\n"
    "s_waitcnt lgkmcnt(4)\n"
    "v_mfma_f32_16x16x32_bf16 v[160:163], v[240:243], v[0:3], 0\n"
    "ds_read_b64_tr_b16 v[236:237], v253 offset:12288\n"
    "buffer_store_dwordx4 v[228:231], v250, s[60:63], s75 offen offset:3072 sc1\n"
    "buffer_store_dwordx4 v[228:231], v250, s[60:63], s70 offen offset:0 sc1\n"
    "v_mfma_f32_16x16x32_bf16 v[164:167], v[240:243], v[4:7], 0\n"
    "ds_read_b64_tr_b16 v[238:239], v253 offset:14336\n"
    "s_waitcnt lgkmcnt(4)\n"
    "v_mfma_f32_16x16x32_bf16 v[172:175], v[244:247], v[4:7], 0\n"
    "ds_read_b64_tr_b16 v[240:241], v252 offset:16384\n"
    "v_cvt_pk_bf16_f32 v216, v144, v145\n"
    "v_mfma_f32_16x16x32_bf16 v[168:171], v[244:247], v[0:3], 0\n"
    "ds_read_b64_tr_b16 v[242:243], v252 offset:18432\n"
    "s_waitcnt lgkmcnt(4)\n"
    "v_mfma_f32_16x16x32_bf16 v[160:163], v[232:235], v[8:11], v[160:163]\n"
    "s_waitcnt vmcnt(4)\n"
    "s_barrier\n"
    "ds_read_b64_tr_b16 v[244:245], v253 offset:16384\n"
    "v_mfma_f32_16x16x32_bf16 v[164:167], v[232:235], v[12:15], v[164:167]\n"
    "ds_read_b64_tr_b16 v[246:247], v253 offset:18432\n"
    "s_waitcnt lgkmcnt(4)\n"
    "v_mfma_f32_16x16x32_bf16 v[172:175], v[236:239], v[12:15], v[172:175]\n"
    "ds_read_b64_tr_b16 v[232:233], v252 offset:20480\n"
    "v_cvt_pk_bf16_f32 v217, v146, v147\n"
    "v_mfma_f32_16x16x32_bf16 v[168:171], v[236:239], v[8:11], v[168:171]\n"
    "ds_read_b64_tr_b16 v[234:235], v252 offset:22528\n"
    "s_waitcnt lgkmcnt(4)\n"
    "v_mfma_f32_16x16x32_bf16 v[160:163], v[240:243], v[16:19], v[160:163]\n"
    "ds_read_b64_tr_b16 v[236:237], v253 offset:20480\n"
    "v_cvt_pk_bf16_f32 v218, v152, v153\n"
    "v_mfma_f32_16x16x32_bf16 v[164:167], v[240:243], v[20:23], v[164:167]\n"
    "ds_read_b64_tr_b16 v[238:239], v253 offset:22528\n"
    "s_waitcnt lgkmcnt(4)\n"
    "v_mfma_f32_16x16x32_bf16 v[172:175], v[244:247], v[20:23], v[172:175]\n"
    "ds_read_b64_tr_b16 v[240:241], v252 offset:24576\n"
    "v_cvt_pk_bf16_f32 v219, v154, v155\n"
    "v_mfma_f32_16x16x32_bf16 v[168:171], v[244:247], v[16:19], v[168:171]\n"
    "ds_read_b64_tr_b16 v[242:243], v252 offset:26624\n"
    "s_waitcnt lgkmcnt(4)\n"
    "v_mfma_f32_16x16x32_bf16 v[160:163], v[232:235], v[24:27], v[160:163]\n"
    "ds_read_b64_tr_b16 v[244:245], v253 offset:24576\n"
    "v_cvt_pk_bf16_f32 v220, v148, v149\n"
    "v_mfma_f32_16x16x32_bf16 v[164:167], v[232:235], v[28:31], v[164:167]\n"
    "ds_read_b64_tr_b16 v[246:247], v253 offset:26624\n"
    "s_waitcnt lgkmcnt(4)\n"
    "v_mfma_f32_16x16x32_bf16 v[172:175], v[236:239], v[28:31], v[172:175]\n"
    "ds_read_b64_tr_b16 v[232:233], v252 offset:28672\n"
    "v_cvt_pk_bf16_f32 v221, v150, v151\n"
    "v_mfma_f32_16x16x32_bf16 v[168:171], v[236:239], v[24:27], v[168:171]\n"
    "ds_read_b64_tr_b16 v[234:235], v252 offset:30720\n"
    "s_waitcnt lgkmcnt(4)\n"
    "v_mfma_f32_16x16x32_bf16 v[160:163], v[240:243], v[32:35], v[160:163]\n"
    "ds_read_b64_tr_b16 v[236:237], v253 offset:28672\n"
    "v_cvt_pk_bf16_f32 v222, v156, v157\n"
    "v_mfma_f32_16x16x32_bf16 v[164:167], v[240:243], v[36:39], v[164:167]\n"
    "ds_read_b64_tr_b16 v[238:239], v253 offset:30720\n"
    "s_waitcnt lgkmcnt(4)\n"
    "v_mfma_f32_16x16x32_bf16 v[172:175], v[244:247], v[36:39], v[172:175]\n"
    "ds_read_b64_tr_b16 v[240:241], v254 offset:8192\n"
    "v_cvt_pk_bf16_f32 v223, v158, v159\n"
    "v_mfma_f32_16x16x32_bf16 v[168:171], v[244:247], v[32:35], v[168:171]\n"
    "ds_read_b64_tr_b16 v[242:243], v254 offset:10240\n"
    "s_waitcnt lgkmcnt(4)\n"
    "v_mfma_f32_16x16x32_bf16 v[160:163], v[232:235], v[40:43], v[160:163]\n"
    "ds_read_b64_tr_b16 v[244:245], v255 offset:8192\n"
    "v_pk_max_i16 v208, v208, 0\n"
    "v_mfma_f32_16x16x32_bf16 v[164:167], v[232:235], v[44:47], v[164:167]\n"
    "ds_read_b64_tr_b16 v[246:247], v255 offset:10240\n"
    "s_waitcnt lgkmcnt(4)\n"
    "v_mfma_f32_16x16x32_bf16 v[172:175], v[236:239], v[44:47], v[172:175]\n"
    "ds_read_b64_tr_b16 v[232:233], v252 offset:32768\n"
    "v_pk_max_i16 v209, v209, 0\n"
    "v_mfma_f32_16x16x32_bf16 v[168:171], v[236:239], v[40:43], v[168:171]\n"
    "ds_read_b64_tr_b16 v[234:235], v252 offset:34816\n"
    "s_waitcnt lgkmcnt(4)\n"
    "v_mfma_f32_16x16x32_bf16 v[144:147], v[240:243], v[0:3], 0\n"
    "ds_read_b64_tr_b16 v[236:237], v253 offset:32768\n"
    "v_pk_max_i16 v210, v210, 0\n"
    "v_mfma_f32_16x16x32_bf16 v[148:151], v[240:243], v[4:7], 0\n"
    "ds_read_b64_tr_b16 v[238:239], v253 offset:34816\n"
    "s_waitcnt lgkmcnt(4)\n"
    "v_mfma_f32_16x16x32_bf16 v[156:159], v[244:247], v[4:7], 0\n"
    "ds_read_b64_tr_b16 v[240:241], v254 offset:12288\n"
    "v_pk_max_i16 v211, v211, 0\n"
    "v_mfma_f32_16x16x32_bf16 v[152:155], v[244:247], v[0:3], 0\n"
    "ds_read_b64_tr_b16 v[242:243], v254 offset:14336\n"
    "s_waitcnt lgkmcnt(4)\n"
    "v_mfma_f32_16x16x32_bf16 v[160:163], v[232:235], v[48:51], v[160:163]\n"
    "ds_read_b64_tr_b16 v[244:245], v255 offset:12288\n"
    "v_pk_max_i16 v212, v212, 0\n"
    "v_mfma_f32_16x16x32_bf16 v[164:167], v[232:235], v[52:55], v[164:167]\n"
    "ds_read_b64_tr_b16 v[246:247], v255 offset:14336\n"
    "s_waitcnt lgkmcnt(4)\n"
    "v_mfma_f32_16x16x32_bf16 v[172:175], v[236:239], v[52:55], v[172:175]\n"
    "ds_read_b64_tr_b16 v[232:233], v252 offset:36864\n"
    "v_pk_max_i16 v213, v213, 0\n"
    "v_mfma_f32_16x16x32_bf16 v[168:171], v[236:239], v[48:51], v[168:171]\n"
    "ds_read_b64_tr_b16 v[234:235], v252 offset:38912\n"
    "s_waitcnt lgkmcnt(4)\n"
    "v_mfma_f32_16x16x32_bf16 v[144:147], v[240:243], v[8:11], v[144:147]\n"
    "ds_read_b64_tr_b16 v[236:237], v253 offset:36864\n"
    "v_add_u32 v252, 0xffff0000, v252\n"
    "v_mfma_f32_16x16x32_bf16 v[148:151], v[240:243], v[12:15], v[148:151]\n"
    "ds_read_b64_tr_b16 v[238:239], v253 offset:38912\n"
    "s_waitcnt lgkmcnt(4)\n"
    "v_mfma_f32_16x16x32_bf16 v[156:159], v[244:247], v[12:15], v[156:159]\n"
    "ds_read_b64_tr_b16 v[240:241], v254 offset:16384\n"
    "v_add_u32 v253, 0xffff0000, v253\n"
    "v_mfma_f32_16x16x32_bf16 v[152:155], v[244:247], v[8:11], v[152:155]\n"
    "ds_read_b64_tr_b16 v[242:243], v254 offset:18432\n"
    "s_waitcnt lgkmcnt(4)\n"
    "v_mfma_f32_16x16x32_bf16 v[160:163], v[232:235], v[56:59], v[160:163]\n"
    "ds_read_b64_tr_b16 v[244:245], v255 offset:16384\n"
    "buffer_store_dwordx4 v[228:231], v250, s[60:63], s70 offen offset:1024 sc1\n"
    "v_mfma_f32_16x16x32_bf16 v[164:167], v[232:235], v[60:63], v[164:167]\n"
    "ds_read_b64_tr_b16 v[246:247], v255 offset:18432\n"
    "s_waitcnt lgkmcnt(4)\n"
    "v_mfma_f32_16x16x32_bf16 v[172:175], v[236:239], v[60:63], v[172:175]\n"
    "ds_read_b64_tr_b16 v[232:233], v252 offset:0\n"
    "v_pk_max_i16 v214, v214, 0\n"
    "v_mfma_f32_16x16x32_bf16 v[168:171], v[236:239], v[56:59], v[168:171]\n"
    "ds_read_b64_tr_b16 v[234:235], v252 offset:2048\n"
    "s_waitcnt lgkmcnt(4)\n"
    "v_mfma_f32_16x16x32_bf16 v[144:147], v[240:243], v[16:19], v[144:147]\n"
    "s_barrier\n"
    "ds_read_b64_tr_b16 v[236:237], v253 offset:0\n"
    "v_mfma_f32_16x16x32_bf16 v[148:151], v[240:243], v[20:23], v[148:151]\n"
    "ds_read_b64_tr_b16 v[238:239], v253 offset:2048\n"
    "s_waitcnt lgkmcnt(4)\n"
    "v_mfma_f32_16x16x32_bf16 v[156:159], v[244:247], v[20:23], v[156:159]\n"
    "ds_read_b64_tr_b16 v[240:241], v254 offset:20480\n"
    "v_pk_max_i16 v215, v215, 0\n"
    "v_mfma_f32_16x16x32_bf16 v[152:155], v[244:247], v[16:19], v[152:155]\n"
    "ds_read_b64_tr_b16 v[242:243], v254 offset:22528\n"
    "s_waitcnt lgkmcnt(4)\n"
    "v_mfma_f32_16x16x32_bf16 v[160:163], v[232:235], v[64:67], v[160:163]\n"
    "ds_read_b64_tr_b16 v[244:245], v255 offset:20480\n"
    "v_pk_max_i16 v216, v216, 0\n"
    "v_mfma_f32_16x16x32_bf16 v[164:167], v[232:235], v[68:71], v[164:167]\n"
    "ds_read_b64_tr_b16 v[246:247], v255 offset:22528\n"
    "s_waitcnt lgkmcnt(4)\n"
    "v_mfma_f32_16x16x32_bf16 v[172:175], v[236:239], v[68:71], v[172:175]\n"
    "ds_read_b64_tr_b16 v[232:233], v252 offset:4096\n"
    "v_pk_max_i16 v217, v217, 0\n"
    "v_mfma_f32_16x16x32_bf16 v[168:171], v[236:239], v[64:67], v[168:171]\n"
    "ds_read_b64_tr_b16 v[234:235], v252 offset:6144\n"
    "s_waitcnt lgkmcnt(4)\n"
    "v_mfma_f32_16x16x32_bf16 v[144:147], v[240:243], v[24:27], v[144:147]\n"
    "ds_read_b64_tr_b16 v[236:237], v253 offset:4096\n"
    "v_pk_max_i16 v218, v218, 0\n"
    "v_mfma_f32_16x16x32_bf16 v[148:151], v[240:243], v[28:31], v[148:151]\n"
    "ds_read_b64_tr_b16 v[238:239], v253 offset:6144\n"
    "s_waitcnt lgkmcnt(4)\n"
    "v_mfma_f32_16x16x32_bf16 v[156:159], v[244:247], v[28:31], v[156:159]\n"
    "ds_read_b64_tr_b16 v[240:241], v254 offset:24576\n"
    "v_pk_max_i16 v219, v219, 0\n"
    "v_mfma_f32_16x16x32_bf16 v[152:155], v[244:247], v[24:27], v[152:155]\n"
    "ds_read_b64_tr_b16 v[242:243], v254 offset:26624\n"
    "s_waitcnt lgkmcnt(4)\n"
    "v_mfma_f32_16x16x32_bf16 v[160:163], v[232:235], v[72:75], v[160:163]\n"
    "ds_read_b64_tr_b16 v[244:245], v255 offset:24576\n"
    "v_pk_max_i16 v220, v220, 0\n"
    "v_mfma_f32_16x16x32_bf16 v[164:167], v[232:235], v[76:79], v[164:167]\n"
    "ds_read_b64_tr_b16 v[246:247], v255 offset:26624\n"
    "s_waitcnt lgkmcnt(4)\n"
    "v_mfma_f32_16x16x32_bf16 v[172:175], v[236:239], v[76:79], v[172:175]\n"
    "ds_read_b64_tr_b16 v[232:233], v252 offset:8192\n"
    "v_pk_max_i16 v221, v221, 0\n"
    "v_mfma_f32_16x16x32_bf16 v[168:171], v[236:239], v[72:75], v[168:171]\n"
    "ds_read_b64_tr_b16 v[234:235], v252 offset:10240\n"
    "s_waitcnt lgkmcnt(4)\n"
    "v_mfma_f32_16x16x32_bf16 v[144:147], v[240:243], v[32:35], v[144:147]\n"
    "ds_read_b64_tr_b16 v[236:237], v253 offset:8192\n"
    "v_pk_max_i16 v222, v222, 0\n"
    "v_mfma_f32_16x16x32_bf16 v[148:151], v[240:243], v[36:39], v[148:151]\n"
    "ds_read_b64_tr_b16 v[238:239], v253 offset:10240\n"
    "s_waitcnt lgkmcnt(4)\n"
    "v_mfma_f32_16x16x32_bf16 v[156:159], v[244:247], v[36:39], v[156:159]\n"
    "ds_read_b64_tr_b16 v[240:241], v254 offset:28672\n"
    "v_pk_max_i16 v223, v223, 0\n"
    "v_mfma_f32_16x16x32_bf16 v[152:155], v[244:247], v[32:35], v[152:155]\n"
    "ds_read_b64_tr_b16 v[242:243], v254 offset:30720\n"
    "s_waitcnt lgkmcnt(4)\n"
    "v_mfma_f32_16x16x32_bf16 v[160:163], v[232:235], v[80:83], v[160:163]\n"
    "ds_read_b64_tr_b16 v[244:245], v255 offset:28672\n"
    "s_add_u32 s68, s68, 32768\n"
    "v_mfma_f32_16x16x32_bf16 v[164:167], v[232:235], v[84:87], v[164:167]\n"
    "ds_read_b64_tr_b16 v[246:247], v255 offset:30720\n"
    "s_waitcnt lgkmcnt(4)\n"
    "v_mfma_f32_16x16x32_bf16 v[172:175], v[236:239], v[84:87], v[172:175]\n"
    "ds_read_b64_tr_b16 v[232:233], v252 offset:12288\n"
    "v_mfma_f32_16x16x32_bf16 v[168:171], v[236:239], v[80:83], v[168:171]\n"
    "ds_read_b64_tr_b16 v[234:235], v252 offset:14336\n"
    "s_waitcnt lgkmcnt(4)\n"
    "v_mfma_f32_16x16x32_bf16 v[144:147], v[240:243], v[40:43], v[144:147]\n"
    "ds_read_b64_tr_b16 v[236:237], v253 offset:12288\n"
    "v_mfma_f32_16x16x32_bf16 v[148:151], v[240:243], v[44:47], v[148:151]\n"
    "ds_read_b64_tr_b16 v[238:239], v253 offset:14336\n"
    "s_waitcnt lgkmcnt(4)\n"
    "v_mfma_f32_16x16x32_bf16 v[156:159], v[244:247], v[44:47], v[156:159]\n"
    "ds_read_b64_tr_b16 v[240:241], v254 offset:32768\n"
    "buffer_store_dwordx4 v[208:211], v250, s[60:63], s72 offen offset:0 sc1\n"
    "v_mfma_f32_16x16x32_bf16 v[152:155], v[244:247], v[40:43], v[152:155]\n"
    "ds_read_b64_tr_b16 v[242:243], v254 offset:34816\n"
    "s_waitcnt lgkmcnt(4)\n"
    "v_mfma_f32_16x16x32_bf16 v[160:163], v[232:235], v[88:91], v[160:163]\n"
    "ds_read_b64_tr_b16 v[244:245], v255 offset:32768\n"
    "buffer_store_dwordx4 v[212:215], v250, s[60:63], s72 offen offset:1024 sc1\n"
    "v_mfma_f32_16x16x32_bf16 v[164:167], v[232:235], v[92:95], v[164:167]\n"
    "ds_read_b64_tr_b16 v[246:247], v255 offset:34816\n"
    "s_waitcnt lgkmcnt(4)\n"
    "v_mfma_f32_16x16x32_bf16 v[172:175], v[236:239], v[92:95], v[172:175]\n"
    "ds_read_b64_tr_b16 v[232:233], v252 offset:16384\n"
    "buffer_store_dwordx4 v[216:219], v250, s[60:63], s72 offen offset:2048 sc1\n"
    "L_st5_%=:\n"
    "s_cbranch_execz L_bk%=\n"
    "v_mfma_f32_16x16x32_bf16 v[168:171], v[236:239], v[88:91], v[168:171]\n"
    "ds_read_b64_tr_b16 v[234:235], v252 offset:18432\n"
    "s_waitcnt lgkmcnt(4)\n"
    "v_mfma_f32_16x16x32_bf16 v[144:147], v[240:243], v[48:51], v[144:147]\n"
    "ds_read_b64_tr_b16 v[236:237], v253 offset:16384\n"
    "buffer_store_dwordx4 v[220:223], v250, s[60:63], s72 offen offset:3072 sc1\n"
    "buffer_store_dwordx4 v[228:231], v250, s[60:63], s70 offen offset:2048 sc1\n"
    "v_mfma_f32_16x16x32_bf16 v[148:151], v[240:243], v[52:55], v[148:151]\n"
    "ds_read_b64_tr_b16 v[238:239], v253 offset:18432\n"
    "s_waitcnt lgkmcnt(4)\n"
    "v_mfma_f32_16x16x32_bf16 v[156:159], v[244:247], v[52:55], v[156:159]\n"
    "ds_read_b64_tr_b16 v[240:241], v254 offset:36864\n"
    "v_mfma_f32_16x16x32_bf16 v[152:155], v[244:247], v[48:51], v[152:155]\n"
    "ds_read_b64_tr_b16 v[242:243], v254 offset:38912\n"
    "s_waitcnt lgkmcnt(4)\n"
    "v_mfma_f32_16x16x32_bf16 v[160:163], v[232:235], v[96:99], v[160:163]\n"
    "s_barrier\n"
    "ds_read_b64_tr_b16 v[244:245], v255 offset:36864\n"
    "v_mfma_f32_16x16x32_bf16 v[164:167], v[232:235], v[100:103], v[164:167]\n"
    "ds_read_b64_tr_b16 v[246:247], v255 offset:38912\n"
    "s_waitcnt lgkmcnt(4)\n"
    "v_mfma_f32_16x16x32_bf16 v[172:175], v[236:239], v[100:103], v[172:175]\n"
    "ds_read_b64_tr_b16 v[232:233], v252 offset:20480\n"
    "v_add_u32 v254, 0xffff0000, v254\n"
    "v_mfma_f32_16x16x32_bf16 v[168:171], v[236:239], v[96:99], v[168:171]\n"
    "ds_read_b64_tr_b16 v[234:235], v252 offset:22528\n"
    "s_waitcnt lgkmcnt(4)\n"
    "v_mfma_f32_16x16x32_bf16 v[144:147], v[240:243], v[56:59], v[144:147]\n"
    "ds_read_b64_tr_b16 v[236:237], v253 offset:20480\n"
    "v_add_u32 v255, 0xffff0000, v255\n"
    "v_mfma_f32_16x16x32_bf16 v[148:151], v[240:243], v[60:63], v[148:151]\n"
    "ds_read_b64_tr_b16 v[238:239], v253 offset:22528\n"
    "s_waitcnt lgkmcnt(4)\n"
    "v_mfma_f32_16x16x32_bf16 v[156:159], v[244:247], v[60:63], v[156:159]\n"
    "ds_read_b64_tr_b16 v[240:241], v254 offset:0\n"
    "v_mfma_f32_16x16x32_bf16 v[152:155], v[244:247], v[56:59], v[152:155]\n"
    "ds_read_b64_tr_b16 v[242:243], v254 offset:2048\n"
    "s_waitcnt lgkmcnt(4)\n"
    "v_mfma_f32_16x16x32_bf16 v[160:163], v[232:235], v[104:107], v[160:163]\n"
    "ds_read_b64_tr_b16 v[244:245], v255 offset:0\n"
    "v_mfma_f32_16x16x32_bf16 v[164:167], v[232:235], v[108:111], v[164:167]\n"
    "ds_read_b64_tr_b16 v[246:247], v255 offset:2048\n"
    "s_waitcnt lgkmcnt(4)\n"
    "v_mfma_f32_16x16x32_bf16 v[172:175], v[236:239], v[108:111], v[172:175]\n"
    "ds_read_b64_tr_b16 v[232:233], v252 offset:24576\n"
    "v_mfma_f32_16x16x32_bf16 v[168:171], v[236:239], v[104:107], v[168:171]\n"
    "ds_read_b64_tr_b16 v[234:235], v252 offset:26624\n"
    "s_waitcnt lgkmcnt(4)\n"
    "v_mfma_f32_16x16x32_bf16 v[144:147], v[240:243], v[64:67], v[144:147]\n"
    "ds_read_b64_tr_b16 v[236:237], v253 offset:24576\n"
    "v_mfma_f32_16x16x32_bf16 v[148:151], v[240:243], v[68:71], v[148:151]\n"
    "ds_read_b64_tr_b16 v[238:239], v253 offset:26624\n"
    "s_waitcnt lgkmcnt(4)\n"
    "v_mfma_f32_16x16x32_bf16 v[156:159], v[244:247], v[68:71], v[156:159]\n"
    "ds_read_b64_tr_b16 v[240:241], v254 offset:4096\n"
    "v_mfma_f32_16x16x32_bf16 v[152:155], v[244:247], v[64:67], v[152:155]\n"
    "ds_read_b64_tr_b16 v[242:243], v254 offset:6144\n"
    "s_waitcnt lgkmcnt(4)\n"
    "v_mfma_f32_16x16x32_bf16 v[160:163], v[232:235], v[112:115], v[160:163]\n"
    "ds_read_b64_tr_b16 v[244:245], v255 offset:4096\n"
    "v_mfma_f32_16x16x32_bf16 v[164:167], v[232:235], v[116:119], v[164:167]\n"
    "ds_read_b64_tr_b16 v[246:247], v255 offset:6144\n"
    "s_waitcnt lgkmcnt(4)\n"
    "v_mfma_f32_16x16x32_bf16 v[172:175], v[236:239], v[116:119], v[172:175]\n"
    "ds_read_b64_tr_b16 v[232:233], v252 offset:28672\n"
    "v_mfma_f32_16x16x32_bf16 v[168:171], v[236:239], v[112:115], v[168:171]\n"
    "ds_read_b64_tr_b16 v[234:235], v252 offset:30720\n"
    "s_waitcnt lgkmcnt(4)\n"
    "v_mfma_f32_16x16x32_bf16 v[144:147], v[240:243], v[72:75], v[144:147]\n"
    "ds_read_b64_tr_b16 v[236:237], v253 offset:28672\n"
    "v_mfma_f32_16x16x32_bf16 v[148:151], v[240:243], v[76:79], v[148:151]\n"
    "ds_read_b64_tr_b16 v[238:239], v253 offset:30720\n"
    "s_waitcnt lgkmcnt(4)\n"
    "v_mfma_f32_16x16x32_bf16 v[156:159], v[244:247], v[76:79], v[156:159]\n"
    "ds_read_b64_tr_b16 v[240:241], v254 offset:8192\n"
    "v_mfma_f32_16x16x32_bf16 v[152:155], v[244:247], v[72:75], v[152:155]\n"
    "ds_read_b64_tr_b16 v[242:243], v254 offset:10240\n"
    "s_waitcnt lgkmcnt(4)\n"
    "v_mfma_f32_16x16x32_bf16 v[160:163], v[232:235], v[120:123], v[160:163]\n"
    "ds_read_b64_tr_b16 v[244:245], v255 offset:8192\n"
    "v_mfma_f32_16x16x32_bf16 v[164:167], v[232:235], v[124:127], v[164:167]\n"
    "ds_read_b64_tr_b16 v[246:247], v255 offset:10240\n"
    "s_waitcnt lgkmcnt(4)\n"
    "v_mfma_f32_16x16x32_bf16 v[172:175], v[236:239], v[124:127], v[172:175]\n"
    "ds_read_b64_tr_b16 v[232:233], v252 offset:32768\n"
    "v_mfma_f32_16x16x32_bf16 v[168:171], v[236:239], v[120:123], v[168:171]\n"
    "ds_read_b64_tr_b16 v[234:235], v252 offset:34816\n"
    "s_waitcnt lgkmcnt(4)\n"
    "v_mfma_f32_16x16x32_bf16 v[144:147], v[240:243], v[80:83], v[144:147]\n"
    "ds_read_b64_tr_b16 v[236:237], v253 offset:32768\n"
    "v_mfma_f32_16x16x32_bf16 v[148:151], v[240:243], v[84:87], v[148:151]\n"
    "ds_read_b64_tr_b16 v[238:239], v253 offset:34816\n"
    "s_waitcnt lgkmcnt(4)\n"
    "v_mfma_f32_16x16x32_bf16 v[156:159], v[244:247], v[84:87], v[156:159]\n"
    "ds_read_b64_tr_b16 v[240:241], v254 offset:12288\n"
    "v_mfma_f32_16x16x32_bf16 v[152:155], v[244:247], v[80:83], v[152:155]\n"
    "ds_read_b64_tr_b16 v[242:243], v254 offset:14336\n"
    "s_waitcnt lgkmcnt(4)\n"
    "v_mfma_f32_16x16x32_bf16 v[160:163], v[232:235], v[128:131], v[160:163]\n"
    "ds_read_b64_tr_b16 v[244:245], v255 offset:12288\n"
    "v_mfma_f32_16x16x32_bf16 v[164:167], v[232:235], v[132:135], v[164:167]\n"
    "ds_read_b64_tr_b16 v[246:247], v255 offset:14336\n"
    "s_waitcnt lgkmcnt(4)\n"
    "v_mfma_f32_16x16x32_bf16 v[172:175], v[236:239], v[132:135], v[172:175]\n"
    "ds_read_b64_tr_b16 v[232:233], v252 offset:36864\n"
    "v_mfma_f32_16x16x32_bf16 v[168:171], v[236:239], v[128:131], v[168:171]\n"
    "ds_read_b64_tr_b16 v[234:235], v252 offset:38912\n"
    "s_waitcnt lgkmcnt(4)\n"
    "v_mfma_f32_16x16x32_bf16 v[144:147], v[240:243], v[88:91], v[144:147]\n"
    "s_barrier\n"
    "ds_read_b64_tr_b16 v[236:237], v253 offset:36864\n"
    "v_mfma_f32_16x16x32_bf16 v[148:151], v[240:243], v[92:95], v[148:151]\n"
    "ds_read_b64_tr_b16 v[238:239], v253 offset:38912\n"
    "s_waitcnt lgkmcnt(4)\n"
    "v_mfma_f32_16x16x32_bf16 v[156:159], v[244:247], v[92:95], v[156:159]\n"
    "ds_read_b64_tr_b16 v[240:241], v254 offset:16384\n"
    "v_mfma_f32_16x16x32_bf16 v[152:155], v[244:247], v[88:91], v[152:155]\n"
    "ds_read_b64_tr_b16 v[242:243], v254 offset:18432\n"
    "s_waitcnt lgkmcnt(4)\n"
    "v_mfma_f32_16x16x32_bf16 v[160:163], v[232:235], v[136:139], v[160:163]\n"
    "ds_read_b64_tr_b16 v[244:245], v255 offset:16384\n"
    "v_mfma_f32_16x16x32_bf16 v[164:167], v[232:235], v[140:143], v[164:167]\n"
    "ds_read_b64_tr_b16 v[246:247], v255 offset:18432\n"
    "s_waitcnt lgkmcnt(4)\n"
    "v_mfma_f32_16x16x32_bf16 v[172:175], v[236:239], v[140:143], v[172:175]\n"
    "ds_read_b64_tr_b16 v[232:233], v254 offset:20480\n"
    "v_mfma_f32_16x16x32_bf16 v[168:171], v[236:239], v[136:139], v[168:171]\n"
    "ds_read_b64_tr_b16 v[234:235], v254 offset:22528\n"
    "s_waitcnt lgkmcnt(4)\n"
    "v_mfma_f32_16x16x32_bf16 v[144:147], v[240:243], v[96:99], v[144:147]\n"
    "ds_read_b64_tr_b16 v[236:237], v255 offset:20480\n"
    "v_mfma_f32_16x16x32_bf16 v[148:151], v[240:243], v[100:103], v[148:151]\n"
    "ds_read_b64_tr_b16 v[238:239], v255 offset:22528\n"
    "s_waitcnt lgkmcnt(4)\n"
    "v_mfma_f32_16x16x32_bf16 v[156:159], v[244:247], v[100:103], v[156:159]\n"
    "ds_read_b64_tr_b16 v[240:241], v254 offset:24576\n"
    "v_cvt_pk_bf16_f32 v224, v160, v161\n"
    "v_mfma_f32_16x16x32_bf16 v[152:155], v[244:247], v[96:99], v[152:155]\n"
    "ds_read_b64_tr_b16 v[242:243], v254 offset:26624\n"
    "s_waitcnt lgkmcnt(4)\n"
    "v_mfma_f32_16x16x32_bf16 v[144:147], v[232:235], v[104:107], v[144:147]\n"
    "ds_read_b64_tr_b16 v[244:245], v255 offset:24576\n"
    "v_cvt_pk_bf16_f32 v225, v162, v163\n"
    "v_mfma_f32_16x16x32_bf16 v[148:151], v[232:235], v[108:111], v[148:151]\n"
    "ds_read_b64_tr_b16 v[246:247], v255 offset:26624\n"
    "s_waitcnt lgkmcnt(4)\n"
    "v_mfma_f32_16x16x32_bf16 v[156:159], v[236:239], v[108:111], v[156:159]\n"
    "ds_read_b64_tr_b16 v[232:233], v254 offset:28672\n"
    "v_cvt_pk_bf16_f32 v226, v168, v169\n"
    "v_mfma_f32_16x16x32_bf16 v[152:155], v[236:239], v[104:107], v[152:155]\n"
    "ds_read_b64_tr_b16 v[234:235], v254 offset:30720\n"
    "s_waitcnt lgkmcnt(4)\n"
    "v_mfma_f32_16x16x32_bf16 v[144:147], v[240:243], v[112:115], v[144:147]\n"
    "ds_read_b64_tr_b16 v[236:237], v255 offset:28672\n"
    "v_cvt_pk_bf16_f32 v227, v170, v171\n"
    "v_mfma_f32_16x16x32_bf16 v[148:151], v[240:243], v[116:119], v[148:151]\n"
    "ds_read_b64_tr_b16 v[238:239], v255 offset:30720\n"
    "s_waitcnt lgkmcnt(4)\n"
    "v_mfma_f32_16x16x32_bf16 v[156:159], v[244:247], v[116:119], v[156:159]\n"
    "ds_read_b64_tr_b16 v[240:241], v254 offset:32768\n"
    "v_cvt_pk_bf16_f32 v228, v164, v165\n"
    "v_mfma_f32_16x16x32_bf16 v[152:155], v[244:247], v[112:115], v[152:155]\n"
    "ds_read_b64_tr_b16 v[242:243], v254 offset:34816\n"
    "s_waitcnt lgkmcnt(4)\n"
    "v_mfma_f32_16x16x32_bf16 v[144:147], v[232:235], v[120:123], v[144:147]\n"
    "ds_read_b64_tr_b16 v[244:245], v255 offset:32768\n"
    "v_cvt_pk_bf16_f32 v229, v166, v167\n"
    "v_mfma_f32_16x16x32_bf16 v[148:151], v[232:235], v[124:127], v[148:151]\n"
    "ds_read_b64_tr_b16 v[246:247], v255 offset:34816\n"
    "s_waitcnt lgkmcnt(4)\n"
    "v_mfma_f32_16x16x32_bf16 v[156:159], v[236:239], v[124:127], v[156:159]\n"
    "ds_read_b64_tr_b16 v[232:233], v254 offset:36864\n"
    "v_cvt_pk_bf16_f32 v230, v172, v173\n"
    "v_mfma_f32_16x16x32_bf16 v[152:155], v[236:239], v[120:123], v[152:155]\n"
    "ds_read_b64_tr_b16 v[234:235], v254 offset:38912\n"
    "s_waitcnt lgkmcnt(4)\n"
    "v_mfma_f32_16x16x32_bf16 v[144:147], v[240:243], v[128:131], v[144:147]\n"
    "ds_read_b64_tr_b16 v[236:237], v255 offset:36864\n"
    "v_cvt_pk_bf16_f32 v231, v174, v175\n"
    "v_mfma_f32_16x16x32_bf16 v[148:151], v[240:243], v[132:135], v[148:151]\n"
    "ds_read_b64_tr_b16 v[238:239], v255 offset:38912\n"
    "s_waitcnt lgkmcnt(4)\n"
    "v_mfma_f32_16x16x32_bf16 v[156:159], v[244:247], v[132:135], v[156:159]\n"
    "ds_read_b64_tr_b16 v[240:241], v248 offset:40960\n"
    "v_mfma_f32_16x16x32_bf16 v[152:155], v[244:247], v[128:131], v[152:155]\n"
    "ds_read_b64_tr_b16 v[242:243], v248 offset:41984\n"
    "s_waitcnt lgkmcnt(4)\n"
    "v_mfma_f32_16x16x32_bf16 v[144:147], v[232:235], v[136:139], v[144:147]\n"
    "ds_read_b64_tr_b16 v[244:245], v249 offset:40960\n"
    "v_mfma_f32_16x16x32_bf16 v[148:151], v[232:235], v[140:143], v[148:151]\n"
    "ds_read_b64_tr_b16 v[246:247], v249 offset:41984\n"
    "s_waitcnt lgkmcnt(4)\n"
    "v_mfma_f32_16x16x32_bf16 v[156:159], v[236:239], v[140:143], v[156:159]\n"
    "ds_read_b64_tr_b16 v[232:233], v248 offset:43008\n"
    "v_mfma_f32_16x16x32_bf16 v[152:155], v[236:239], v[136:139], v[152:155]\n"
    "ds_read_b64_tr_b16 v[234:235], v248 offset:44032\n"
    "s_waitcnt lgkmcnt(4)\n"
    "v_mfma_f32_16x16x32_bf16 v[160:163], v[240:243], v[0:3], 0\n"
    "ds_read_b64_tr_b16 v[236:237], v249 offset:43008\n"
    "v_mfma_f32_16x16x32_bf16 v[164:167], v[240:243], v[4:7], 0\n"
    "ds_read_b64_tr_b16 v[238:239], v249 offset:44032\n"
    "s_waitcnt lgkmcnt(4)\n"
    "v_mfma_f32_16x16x32_bf16 v[172:175], v[244:247], v[4:7], 0\n"
    "ds_read_b64_tr_b16 v[240:241], v248 offset:45056\n"
    "v_cvt_pk_bf16_f32 v144, v144, v145\n"
    "v_mfma_f32_16x16x32_bf16 v[168:171], v[244:247], v[0:3], 0\n"
    "ds_read_b64_tr_b16 v[242:243], v248 offset:46080\n"
    "s_waitcnt lgkmcnt(4)\n"
    "v_mfma_f32_16x16x32_bf16 v[160:163], v[232:235], v[8:11], v[160:163]\n"
    "s_waitcnt vmcnt(1)\n"
    "s_barrier\n"
    "ds_read_b64_tr_b16 v[244:245], v249 offset:45056\n"
    "v_mfma_f32_16x16x32_bf16 v[164:167], v[232:235], v[12:15], v[164:167]\n"
    "ds_read_b64_tr_b16 v[246:247], v249 offset:46080\n"
    "s_waitcnt lgkmcnt(4)\n"
    "v_mfma_f32_16x16x32_bf16 v[172:175], v[236:239], v[12:15], v[172:175]\n"
    "ds_read_b64_tr_b16 v[232:233], v248 offset:47104\n"
    "v_cvt_pk_bf16_f32 v145, v146, v147\n"
    "v_mfma_f32_16x16x32_bf16 v[168:171], v[236:239], v[8:11], v[168:171]\n"
    "ds_read_b64_tr_b16 v[234:235], v248 offset:48128\n"
    "s_waitcnt lgkmcnt(4)\n"
    "v_mfma_f32_16x16x32_bf16 v[160:163], v[240:243], v[16:19], v[160:163]\n"
    "ds_read_b64_tr_b16 v[236:237], v249 offset:47104\n"
    "v_cvt_pk_bf16_f32 v146, v152, v153\n"
    "v_mfma_f32_16x16x32_bf16 v[164:167], v[240:243], v[20:23], v[164:167]\n"
    "ds_read_b64_tr_b16 v[238:239], v249 offset:48128\n"
    "s_waitcnt lgkmcnt(4)\n"
    "v_mfma_f32_16x16x32_bf16 v[172:175], v[244:247], v[20:23], v[172:175]\n"
    "ds_read_b64_tr_b16 v[240:241], v248 offset:49152\n"
    "v_cvt_pk_bf16_f32 v147, v154, v155\n"
    "v_mfma_f32_16x16x32_bf16 v[168:171], v[244:247], v[16:19], v[168:171]\n"
    "ds_read_b64_tr_b16 v[242:243], v248 offset:50176\n"
    "s_waitcnt lgkmcnt(4)\n"
    "v_mfma_f32_16x16x32_bf16 v[160:163], v[232:235], v[24:27], v[160:163]\n"
    "ds_read_b64_tr_b16 v[244:245], v249 offset:49152\n"
    "v_cvt_pk_bf16_f32 v148, v148, v149\n"
    "v_mfma_f32_16x16x32_bf16 v[164:167], v[232:235], v[28:31], v[164:167]\n"
    "ds_read_b64_tr_b16 v[246:247], v249 offset:50176\n"
    "s_waitcnt lgkmcnt(4)\n"
    "v_mfma_f32_16x16x32_bf16 v[172:175], v[236:239], v[28:31], v[172:175]\n"
    "ds_read_b64_tr_b16 v[232:233], v248 offset:51200\n"
    "v_cvt_pk_bf16_f32 v149, v150, v151\n"
    "v_mfma_f32_16x16x32_bf16 v[168:171], v[236:239], v[24:27], v[168:171]\n"
    "ds_read_b64_tr_b16 v[234:235], v248 offset:52224\n"
    "s_waitcnt lgkmcnt(4)\n"
    "v_mfma_f32_16x16x32_bf16 v[160:163], v[240:243], v[32:35], v[160:163]\n"
    "ds_read_b64_tr_b16 v[236:237], v249 offset:51200\n"
    "v_cvt_pk_bf16_f32 v150, v156, v157\n"
    "v_mfma_f32_16x16x32_bf16 v[164:167], v[240:243], v[36:39], v[164:167]\n"
    "ds_read_b64_tr_b16 v[238:239], v249 offset:52224\n"
    "s_waitcnt lgkmcnt(4)\n"
    "v_mfma_f32_16x16x32_bf16 v[172:175], v[244:247], v[36:39], v[172:175]\n"
    "ds_read_b64_tr_b16 v[240:241], v248 offset:53248\n"
    "v_cvt_pk_bf16_f32 v151, v158, v159\n"
    "v_mfma_f32_16x16x32_bf16 v[168:171], v[244:247], v[32:35], v[168:171]\n"
    "ds_read_b64_tr_b16 v[242:243], v248 offset:54272\n"
    "s_waitcnt lgkmcnt(4)\n"
    "v_mfma_f32_16x16x32_bf16 v[160:163], v[232:235], v[40:43], v[160:163]\n"
    "ds_read_b64_tr_b16 v[244:245], v249 offset:53248\n"
    "v_pk_max_i16 v224, v224, 0\n"
    "v_mfma_f32_16x16x32_bf16 v[164:167], v[232:235], v[44:47], v[164:167]\n"
    "ds_read_b64_tr_b16 v[246:247], v249 offset:54272\n"
    "s_waitcnt lgkmcnt(4)\n"
    "v_mfma_f32_16x16x32_bf16 v[172:175], v[236:239], v[44:47], v[172:175]\n"
    "ds_read_b64_tr_b16 v[232:233], v248 offset:55296\n"
    "v_pk_max_i16 v225, v225, 0\n"
    "v_mfma_f32_16x16x32_bf16 v[168:171], v[236:239], v[40:43], v[168:171]\n"
    "ds_read_b64_tr_b16 v[234:235], v248 offset:56320\n"
    "s_waitcnt lgkmcnt(4)\n"
    "v_mfma_f32_16x16x32_bf16 v[160:163], v[240:243], v[48:51], v[160:163]\n"
    "ds_read_b64_tr_b16 v[236:237], v249 offset:55296\n"
    "v_pk_max_i16 v226, v226, 0\n"
    "v_mfma_f32_16x16x32_bf16 v[164:167], v[240:243], v[52:55], v[164:167]\n"
    "ds_read_b64_tr_b16 v[238:239], v249 offset:56320\n"
    "s_waitcnt lgkmcnt(4)\n"
    "v_mfma_f32_16x16x32_bf16 v[172:175], v[244:247], v[52:55], v[172:175]\n"
    "ds_read_b64_tr_b16 v[240:241], v248 offset:57344\n"
    "v_pk_max_i16 v227, v227, 0\n"
    "v_mfma_f32_16x16x32_bf16 v[168:171], v[244:247], v[48:51], v[168:171]\n"
    "ds_read_b64_tr_b16 v[242:243], v248 offset:58368\n"
    "s_waitcnt lgkmcnt(4)\n"
    "v_mfma_f32_16x16x32_bf16 v[160:163], v[232:235], v[56:59], v[160:163]\n"
    "ds_read_b64_tr_b16 v[244:245], v249 offset:57344\n"
    "v_pk_max_i16 v228, v228, 0\n"
    "v_mfma_f32_16x16x32_bf16 v[164:167], v[232:235], v[60:63], v[164:167]\n"
    "ds_read_b64_tr_b16 v[246:247], v249 offset:58368\n"
    "s_waitcnt lgkmcnt(4)\n"
    "v_mfma_f32_16x16x32_bf16 v[172:175], v[236:239], v[60:63], v[172:175]\n"
    "ds_read_b64_tr_b16 v[232:233], v248 offset:59392\n"
    "v_pk_max_i16 v229, v229, 0\n"
    "v_mfma_f32_16x16x32_bf16 v[168:171], v[236:239], v[56:59], v[168:171]\n"
    "ds_read_b64_tr_b16 v[234:235], v248 offset:60416\n"
    "s_waitcnt lgkmcnt(4)\n"
    "v_mfma_f32_16x16x32_bf16 v[160:163], v[240:243], v[64:67], v[160:163]\n"
    "ds_read_b64_tr_b16 v[236:237], v249 offset:59392\n"
    "v_pk_max_i16 v230, v230, 0\n"
    "v_mfma_f32_16x16x32_bf16 v[164:167], v[240:243], v[68:71], v[164:167]\n"
    "ds_read_b64_tr_b16 v[238:239], v249 offset:60416\n"
    "s_waitcnt lgkmcnt(4)\n"
    "v_mfma_f32_16x16x32_bf16 v[172:175], v[244:247], v[68:71], v[172:175]\n"
    "ds_read_b64_tr_b16 v[240:241], v248 offset:61440\n"
    "v_pk_max_i16 v231, v231, 0\n"
    "v_mfma_f32_16x16x32_bf16 v[168:171], v[244:247], v[64:67], v[168:171]\n"
    "ds_read_b64_tr_b16 v[242:243], v248 offset:62464\n"
    "s_waitcnt lgkmcnt(4)\n"
    "v_mfma_f32_16x16x32_bf16 v[160:163], v[232:235], v[72:75], v[160:163]\n"
    "ds_read_b64_tr_b16 v[244:245], v249 offset:61440\n"
    "v_pk_max_i16 v144, v144, 0\n"
    "v_mfma_f32_16x16x32_bf16 v[164:167], v[232:235], v[76:79], v[164:167]\n"
    "ds_read_b64_tr_b16 v[246:247], v249 offset:62464\n"
    "s_waitcnt lgkmcnt(4)\n"
    "v_mfma_f32_16x16x32_bf16 v[172:175], v[236:239], v[76:79], v[172:175]\n"
    "ds_read_b64_tr_b16 v[232:233], v248 offset:63488\n"
    "v_pk_max_i16 v145, v145, 0\n"
    "v_mfma_f32_16x16x32_bf16 v[168:171], v[236:239], v[72:75], v[168:171]\n"
    "ds_read_b64_tr_b16 v[234:235], v248 offset:64512\n"
    "s_waitcnt lgkmcnt(4)\n"
    "v_mfma_f32_16x16x32_bf16 v[160:163], v[240:243], v[80:83], v[160:163]\n"
    "s_barrier\n"
    "ds_read_b64_tr_b16 v[236:237], v249 offset:63488\n"
    "v_mfma_f32_16x16x32_bf16 v[164:167], v[240:243], v[84:87], v[164:167]\n"
    "ds_read_b64_tr_b16 v[238:239], v249 offset:64512\n"
    "s_waitcnt lgkmcnt(4)\n"
    "v_mfma_f32_16x16x32_bf16 v[172:175], v[244:247], v[84:87], v[172:175]\n"
    "v_add_u32 v248, 0x10000, v248\n"
    "ds_read_b64_tr_b16 v[240:241], v248 offset:0\n"
    "v_add_u32 v249, 0x10000, v249\n"
    "v_mfma_f32_16x16x32_bf16 v[168:171], v[244:247], v[80:83], v[168:171]\n"
    "ds_read_b64_tr_b16 v[242:243], v248 offset:1024\n"
    "s_waitcnt lgkmcnt(4)\n"
    "v_mfma_f32_16x16x32_bf16 v[160:163], v[232:235], v[88:91], v[160:163]\n"
    "ds_read_b64_tr_b16 v[244:245], v249 offset:0\n"
    "v_pk_max_i16 v146, v146, 0\n"
    "v_mfma_f32_16x16x32_bf16 v[164:167], v[232:235], v[92:95], v[164:167]\n"
    "ds_read_b64_tr_b16 v[246:247], v249 offset:1024\n"
    "s_waitcnt lgkmcnt(4)\n"
    "v_mfma_f32_16x16x32_bf16 v[172:175], v[236:239], v[92:95], v[172:175]\n"
    "ds_read_b64_tr_b16 v[232:233], v248 offset:2048\n"
    "v_pk_max_i16 v147, v147, 0\n"
    "v_mfma_f32_16x16x32_bf16 v[168:171], v[236:239], v[88:91], v[168:171]\n"
    "ds_read_b64_tr_b16 v[234:235], v248 offset:3072\n"
    "s_waitcnt lgkmcnt(4)\n"
    "v_mfma_f32_16x16x32_bf16 v[160:163], v[240:243], v[96:99], v[160:163]\n"
    "ds_read_b64_tr_b16 v[236:237], v249 offset:2048\n"
    "v_pk_max_i16 v148, v148, 0\n"
    "v_mfma_f32_16x16x32_bf16 v[164:167], v[240:243], v[100:103], v[164:167]\n"
    "ds_read_b64_tr_b16 v[238:239], v249 offset:3072\n"
    "s_waitcnt lgkmcnt(4)\n"
    "v_mfma_f32_16x16x32_bf16 v[172:175], v[244:247], v[100:103], v[172:175]\n"
    "ds_read_b64_tr_b16 v[240:241], v248 offset:4096\n"
    "v_pk_max_i16 v149, v149, 0\n"
    "v_mfma_f32_16x16x32_bf16 v[168:171], v[244:247], v[96:99], v[168:171]\n"
    "ds_read_b64_tr_b16 v[242:243], v248 offset:5120\n"
    "s_waitcnt lgkmcnt(4)\n"
    "v_mfma_f32_16x16x32_bf16 v[160:163], v[232:235], v[104:107], v[160:163]\n"
    "ds_read_b64_tr_b16 v[244:245], v249 offset:4096\n"
    "v_pk_max_i16 v150, v150, 0\n"
    "v_mfma_f32_16x16x32_bf16 v[164:167], v[232:235], v[108:111], v[164:167]\n"
    "ds_read_b64_tr_b16 v[246:247], v249 offset:5120\n"
    "s_waitcnt lgkmcnt(4)\n"
    "v_mfma_f32_16x16x32_bf16 v[172:175], v[236:239], v[108:111], v[172:175]\n"
    "ds_read_b64_tr_b16 v[232:233], v248 offset:6144\n"
    "v_pk_max_i16 v151, v151, 0\n"
    "v_mfma_f32_16x16x32_bf16 v[168:171], v[236:239], v[104:107], v[168:171]\n"
    "ds_read_b64_tr_b16 v[234:235], v248 offset:7168\n"
    "s_add_u32 s72, s72, 32768\n"
    "v_lshrrev_b32 v252, 9, v250\n"
    "v_lshlrev_b32 v252, 11, v252\n"
    "v_bfe_u32 v253, v250, 6, 3\n"
    "v_lshl_add_u32 v252, v253, 7, v252\n"
    "v_bfe_u32 v253, v250, 7, 2\n"
    "v_lshl_add_u32 v252, v253, 5, v252\n"
    "v_bfe_u32 v253, v250, 4, 2\n"
    "v_lshl_add_u32 v252, v253, 3, v252\n"
    "v_xor_b32 v253, 32, v252\n"
    "v_xor_b32 v254, 64, v252\n"
    "v_xor_b32 v255, 96, v252\n"
    "v_add_u32 v251, s67, v250\n"
    "s_waitcnt lgkmcnt(4)\n"
    "v_mfma_f32_16x16x32_bf16 v[160:163], v[240:243], v[112:115], v[160:163]\n"
    "ds_read_b64_tr_b16 v[236:237], v249 offset:6144\n"
    "v_mfma_f32_16x16x32_bf16 v[164:167], v[240:243], v[116:119], v[164:167]\n"
    "ds_read_b64_tr_b16 v[238:239], v249 offset:7168\n"
    "s_waitcnt lgkmcnt(4)\n"
    "v_mfma_f32_16x16x32_bf16 v[172:175], v[244:247], v[116:119], v[172:175]\n"
    "ds_read_b64_tr_b16 v[240:241], v248 offset:8192\n"
    "v_mfma_f32_16x16x32_bf16 v[168:171], v[244:247], v[112:115], v[168:171]\n"
    "ds_read_b64_tr_b16 v[242:243], v248 offset:9216\n"
    "s_waitcnt lgkmcnt(4)\n"
    "v_mfma_f32_16x16x32_bf16 v[160:163], v[232:235], v[120:123], v[160:163]\n"
    "ds_read_b64_tr_b16 v[244:245], v249 offset:8192\n"
    "v_add_u32 v252, 0x13000, v252\n"
    "v_mfma_f32_16x16x32_bf16 v[164:167], v[232:235], v[124:127], v[164:167]\n"
    "ds_read_b64_tr_b16 v[246:247], v249 offset:9216\n"
    "s_waitcnt lgkmcnt(4)\n"
    "v_mfma_f32_16x16x32_bf16 v[172:175], v[236:239], v[124:127], v[172:175]\n"
    "ds_read_b64_tr_b16 v[232:233], v248 offset:10240\n"
    "v_add_u32 v253, 0x13000, v253\n"
    "v_mfma_f32_16x16x32_bf16 v[168:171], v[236:239], v[120:123], v[168:171]\n"
    "ds_read_b64_tr_b16 v[234:235], v248 offset:11264\n"
    "s_waitcnt lgkmcnt(4)\n"
    "v_mfma_f32_16x16x32_bf16 v[160:163], v[240:243], v[128:131], v[160:163]\n"
    "ds_read_b64_tr_b16 v[236:237], v249 offset:10240\n"
    "v_add_u32 v254, 0x13000, v254\n"
    "v_mfma_f32_16x16x32_bf16 v[164:167], v[240:243], v[132:135], v[164:167]\n"
    "ds_read_b64_tr_b16 v[238:239], v249 offset:11264\n"
    "s_waitcnt lgkmcnt(4)\n"
    "v_mfma_f32_16x16x32_bf16 v[172:175], v[244:247], v[132:135], v[172:175]\n"
    "ds_read_b64_tr_b16 v[240:241], v252 offset:0\n"
    "buffer_store_dwordx4 v[224:227], v250, s[60:63], s68 offen offset:0 sc1\n"
    "v_mfma_f32_16x16x32_bf16 v[168:171], v[244:247], v[128:131], v[168:171]\n"
    "ds_read_b64_tr_b16 v[242:243], v252 offset:4096\n"
    "s_waitcnt lgkmcnt(4)\n"
    "v_mfma_f32_16x16x32_bf16 v[160:163], v[232:235], v[136:139], v[160:163]\n"
    "ds_read_b64_tr_b16 v[244:245], v253 offset:0\n"
    "buffer_store_dwordx4 v[228:231], v250, s[60:63], s68 offen offset:1024 sc1\n"
    "v_mfma_f32_16x16x32_bf16 v[164:167], v[232:235], v[140:143], v[164:167]\n"
    "ds_read_b64_tr_b16 v[246:247], v253 offset:4096\n"
    "s_waitcnt lgkmcnt(4)\n"
    "v_mfma_f32_16x16x32_bf16 v[172:175], v[236:239], v[140:143], v[172:175]\n"
    "ds_read_b64_tr_b16 v[232:233], v254 offset:0\n"
    "buffer_store_dwordx4 v[144:147], v250, s[60:63], s68 offen offset:2048 sc1\n"
    "v_mfma_f32_16x16x32_bf16 v[168:171], v[236:239], v[136:139], v[168:171]\n"
    "ds_read_b64_tr_b16 v[234:235], v254 offset:4096\n"
    "s_waitcnt lgkmcnt(4)\n"
    "v_mfma_f32_16x16x32_bf16 v[0:3], v[240:243], v[176:179], 0\n"
    "v_add_u32 v255, 0x13000, v255\n"
    "ds_read_b64_tr_b16 v[236:237], v255 offset:0\n"
    "buffer_store_dwordx4 v[148:151], v250, s[60:63], s68 offen offset:3072 sc1\n"
    "v_add_u32 v251, 0x15000, v251\n"
    "ds_read_b128 v[96:99], v251 offset:0\n"
    "v_mfma_f32_16x16x32_bf16 v[4:7], v[240:243], v[180:183], 0\n"
    "ds_read_b64_tr_b16 v[238:239], v255 offset:4096\n"
    "s_waitcnt lgkmcnt(5)\n"
    "v_mfma_f32_16x16x32_bf16 v[12:15], v[244:247], v[180:183], 0\n"
    "ds_read_b64_tr_b16 v[240:241], v252 offset:1024\n"
    "ds_read_b128 v[100:103], v251 offset:1024\n"
    "v_mfma_f32_16x16x32_bf16 v[8:11], v[244:247], v[176:179], 0\n"
    "ds_read_b64_tr_b16 v[242:243], v252 offset:5120\n"
    "s_waitcnt lgkmcnt(6)\n"
    "v_mfma_f32_16x16x32_bf16 v[16:19], v[232:235], v[176:179], 0\n"
    "s_barrier\n"
    "ds_read_b64_tr_b16 v[244:245], v253 offset:1024\n"
    "v_mfma_f32_16x16x32_bf16 v[20:23], v[232:235], v[180:183], 0\n"
    "ds_read_b64_tr_b16 v[246:247], v253 offset:5120\n"
    "s_waitcnt lgkmcnt(5)\n"
    "v_mfma_f32_16x16x32_bf16 v[28:31], v[236:239], v[180:183], 0\n"
    "ds_read_b64_tr_b16 v[120:121], v254 offset:1024\n"
    "v_cvt_pk_bf16_f32 v152, v160, v161\n"
    "v_mfma_f32_16x16x32_bf16 v[24:27], v[236:239], v[176:179], 0\n"
    "ds_read_b64_tr_b16 v[122:123], v254 offset:5120\n"
    "ds_read_b64_tr_b16 v[124:125], v255 offset:1024\n"
    "ds_read_b64_tr_b16 v[126:127], v255 offset:5120\n"
    "s_waitcnt lgkmcnt(6)\n"
    "v_mfma_f32_16x16x32_bf16 v[32:35], v[240:243], v[176:179], 0\n"
    "ds_read_b64_tr_b16 v[128:129], v252 offset:16384\n"
    "v_cvt_pk_bf16_f32 v153, v162, v163\n"
    "v_mfma_f32_16x16x32_bf16 v[36:39], v[240:243], v[180:183], 0\n"
    "ds_read_b64_tr_b16 v[130:131], v252 offset:20480\n"
    "s_waitcnt lgkmcnt(6)\n"
    "v_mfma_f32_16x16x32_bf16 v[44:47], v[244:247], v[180:183], 0\n"
    "ds_read_b64_tr_b16 v[132:133], v253 offset:16384\n"
    "v_cvt_pk_bf16_f32 v154, v168, v169\n"
    "v_mfma_f32_16x16x32_bf16 v[40:43], v[244:247], v[176:179], 0\n"
    "ds_read_b64_tr_b16 v[134:135], v253 offset:20480\n"
    "s_waitcnt lgkmcnt(6)\n"
    "v_mfma_f32_16x16x32_bf16 v[48:51], v[120:123], v[176:179], 0\n"
    "ds_read_b64_tr_b16 v[232:233], v254 offset:16384\n"
    "v_cvt_pk_bf16_f32 v155, v170, v171\n"
    "v_mfma_f32_16x16x32_bf16 v[52:55], v[120:123], v[180:183], 0\n"
    "ds_read_b64_tr_b16 v[234:235], v254 offset:20480\n"
    "ds_read_b64_tr_b16 v[236:237], v255 offset:16384\n"
    "ds_read_b64_tr_b16 v[238:239], v255 offset:20480\n"
    "s_waitcnt lgkmcnt(8)\n"
    "v_mfma_f32_16x16x32_bf16 v[60:63], v[124:127], v[180:183], 0\n"
    "ds_read_b64_tr_b16 v[240:241], v252 offset:17408\n"
    "v_cvt_pk_bf16_f32 v156, v164, v165\n"
    "v_mfma_f32_16x16x32_bf16 v[56:59], v[124:127], v[176:179], 0\n"
    "ds_read_b64_tr_b16 v[242:243], v252 offset:21504\n"
    "s_waitcnt lgkmcnt(8)\n"
    "v_mfma_f32_16x16x32_bf16 v[0:3], v[128:131], v[96:99], v[0:3]\n"
    "ds_read_b64_tr_b16 v[244:245], v253 offset:17408\n"
    "v_add_u32 v252, 0x5000, v252\n"
    "v_mfma_f32_16x16x32_bf16 v[4:7], v[128:131], v[100:103], v[4:7]\n"
    "ds_read_b64_tr_b16 v[246:247], v253 offset:21504\n"
    "s_waitcnt lgkmcnt(8)\n"
    "v_mfma_f32_16x16x32_bf16 v[12:15], v[132:135], v[100:103], v[12:15]\n"
    "ds_read_b64_tr_b16 v[120:121], v254 offset:17408\n"
    "v_add_u32 v253, 0x5000, v253\n"
    "v_mfma_f32_16x16x32_bf16 v[8:11], v[132:135], v[96:99], v[8:11]\n"
    "ds_read_b64_tr_b16 v[122:123], v254 offset:21504\n"
    "ds_read_b64_tr_b16 v[124:125], v255 offset:17408\n"
    "ds_read_b64_tr_b16 v[126:127], v255 offset:21504\n"
    "s_waitcnt lgkmcnt(10)\n"
    "v_mfma_f32_16x16x32_bf16 v[16:19], v[232:235], v[96:99], v[16:19]\n"
    "ds_read_b64_tr_b16 v[128:129], v252 offset:46080\n"
    "v_add_u32 v254, 0x5000, v254\n"
    "v_mfma_f32_16x16x32_bf16 v[20:23], v[232:235], v[100:103], v[20:23]\n"
    "ds_read_b64_tr_b16 v[130:131], v252 offset:50176\n"
    "s_waitcnt lgkmcnt(10)\n"
    "v_mfma_f32_16x16x32_bf16 v[28:31], v[236:239], v[100:103], v[28:31]\n"
    "ds_read_b64_tr_b16 v[132:133], v253 offset:46080\n"
    "v_add_u32 v255, 0x5000, v255\n"
    "v_mfma_f32_16x16x32_bf16 v[24:27], v[236:239], v[96:99], v[24:27]\n"
    "ds_read_b64_tr_b16 v[134:135], v253 offset:50176\n"
    "s_waitcnt lgkmcnt(10)\n"
    "v_mfma_f32_16x16x32_bf16 v[32:35], v[240:243], v[96:99], v[32:35]\n"
    "ds_read_b64_tr_b16 v[232:233], v254 offset:46080\n"
    "v_cvt_pk_bf16_f32 v157, v166, v167\n"
    "v_mfma_f32_16x16x32_bf16 v[36:39], v[240:243], v[100:103], v[36:39]\n"
    "ds_read_b64_tr_b16 v[234:235], v254 offset:50176\n"
    "s_waitcnt lgkmcnt(10)\n"
    "v_mfma_f32_16x16x32_bf16 v[44:47], v[244:247], v[100:103], v[44:47]\n"
    "ds_read_b64_tr_b16 v[236:237], v255 offset:46080\n"
    "v_add_u32 v251, 0x3000, v251\n"
    "v_mfma_f32_16x16x32_bf16 v[40:43], v[244:247], v[96:99], v[40:43]\n"
    "ds_read_b64_tr_b16 v[238:239], v255 offset:50176\n"
    "s_waitcnt lgkmcnt(10)\n"
    "v_mfma_f32_16x16x32_bf16 v[48:51], v[120:123], v[96:99], v[48:51]\n"
    "s_barrier\n"
    "ds_read_b64_tr_b16 v[240:241], v252 offset:47104\n"
    "v_mfma_f32_16x16x32_bf16 v[52:55], v[120:123], v[100:103], v[52:55]\n"
    "ds_read_b64_tr_b16 v[242:243], v252 offset:51200\n"
    "s_waitcnt lgkmcnt(10)\n"
    "v_mfma_f32_16x16x32_bf16 v[60:63], v[124:127], v[100:103], v[60:63]\n"
    "ds_read_b64_tr_b16 v[244:245], v253 offset:47104\n"
    "v_add_u32 v252, 0xfffe8000, v252\n"
    "v_mfma_f32_16x16x32_bf16 v[56:59], v[124:127], v[96:99], v[56:59]\n"
    "ds_read_b64_tr_b16 v[246:247], v253 offset:51200\n"
    "s_waitcnt lgkmcnt(10)\n"
    "v_mfma_f32_16x16x32_bf16 v[64:67], v[128:131], v[176:179], 0\n"
    "ds_read_b64_tr_b16 v[120:121], v254 offset:47104\n"
    "ds_read_b128 v[104:107], v251 offset:54272\n"
    "v_mfma_f32_16x16x32_bf16 v[68:71], v[128:131], v[180:183], 0\n"
    "ds_read_b64_tr_b16 v[122:123], v254 offset:51200\n"
    "s_waitcnt lgkmcnt(11)\n"
    "v_mfma_f32_16x16x32_bf16 v[76:79], v[132:135], v[180:183], 0\n"
    "ds_read_b64_tr_b16 v[124:125], v255 offset:47104\n"
    "ds_read_b128 v[108:111], v251 offset:55296\n"
    "v_mfma_f32_16x16x32_bf16 v[72:75], v[132:135], v[176:179], 0\n"
    "ds_read_b64_tr_b16 v[126:127], v255 offset:51200\n"
    "s_waitcnt lgkmcnt(12)\n"
    "v_mfma_f32_16x16x32_bf16 v[80:83], v[232:235], v[176:179], 0\n"
    "ds_read_b64_tr_b16 v[128:129], v252 offset:0\n"
    "v_add_u32 v253, 0xfffe8000, v253\n"
    "v_mfma_f32_16x16x32_bf16 v[84:87], v[232:235], v[180:183], 0\n"
    "ds_read_b64_tr_b16 v[130:131], v252 offset:4096\n"
    "s_waitcnt lgkmcnt(12)\n"
    "v_mfma_f32_16x16x32_bf16 v[92:95], v[236:239], v[180:183], 0\n"
    "ds_read_b64_tr_b16 v[132:133], v253 offset:0\n"
    "v_add_u32 v254, 0xfffe8000, v254\n"
    "v_mfma_f32_16x16x32_bf16 v[88:91], v[236:239], v[176:179], 0\n"
    "ds_read_b64_tr_b16 v[134:135], v253 offset:4096\n"
    "s_waitcnt lgkmcnt(12)\n"
    "v_mfma_f32_16x16x32_bf16 v[64:67], v[240:243], v[184:187], v[64:67]\n"
    "ds_read_b64_tr_b16 v[232:233], v254 offset:0\n"
    "v_add_u32 v255, 0xfffe8000, v255\n"
    "v_mfma_f32_16x16x32_bf16 v[68:71], v[240:243], v[188:191], v[68:71]\n"
    "ds_read_b64_tr_b16 v[234:235], v254 offset:4096\n"
    "s_waitcnt lgkmcnt(12)\n"
    "v_mfma_f32_16x16x32_bf16 v[76:79], v[244:247], v[188:191], v[76:79]\n"
    "ds_read_b64_tr_b16 v[236:237], v255 offset:0\n"
    "v_cvt_pk_bf16_f32 v158, v172, v173\n"
    "v_mfma_f32_16x16x32_bf16 v[72:75], v[244:247], v[184:187], v[72:75]\n"
    "s_nop 0\n"
    "L_st6_%=:\n"
    "s_cbranch_execz L_bk%=\n"
    "ds_read_b64_tr_b16 v[238:239], v255 offset:4096\n"
    "s_waitcnt lgkmcnt(11)\n"
    "v_mfma_f32_16x16x32_bf16 v[80:83], v[120:123], v[184:187], v[80:83]\n"
    "ds_read_b64_tr_b16 v[240:241], v252 offset:1024\n"
    "v_cvt_pk_bf16_f32 v159, v174, v175\n"
    "v_mfma_f32_16x16x32_bf16 v[84:87], v[120:123], v[188:191], v[84:87]\n"
    "ds_read_b64_tr_b16 v[242:243], v252 offset:5120\n"
    "v_pk_max_i16 v152, v152, 0\n"
    "v_pk_max_i16 v153, v153, 0\n"
    "v_pk_max_i16 v154, v154, 0\n"
    "v_pk_max_i16 v155, v155, 0\n"
    "v_pk_max_i16 v156, v156, 0\n"
    "v_pk_max_i16 v157, v157, 0\n"
    "v_pk_max_i16 v158, v158, 0\n"
    "v_pk_max_i16 v159, v159, 0\n"
    "s_waitcnt lgkmcnt(10)\n"
    "v_mfma_f32_16x16x32_bf16 v[92:95], v[124:127], v[188:191], v[92:95]\n"
    "ds_read_b64_tr_b16 v[244:245], v253 offset:1024\n"
    "buffer_store_dwordx4 v[152:155], v250, s[60:63], s72 offen offset:0 sc1\n"
    "v_mfma_f32_16x16x32_bf16 v[88:91], v[124:127], v[184:187], v[88:91]\n"
    "ds_read_b64_tr_b16 v[246:247], v253 offset:5120\n"
    "s_waitcnt lgkmcnt(10)\n"
    "v_mfma_f32_16x16x32_bf16 v[0:3], v[128:131], v[104:107], v[0:3]\n"
    "ds_read_b64_tr_b16 v[120:121], v254 offset:1024\n"
    "buffer_store_dwordx4 v[156:159], v250, s[60:63], s72 offen offset:1024 sc1\n"
    "v_mfma_f32_16x16x32_bf16 v[4:7], v[128:131], v[108:111], v[4:7]\n"
    "ds_read_b64_tr_b16 v[122:123], v254 offset:5120\n"
    "s_waitcnt lgkmcnt(10)\n"
    "v_mfma_f32_16x16x32_bf16 v[12:15], v[132:135], v[108:111], v[12:15]\n"
    "ds_read_b64_tr_b16 v[124:125], v255 offset:1024\n"
    "v_mfma_f32_16x16x32_bf16 v[8:11], v[132:135], v[104:107], v[8:11]\n"
    "ds_read_b64_tr_b16 v[126:127], v255 offset:5120\n"
    "s_waitcnt lgkmcnt(10)\n"
    "v_mfma_f32_16x16x32_bf16 v[16:19], v[232:235], v[104:107], v[16:19]\n"
    "s_barrier\n"
    "ds_read_b64_tr_b16 v[128:129], v252 offset:8192\n"
    "v_mfma_f32_16x16x32_bf16 v[20:23], v[232:235], v[108:111], v[20:23]\n"
    "ds_read_b64_tr_b16 v[130:131], v252 offset:12288\n"
    "s_waitcnt lgkmcnt(10)\n"
    "v_mfma_f32_16x16x32_bf16 v[28:31], v[236:239], v[108:111], v[28:31]\n"
    "ds_read_b64_tr_b16 v[132:133], v253 offset:8192\n"
    "v_mfma_f32_16x16x32_bf16 v[24:27], v[236:239], v[104:107], v[24:27]\n"
    "ds_read_b64_tr_b16 v[134:135], v253 offset:12288\n"
    "s_waitcnt lgkmcnt(10)\n"
    "v_mfma_f32_16x16x32_bf16 v[32:35], v[240:243], v[104:107], v[32:35]\n"
    "ds_read_b64_tr_b16 v[232:233], v254 offset:8192\n"
    "v_mfma_f32_16x16x32_bf16 v[36:39], v[240:243], v[108:111], v[36:39]\n"
    "ds_read_b64_tr_b16 v[234:235], v254 offset:12288\n"
    "s_waitcnt lgkmcnt(10)\n"
    "v_mfma_f32_16x16x32_bf16 v[44:47], v[244:247], v[108:111], v[44:47]\n"
    "ds_read_b64_tr_b16 v[236:237], v255 offset:8192\n"
    "v_add_u32 v251, 0xfffec000, v251\n"
    "v_mfma_f32_16x16x32_bf16 v[40:43], v[244:247], v[104:107], v[40:43]\n"
    "ds_read_b64_tr_b16 v[238:239], v255 offset:12288\n"
    "s_waitcnt lgkmcnt(10)\n"
    "v_mfma_f32_16x16x32_bf16 v[48:51], v[120:123], v[104:107], v[48:51]\n"
    "ds_read_b64_tr_b16 v[240:241], v252 offset:9216\n"
    "v_lshlrev_b32 v162, 6, v250\n"
    "v_mfma_f32_16x16x32_bf16 v[52:55], v[120:123], v[108:111], v[52:55]\n"
    "ds_read_b64_tr_b16 v[242:243], v252 offset:13312\n"
    "s_waitcnt lgkmcnt(10)\n"
    "v_mfma_f32_16x16x32_bf16 v[60:63], v[124:127], v[108:111], v[60:63]\n"
    "ds_read_b64_tr_b16 v[244:245], v253 offset:9216\n"
    "v_add_u32 v162, s48, v162\n"
    "v_mfma_f32_16x16x32_bf16 v[56:59], v[124:127], v[104:107], v[56:59]\n"
    "ds_read_b64_tr_b16 v[246:247], v253 offset:13312\n"
    "s_waitcnt lgkmcnt(10)\n"
    "v_mfma_f32_16x16x32_bf16 v[64:67], v[128:131], v[192:195], v[64:67]\n"
    "ds_read_b64_tr_b16 v[120:121], v254 offset:9216\n"
    "ds_read_b128 v[112:115], v251 offset:0\n"
    "v_mfma_f32_16x16x32_bf16 v[68:71], v[128:131], v[196:199], v[68:71]\n"
    "ds_read_b64_tr_b16 v[122:123], v254 offset:13312\n"
    "s_waitcnt lgkmcnt(11)\n"
    "v_mfma_f32_16x16x32_bf16 v[76:79], v[132:135], v[196:199], v[76:79]\n"
    "ds_read_b64_tr_b16 v[124:125], v255 offset:9216\n"
    "ds_read_b128 v[116:119], v251 offset:1024\n"
    "v_mfma_f32_16x16x32_bf16 v[72:75], v[132:135], v[192:195], v[72:75]\n"
    "ds_read_b64_tr_b16 v[126:127], v255 offset:13312\n"
    "s_waitcnt lgkmcnt(12)\n"
    "v_mfma_f32_16x16x32_bf16 v[80:83], v[232:235], v[192:195], v[80:83]\n"
    "ds_read_b64_tr_b16 v[128:129], v252 offset:24576\n"
    "s_mov_b64 exec, s[52:53]\n"
    "buffer_load_dwordx2 v[160:161], v162, s[60:63], 0 offen sc1\n"
    "s_mov_b64 exec, -1\n"
    "v_mfma_f32_16x16x32_bf16 v[84:87], v[232:235], v[196:199], v[84:87]\n"
    "ds_read_b64_tr_b16 v[130:131], v252 offset:28672\n"
    "s_waitcnt lgkmcnt(12)\n"
    "v_mfma_f32_16x16x32_bf16 v[92:95], v[236:239], v[196:199], v[92:95]\n"
    "ds_read_b64_tr_b16 v[132:133], v253 offset:24576\n"
    "v_lshrrev_b32 v248, 6, v250\n"
    "v_mfma_f32_16x16x32_bf16 v[88:91], v[236:239], v[192:195], v[88:91]\n"
    "ds_read_b64_tr_b16 v[134:135], v253 offset:28672\n"
    "s_waitcnt lgkmcnt(12)\n"
    "v_mfma_f32_16x16x32_bf16 v[64:67], v[240:243], v[200:203], v[64:67]\n"
    "ds_read_b64_tr_b16 v[232:233], v254 offset:24576\n"
    "v_mul_u32_u24 v248, 288, v248\n"
    "v_mfma_f32_16x16x32_bf16 v[68:71], v[240:243], v[204:207], v[68:71]\n"
    "ds_read_b64_tr_b16 v[234:235], v254 offset:28672\n"
    "s_waitcnt lgkmcnt(12)\n"
    "v_mfma_f32_16x16x32_bf16 v[76:79], v[244:247], v[204:207], v[76:79]\n"
    "ds_read_b64_tr_b16 v[236:237], v255 offset:24576\n"
    "v_bfe_u32 v249, v250, 4, 2\n"
    "v_mfma_f32_16x16x32_bf16 v[72:75], v[244:247], v[200:203], v[72:75]\n"
    "ds_read_b64_tr_b16 v[238:239], v255 offset:28672\n"
    "s_waitcnt lgkmcnt(11)\n"
    "v_mfma_f32_16x16x32_bf16 v[80:83], v[120:123], v[200:203], v[80:83]\n"
    "s_barrier\n"
    "ds_read_b64_tr_b16 v[240:241], v252 offset:25600\n"
    "v_mfma_f32_16x16x32_bf16 v[84:87], v[120:123], v[204:207], v[84:87]\n"
    "ds_read_b64_tr_b16 v[242:243], v252 offset:29696\n"
    "s_waitcnt lgkmcnt(10)\n"
    "v_mfma_f32_16x16x32_bf16 v[92:95], v[124:127], v[204:207], v[92:95]\n"
    "ds_read_b64_tr_b16 v[244:245], v253 offset:25600\n"
    "v_lshl_add_u32 v248, v249, 3, v248\n"
    "v_mfma_f32_16x16x32_bf16 v[88:91], v[124:127], v[200:203], v[88:91]\n"
    "ds_read_b64_tr_b16 v[246:247], v253 offset:29696\n"
    "s_waitcnt lgkmcnt(10)\n"
    "v_mfma_f32_16x16x32_bf16 v[0:3], v[128:131], v[112:115], v[0:3]\n"
    "ds_read_b64_tr_b16 v[120:121], v254 offset:25600\n"
    "v_add_u32 v249, s67, v250\n"
    "v_mfma_f32_16x16x32_bf16 v[4:7], v[128:131], v[116:119], v[4:7]\n"
    "ds_read_b64_tr_b16 v[122:123], v254 offset:29696\n"
    "s_waitcnt lgkmcnt(10)\n"
    "v_mfma_f32_16x16x32_bf16 v[12:15], v[132:135], v[116:119], v[12:15]\n"
    "ds_read_b64_tr_b16 v[124:125], v255 offset:25600\n"
    "v_lshrrev_b32 v249, 1, v249\n"
    "v_mfma_f32_16x16x32_bf16 v[8:11], v[132:135], v[112:115], v[8:11]\n"
    "ds_read_b64_tr_b16 v[126:127], v255 offset:29696\n"
    "s_waitcnt lgkmcnt(10)\n"
    "v_mfma_f32_16x16x32_bf16 v[16:19], v[232:235], v[112:115], v[16:19]\n"
    "ds_read_b64_tr_b16 v[128:129], v252 offset:32768\n"
    "v_mfma_f32_16x16x32_bf16 v[20:23], v[232:235], v[116:119], v[20:23]\n"
    "ds_read_b64_tr_b16 v[130:131], v252 offset:36864\n"
    "s_waitcnt lgkmcnt(10)\n"
    "v_mfma_f32_16x16x32_bf16 v[28:31], v[236:239], v[116:119], v[28:31]\n"
    "ds_read_b64_tr_b16 v[132:133], v253 offset:32768\n"
    "v_mfma_f32_16x16x32_bf16 v[24:27], v[236:239], v[112:115], v[24:27]\n"
    "ds_read_b64_tr_b16 v[134:135], v253 offset:36864\n"
    "s_waitcnt lgkmcnt(10)\n"
    "v_mfma_f32_16x16x32_bf16 v[32:35], v[240:243], v[112:115], v[32:35]\n"
    "ds_read_b64_tr_b16 v[232:233], v254 offset:32768\n"
    "v_mfma_f32_16x16x32_bf16 v[36:39], v[240:243], v[116:119], v[36:39]\n"
    "ds_read_b64_tr_b16 v[234:235], v254 offset:36864\n"
    "s_waitcnt lgkmcnt(10)\n"
    "v_mfma_f32_16x16x32_bf16 v[44:47], v[244:247], v[116:119], v[44:47]\n"
    "ds_read_b64_tr_b16 v[236:237], v255 offset:32768\n"
    "v_mfma_f32_16x16x32_bf16 v[40:43], v[244:247], v[112:115], v[40:43]\n"
    "ds_read_b64_tr_b16 v[238:239], v255 offset:36864\n"
    "s_waitcnt lgkmcnt(10)\n"
    "v_mfma_f32_16x16x32_bf16 v[48:51], v[120:123], v[112:115], v[48:51]\n"
    "ds_read_b64_tr_b16 v[240:241], v252 offset:33792\n"
    "v_mfma_f32_16x16x32_bf16 v[52:55], v[120:123], v[116:119], v[52:55]\n"
    "ds_read_b64_tr_b16 v[242:243], v252 offset:37888\n"
    "s_waitcnt lgkmcnt(10)\n"
    "v_mfma_f32_16x16x32_bf16 v[60:63], v[124:127], v[116:119], v[60:63]\n"
    "ds_read_b64_tr_b16 v[244:245], v253 offset:33792\n"
    "v_mfma_f32_16x16x32_bf16 v[56:59], v[124:127], v[112:115], v[56:59]\n"
    "ds_read_b64_tr_b16 v[246:247], v253 offset:37888\n"
    "s_waitcnt lgkmcnt(10)\n"
    "v_mfma_f32_16x16x32_bf16 v[64:67], v[128:131], v[208:211], v[64:67]\n"
    "ds_read_b64_tr_b16 v[120:121], v254 offset:33792\n"
    "ds_read_b128 v[96:99], v251 offset:24576\n"
    "v_mfma_f32_16x16x32_bf16 v[68:71], v[128:131], v[212:215], v[68:71]\n"
    "ds_read_b64_tr_b16 v[122:123], v254 offset:37888\n"
    "s_waitcnt lgkmcnt(11)\n"
    "v_mfma_f32_16x16x32_bf16 v[76:79], v[132:135], v[212:215], v[76:79]\n"
    "ds_read_b64_tr_b16 v[124:125], v255 offset:33792\n"
    "ds_read_b128 v[100:103], v251 offset:25600\n"
    "v_mfma_f32_16x16x32_bf16 v[72:75], v[132:135], v[208:211], v[72:75]\n"
    "ds_read_b64_tr_b16 v[126:127], v255 offset:37888\n"
    "s_waitcnt lgkmcnt(12)\n"
    "v_mfma_f32_16x16x32_bf16 v[80:83], v[232:235], v[208:211], v[80:83]\n"
    "s_barrier\n"
    "ds_read_b64_tr_b16 v[128:129], v252 offset:49152\n"
    "v_mfma_f32_16x16x32_bf16 v[84:87], v[232:235], v[212:215], v[84:87]\n"
    "ds_read_b64_tr_b16 v[130:131], v252 offset:53248\n"
    "s_waitcnt lgkmcnt(12)\n"
    "v_mfma_f32_16x16x32_bf16 v[92:95], v[236:239], v[212:215], v[92:95]\n"
    "ds_read_b64_tr_b16 v[132:133], v253 offset:49152\n"
    "s_waitcnt vmcnt(0)\n"
    "v_add_co_u32 v160, vcc, 1, v160\n"
    "v_mfma_f32_16x16x32_bf16 v[88:91], v[236:239], v[208:211], v[88:91]\n"
    "ds_read_b64_tr_b16 v[134:135], v253 offset:53248\n"
    "s_waitcnt lgkmcnt(12)\n"
    "v_mfma_f32_16x16x32_bf16 v[64:67], v[240:243], v[216:219], v[64:67]\n"
    "ds_read_b64_tr_b16 v[232:233], v254 offset:49152\n"
    "v_addc_co_u32 v161, vcc, 0, v161, vcc\n"
    "v_mfma_f32_16x16x32_bf16 v[68:71], v[240:243], v[220:223], v[68:71]\n"
    "ds_read_b64_tr_b16 v[234:235], v254 offset:53248\n"
    "s_waitcnt lgkmcnt(12)\n"
    "v_mfma_f32_16x16x32_bf16 v[76:79], v[244:247], v[220:223], v[76:79]\n"
    "ds_read_b64_tr_b16 v[236:237], v255 offset:49152\n"
    "v_mfma_f32_16x16x32_bf16 v[72:75], v[244:247], v[216:219], v[72:75]\n"
    "ds_read_b64_tr_b16 v[238:239], v255 offset:53248\n"
    "s_waitcnt lgkmcnt(11)\n"
    "v_mfma_f32_16x16x32_bf16 v[80:83], v[120:123], v[216:219], v[80:83]\n"
    "ds_read_b64_tr_b16 v[240:241], v252 offset:50176\n"
    "v_mfma_f32_16x16x32_bf16 v[84:87], v[120:123], v[220:223], v[84:87]\n"
    "ds_read_b64_tr_b16 v[242:243], v252 offset:54272\n"
    "s_waitcnt lgkmcnt(10)\n"
    "v_mfma_f32_16x16x32_bf16 v[92:95], v[124:127], v[220:223], v[92:95]\n"
    "ds_read_b64_tr_b16 v[244:245], v253 offset:50176\n"
    "v_mfma_f32_16x16x32_bf16 v[88:91], v[124:127], v[216:219], v[88:91]\n"
    "ds_read_b64_tr_b16 v[246:247], v253 offset:54272\n"
    "s_waitcnt lgkmcnt(10)\n"
    "v_mfma_f32_16x16x32_bf16 v[0:3], v[128:131], v[96:99], v[0:3]\n"
    "ds_read_b64_tr_b16 v[120:121], v254 offset:50176\n"
    "v_mfma_f32_16x16x32_bf16 v[4:7], v[128:131], v[100:103], v[4:7]\n"
    "ds_read_b64_tr_b16 v[122:123], v254 offset:54272\n"
    "s_waitcnt lgkmcnt(10)\n"
    "v_mfma_f32_16x16x32_bf16 v[12:15], v[132:135], v[100:103], v[12:15]\n"
    "ds_read_b64_tr_b16 v[124:125], v255 offset:50176\n"
    "v_mfma_f32_16x16x32_bf16 v[8:11], v[132:135], v[96:99], v[8:11]\n"
    "ds_read_b64_tr_b16 v[126:127], v255 offset:54272\n"
    "s_waitcnt lgkmcnt(10)\n"
    "v_mfma_f32_16x16x32_bf16 v[16:19], v[232:235], v[96:99], v[16:19]\n"
    "ds_read_b64_tr_b16 v[128:129], v252 offset:57344\n"
    "v_mfma_f32_16x16x32_bf16 v[20:23], v[232:235], v[100:103], v[20:23]\n"
    "ds_read_b64_tr_b16 v[130:131], v252 offset:61440\n"
    "s_waitcnt lgkmcnt(10)\n"
    "v_mfma_f32_16x16x32_bf16 v[28:31], v[236:239], v[100:103], v[28:31]\n"
    "ds_read_b64_tr_b16 v[132:133], v253 offset:57344\n"
    "v_mfma_f32_16x16x32_bf16 v[24:27], v[236:239], v[96:99], v[24:27]\n"
    "ds_read_b64_tr_b16 v[134:135], v253 offset:61440\n"
    "s_waitcnt lgkmcnt(10)\n"
    "v_mfma_f32_16x16x32_bf16 v[32:35], v[240:243], v[96:99], v[32:35]\n"
    "ds_read_b64_tr_b16 v[232:233], v254 offset:57344\n"
    "v_mfma_f32_16x16x32_bf16 v[36:39], v[240:243], v[100:103], v[36:39]\n"
    "ds_read_b64_tr_b16 v[234:235], v254 offset:61440\n"
    "s_waitcnt lgkmcnt(10)\n"
    "v_mfma_f32_16x16x32_bf16 v[44:47], v[244:247], v[100:103], v[44:47]\n"
    "ds_read_b64_tr_b16 v[236:237], v255 offset:57344\n"
    "v_mfma_f32_16x16x32_bf16 v[40:43], v[244:247], v[96:99], v[40:43]\n"
    "ds_read_b64_tr_b16 v[238:239], v255 offset:61440\n"
    "s_waitcnt lgkmcnt(10)\n"
    "v_mfma_f32_16x16x32_bf16 v[48:51], v[120:123], v[96:99], v[48:51]\n"
    "s_barrier\n"
    "ds_read_b64_tr_b16 v[240:241], v252 offset:58368\n"
    "v_mfma_f32_16x16x32_bf16 v[52:55], v[120:123], v[100:103], v[52:55]\n"
    "ds_read_b64_tr_b16 v[242:243], v252 offset:62464\n"
    "s_waitcnt lgkmcnt(10)\n"
    "v_mfma_f32_16x16x32_bf16 v[60:63], v[124:127], v[100:103], v[60:63]\n"
    "ds_read_b64_tr_b16 v[244:245], v253 offset:58368\n"
    "v_add_u32 v252, 0x12000, v252\n"
    "v_mfma_f32_16x16x32_bf16 v[56:59], v[124:127], v[96:99], v[56:59]\n"
    "ds_read_b64_tr_b16 v[246:247], v253 offset:62464\n"
    "s_waitcnt lgkmcnt(10)\n"
    "v_mfma_f32_16x16x32_bf16 v[64:67], v[128:131], v[224:227], v[64:67]\n"
    "ds_read_b64_tr_b16 v[120:121], v254 offset:58368\n"
    "ds_read_b128 v[104:107], v251 offset:49152\n"
    "v_mfma_f32_16x16x32_bf16 v[68:71], v[128:131], v[228:231], v[68:71]\n"
    "ds_read_b64_tr_b16 v[122:123], v254 offset:62464\n"
    "s_waitcnt lgkmcnt(11)\n"
    "v_mfma_f32_16x16x32_bf16 v[76:79], v[132:135], v[228:231], v[76:79]\n"
    "ds_read_b64_tr_b16 v[124:125], v255 offset:58368\n"
    "ds_read_b128 v[108:111], v251 offset:50176\n"
    "v_mfma_f32_16x16x32_bf16 v[72:75], v[132:135], v[224:227], v[72:75]\n"
    "ds_read_b64_tr_b16 v[126:127], v255 offset:62464\n"
    "s_waitcnt lgkmcnt(12)\n"
    "v_mfma_f32_16x16x32_bf16 v[80:83], v[232:235], v[224:227], v[80:83]\n"
    "ds_read_b64_tr_b16 v[128:129], v252 offset:0\n"
    "v_add_u32 v253, 0x12000, v253\n"
    "v_mfma_f32_16x16x32_bf16 v[84:87], v[232:235], v[228:231], v[84:87]\n"
    "ds_read_b64_tr_b16 v[130:131], v252 offset:4096\n"
    "s_waitcnt lgkmcnt(12)\n"
    "v_mfma_f32_16x16x32_bf16 v[92:95], v[236:239], v[228:231], v[92:95]\n"
    "ds_read_b64_tr_b16 v[132:133], v253 offset:0\n"
    "v_add_u32 v254, 0x12000, v254\n"
    "v_mfma_f32_16x16x32_bf16 v[88:91], v[236:239], v[224:227], v[88:91]\n"
    "ds_read_b64_tr_b16 v[134:135], v253 offset:4096\n"
    "s_waitcnt lgkmcnt(12)\n"
    "v_mfma_f32_16x16x32_bf16 v[64:67], v[240:243], v[144:147], v[64:67]\n"
    "ds_read_b64_tr_b16 v[232:233], v254 offset:0\n"
    "v_add_u32 v255, 0x12000, v255\n"
    "v_mfma_f32_16x16x32_bf16 v[68:71], v[240:243], v[148:151], v[68:71]\n"
    "ds_read_b64_tr_b16 v[234:235], v254 offset:4096\n"
    "s_waitcnt lgkmcnt(12)\n"
    "v_mfma_f32_16x16x32_bf16 v[76:79], v[244:247], v[148:151], v[76:79]\n"
    "ds_read_b64_tr_b16 v[236:237], v255 offset:0\n"
    "v_mfma_f32_16x16x32_bf16 v[72:75], v[244:247], v[144:147], v[72:75]\n"
    "ds_read_b64_tr_b16 v[238:239], v255 offset:4096\n"
    "s_waitcnt lgkmcnt(11)\n"
    "v_mfma_f32_16x16x32_bf16 v[80:83], v[120:123], v[144:147], v[80:83]\n"
    "ds_read_b64_tr_b16 v[240:241], v252 offset:1024\n"
    "v_mfma_f32_16x16x32_bf16 v[84:87], v[120:123], v[148:151], v[84:87]\n"
    "ds_read_b64_tr_b16 v[242:243], v252 offset:5120\n"
    "s_waitcnt lgkmcnt(10)\n"
    "v_mfma_f32_16x16x32_bf16 v[92:95], v[124:127], v[148:151], v[92:95]\n"
    "ds_read_b64_tr_b16 v[244:245], v253 offset:1024\n"
    "v_mfma_f32_16x16x32_bf16 v[88:91], v[124:127], v[144:147], v[88:91]\n"
    "ds_read_b64_tr_b16 v[246:247], v253 offset:5120\n"
    "s_waitcnt lgkmcnt(10)\n"
    "v_mfma_f32_16x16x32_bf16 v[0:3], v[128:131], v[104:107], v[0:3]\n"
    "ds_read_b64_tr_b16 v[120:121], v254 offset:1024\n"
    "v_mfma_f32_16x16x32_bf16 v[4:7], v[128:131], v[108:111], v[4:7]\n"
    "ds_read_b64_tr_b16 v[122:123], v254 offset:5120\n"
    "s_waitcnt lgkmcnt(10)\n"
    "v_mfma_f32_16x16x32_bf16 v[12:15], v[132:135], v[108:111], v[12:15]\n"
    "ds_read_b64_tr_b16 v[124:125], v255 offset:1024\n"
    "v_add_u32 v251, 0x12000, v251\n"
    "v_mfma_f32_16x16x32_bf16 v[8:11], v[132:135], v[104:107], v[8:11]\n"
    "ds_read_b64_tr_b16 v[126:127], v255 offset:5120\n"
    "s_waitcnt lgkmcnt(10)\n"
    "v_mfma_f32_16x16x32_bf16 v[16:19], v[232:235], v[104:107], v[16:19]\n"
    "s_barrier\n"
    "ds_read_b64_tr_b16 v[128:129], v252 offset:8192\n"
    "v_mfma_f32_16x16x32_bf16 v[20:23], v[232:235], v[108:111], v[20:23]\n"
    "ds_read_b64_tr_b16 v[130:131], v252 offset:12288\n"
    "s_waitcnt lgkmcnt(10)\n"
    "v_mfma_f32_16x16x32_bf16 v[28:31], v[236:239], v[108:111], v[28:31]\n"
    "ds_read_b64_tr_b16 v[132:133], v253 offset:8192\n"
    "s_cmp_lg_u32 s66, 0\n"
    "s_cbranch_scc1 L_q13_%=\n"
    "s_mov_b64 exec, s[50:51]\n"
    "buffer_store_dwordx2 v[160:161], v162, s[60:63], s49 offen offset:448 sc1\n"
    "s_mov_b64 exec, -1\n"
    "L_q13_%=:\n"
    "v_mfma_f32_16x16x32_bf16 v[24:27], v[236:239], v[104:107], v[24:27]\n"
    "ds_read_b64_tr_b16 v[134:135], v253 offset:12288\n"
    "s_waitcnt lgkmcnt(10)\n"
    "v_mfma_f32_16x16x32_bf16 v[32:35], v[240:243], v[104:107], v[32:35]\n"
    "ds_read_b64_tr_b16 v[232:233], v254 offset:8192\n"
    "ds_read_b128 v[112:115], v251 offset:0\n"
    "v_mfma_f32_16x16x32_bf16 v[36:39], v[240:243], v[108:111], v[36:39]\n"
    "ds_read_b64_tr_b16 v[234:235], v254 offset:12288\n"
    "s_waitcnt lgkmcnt(11)\n"
    "v_mfma_f32_16x16x32_bf16 v[44:47], v[244:247], v[108:111], v[44:47]\n"
    "ds_read_b64_tr_b16 v[236:237], v255 offset:8192\n"
    "ds_read_b128 v[116:119], v251 offset:1024\n"
    "v_mfma_f32_16x16x32_bf16 v[40:43], v[244:247], v[104:107], v[40:43]\n"
    "ds_read_b64_tr_b16 v[238:239], v255 offset:12288\n"
    "s_waitcnt lgkmcnt(12)\n"
    "v_mfma_f32_16x16x32_bf16 v[48:51], v[120:123], v[104:107], v[48:51]\n"
    "ds_read_b64_tr_b16 v[240:241], v252 offset:24576\n"
    "v_mfma_f32_16x16x32_bf16 v[52:55], v[120:123], v[108:111], v[52:55]\n"
    "ds_read_b64_tr_b16 v[242:243], v252 offset:28672\n"
    "s_waitcnt lgkmcnt(12)\n"
    "v_mfma_f32_16x16x32_bf16 v[60:63], v[124:127], v[108:111], v[60:63]\n"
    "ds_read_b64_tr_b16 v[244:245], v253 offset:24576\n"
    "v_mfma_f32_16x16x32_bf16 v[56:59], v[124:127], v[104:107], v[56:59]\n"
    "ds_read_b64_tr_b16 v[246:247], v253 offset:28672\n"
    "s_waitcnt lgkmcnt(12)\n"
    "v_mfma_f32_16x16x32_bf16 v[64:67], v[128:131], v[152:155], v[64:67]\n"
    "ds_read_b64_tr_b16 v[120:121], v254 offset:24576\n"
    "v_mfma_f32_16x16x32_bf16 v[68:71], v[128:131], v[156:159], v[68:71]\n"
    "ds_read_b64_tr_b16 v[122:123], v254 offset:28672\n"
    "s_waitcnt lgkmcnt(12)\n"
    "v_mfma_f32_16x16x32_bf16 v[76:79], v[132:135], v[156:159], v[76:79]\n"
    "ds_read_b64_tr_b16 v[124:125], v255 offset:24576\n"
    "v_mfma_f32_16x16x32_bf16 v[72:75], v[132:135], v[152:155], v[72:75]\n"
    "ds_read_b64_tr_b16 v[126:127], v255 offset:28672\n"
    "s_waitcnt lgkmcnt(11)\n"
    "v_mfma_f32_16x16x32_bf16 v[80:83], v[232:235], v[152:155], v[80:83]\n"
    "ds_read_b64_tr_b16 v[128:129], v252 offset:25600\n"
    "v_mfma_f32_16x16x32_bf16 v[84:87], v[232:235], v[156:159], v[84:87]\n"
    "ds_read_b64_tr_b16 v[130:131], v252 offset:29696\n"
    "s_waitcnt lgkmcnt(10)\n"
    "v_mfma_f32_16x16x32_bf16 v[92:95], v[236:239], v[156:159], v[92:95]\n"
    "ds_read_b64_tr_b16 v[132:133], v253 offset:25600\n"
    "v_mfma_f32_16x16x32_bf16 v[88:91], v[236:239], v[152:155], v[88:91]\n"
    "ds_read_b64_tr_b16 v[134:135], v253 offset:29696\n"
    "s_waitcnt lgkmcnt(10)\n"
    "v_mfma_f32_16x16x32_bf16 v[0:3], v[240:243], v[112:115], v[0:3]\n"
    "ds_read_b64_tr_b16 v[232:233], v254 offset:25600\n"
    "ds_read_b128 v[96:99], v251 offset:16384\n"
    "v_mfma_f32_16x16x32_bf16 v[4:7], v[240:243], v[116:119], v[4:7]\n"
    "ds_read_b64_tr_b16 v[234:235], v254 offset:29696\n"
    "s_waitcnt lgkmcnt(11)\n"
    "v_mfma_f32_16x16x32_bf16 v[12:15], v[244:247], v[116:119], v[12:15]\n"
    "ds_read_b64_tr_b16 v[236:237], v255 offset:25600\n"
    "ds_read_b128 v[100:103], v251 offset:17408\n"
    "v_mfma_f32_16x16x32_bf16 v[8:11], v[244:247], v[112:115], v[8:11]\n"
    "ds_read_b64_tr_b16 v[238:239], v255 offset:29696\n"
    "s_waitcnt lgkmcnt(12)\n"
    "v_mfma_f32_16x16x32_bf16 v[16:19], v[120:123], v[112:115], v[16:19]\n"
    "s_barrier\n"
    "ds_read_b64_tr_b16 v[240:241], v252 offset:40960\n"
    "v_mfma_f32_16x16x32_bf16 v[20:23], v[120:123], v[116:119], v[20:23]\n"
    "ds_read_b64_tr_b16 v[242:243], v252 offset:45056\n"
    "s_waitcnt lgkmcnt(12)\n"
    "v_mfma_f32_16x16x32_bf16 v[28:31], v[124:127], v[116:119], v[28:31]\n"
    "ds_read_b64_tr_b16 v[244:245], v253 offset:40960\n"
    "v_cvt_pk_bf16_f32 v72, v72, v73\n"
    "v_mfma_f32_16x16x32_bf16 v[24:27], v[124:127], v[112:115], v[24:27]\n"
    "ds_read_b64_tr_b16 v[246:247], v253 offset:45056\n"
    "s_waitcnt lgkmcnt(12)\n"
    "v_mfma_f32_16x16x32_bf16 v[32:35], v[128:131], v[112:115], v[32:35]\n"
    "ds_read_b64_tr_b16 v[120:121], v254 offset:40960\n"
    "v_cvt_pk_bf16_f32 v73, v74, v75\n"
    "v_mfma_f32_16x16x32_bf16 v[36:39], v[128:131], v[116:119], v[36:39]\n"
    "ds_read_b64_tr_b16 v[122:123], v254 offset:45056\n"
    "s_waitcnt lgkmcnt(12)\n"
    "v_mfma_f32_16x16x32_bf16 v[44:47], v[132:135], v[116:119], v[44:47]\n"
    "ds_read_b64_tr_b16 v[124:125], v255 offset:40960\n"
    "v_cvt_pk_bf16_f32 v74, v76, v77\n"
    "v_mfma_f32_16x16x32_bf16 v[40:43], v[132:135], v[112:115], v[40:43]\n"
    "ds_read_b64_tr_b16 v[126:127], v255 offset:45056\n"
    "s_waitcnt lgkmcnt(11)\n"
    "v_mfma_f32_16x16x32_bf16 v[48:51], v[232:235], v[112:115], v[48:51]\n"
    "ds_read_b64_tr_b16 v[128:129], v252 offset:41984\n"
    "v_cvt_pk_bf16_f32 v75, v78, v79\n"
    "v_mfma_f32_16x16x32_bf16 v[52:55], v[232:235], v[116:119], v[52:55]\n"
    "ds_read_b64_tr_b16 v[130:131], v252 offset:46080\n"
    "s_waitcnt lgkmcnt(10)\n"
    "v_mfma_f32_16x16x32_bf16 v[60:63], v[236:239], v[116:119], v[60:63]\n"
    "ds_read_b64_tr_b16 v[132:133], v253 offset:41984\n"
    "v_cvt_pk_bf16_f32 v80, v80, v81\n"
    "v_mfma_f32_16x16x32_bf16 v[56:59], v[236:239], v[112:115], v[56:59]\n"
    "ds_read_b64_tr_b16 v[134:135], v253 offset:46080\n"
    "s_waitcnt lgkmcnt(10)\n"
    "v_mfma_f32_16x16x32_bf16 v[0:3], v[240:243], v[96:99], v[0:3]\n"
    "ds_read_b64_tr_b16 v[232:233], v254 offset:41984\n"
    "ds_read_b128 v[104:107], v251 offset:32768\n"
    "v_mfma_f32_16x16x32_bf16 v[4:7], v[240:243], v[100:103], v[4:7]\n"
    "ds_read_b64_tr_b16 v[234:235], v254 offset:46080\n"
    "s_waitcnt lgkmcnt(11)\n"
    "v_mfma_f32_16x16x32_bf16 v[12:15], v[244:247], v[100:103], v[12:15]\n"
    "ds_read_b64_tr_b16 v[236:237], v255 offset:41984\n"
    "ds_read_b128 v[108:111], v251 offset:33792\n"
    "v_mfma_f32_16x16x32_bf16 v[8:11], v[244:247], v[96:99], v[8:11]\n"
    "ds_read_b64_tr_b16 v[238:239], v255 offset:46080\n"
    "s_waitcnt lgkmcnt(12)\n"
    "v_mfma_f32_16x16x32_bf16 v[16:19], v[120:123], v[96:99], v[16:19]\n"
    "ds_read_b64_tr_b16 v[240:241], v252 offset:57344\n"
    "v_cvt_pk_bf16_f32 v81, v82, v83\n"
    "v_mfma_f32_16x16x32_bf16 v[20:23], v[120:123], v[100:103], v[20:23]\n"
    "ds_read_b64_tr_b16 v[242:243], v252 offset:61440\n"
    "s_waitcnt lgkmcnt(12)\n"
    "v_mfma_f32_16x16x32_bf16 v[28:31], v[124:127], v[100:103], v[28:31]\n"
    "ds_read_b64_tr_b16 v[244:245], v253 offset:57344\n"
    "v_cvt_pk_bf16_f32 v82, v84, v85\n"
    "v_mfma_f32_16x16x32_bf16 v[24:27], v[124:127], v[96:99], v[24:27]\n"
    "ds_read_b64_tr_b16 v[246:247], v253 offset:61440\n"
    "s_waitcnt lgkmcnt(12)\n"
    "v_mfma_f32_16x16x32_bf16 v[32:35], v[128:131], v[96:99], v[32:35]\n"
    "ds_read_b64_tr_b16 v[120:121], v254 offset:57344\n"
    "v_cvt_pk_bf16_f32 v83, v86, v87\n"
    "v_mfma_f32_16x16x32_bf16 v[36:39], v[128:131], v[100:103], v[36:39]\n"
    "ds_read_b64_tr_b16 v[122:123], v254 offset:61440\n"
    "s_waitcnt lgkmcnt(12)\n"
    "v_mfma_f32_16x16x32_bf16 v[44:47], v[132:135], v[100:103], v[44:47]\n"
    "ds_read_b64_tr_b16 v[124:125], v255 offset:57344\n"
    "v_cvt_pk_bf16_f32 v88, v88, v89\n"
    "v_mfma_f32_16x16x32_bf16 v[40:43], v[132:135], v[96:99], v[40:43]\n"
    "ds_read_b64_tr_b16 v[126:127], v255 offset:61440\n"
    "s_waitcnt lgkmcnt(11)\n"
    "v_mfma_f32_16x16x32_bf16 v[48:51], v[232:235], v[96:99], v[48:51]\n"
    "s_barrier\n"
    "ds_read_b64_tr_b16 v[128:129], v252 offset:58368\n"
    "v_mfma_f32_16x16x32_bf16 v[52:55], v[232:235], v[100:103], v[52:55]\n"
    "ds_read_b64_tr_b16 v[130:131], v252 offset:62464\n"
    "s_waitcnt lgkmcnt(10)\n"
    "v_mfma_f32_16x16x32_bf16 v[60:63], v[236:239], v[100:103], v[60:63]\n"
    "ds_read_b64_tr_b16 v[132:133], v253 offset:58368\n"
    "v_add_u32 v252, 0x6000, v252\n"
    "v_mfma_f32_16x16x32_bf16 v[56:59], v[236:239], v[96:99], v[56:59]\n"
    "ds_read_b64_tr_b16 v[134:135], v253 offset:62464\n"
    "s_waitcnt lgkmcnt(10)\n"
    "v_mfma_f32_16x16x32_bf16 v[0:3], v[240:243], v[104:107], v[0:3]\n"
    "ds_read_b64_tr_b16 v[232:233], v254 offset:58368\n"
    "ds_read_b128 v[112:115], v251 offset:49152\n"
    "v_mfma_f32_16x16x32_bf16 v[4:7], v[240:243], v[108:111], v[4:7]\n"
    "ds_read_b64_tr_b16 v[234:235], v254 offset:62464\n"
    "s_waitcnt lgkmcnt(11)\n"
    "v_mfma_f32_16x16x32_bf16 v[12:15], v[244:247], v[108:111], v[12:15]\n"
    "ds_read_b64_tr_b16 v[236:237], v255 offset:58368\n"
    "ds_read_b128 v[116:119], v251 offset:50176\n"
    "v_mfma_f32_16x16x32_bf16 v[8:11], v[244:247], v[104:107], v[8:11]\n"
    "ds_read_b64_tr_b16 v[238:239], v255 offset:62464\n"
    "s_waitcnt lgkmcnt(12)\n"
    "v_mfma_f32_16x16x32_bf16 v[16:19], v[120:123], v[104:107], v[16:19]\n"
    "ds_read_b64_tr_b16 v[240:241], v252 offset:49152\n"
    "v_add_u32 v253, 0x6000, v253\n"
    "v_mfma_f32_16x16x32_bf16 v[20:23], v[120:123], v[108:111], v[20:23]\n"
    "ds_read_b64_tr_b16 v[242:243], v252 offset:53248\n"
    "s_waitcnt lgkmcnt(12)\n"
    "v_mfma_f32_16x16x32_bf16 v[28:31], v[124:127], v[108:111], v[28:31]\n"
    "ds_read_b64_tr_b16 v[244:245], v253 offset:49152\n"
    "v_add_u32 v254, 0x6000, v254\n"
    "v_mfma_f32_16x16x32_bf16 v[24:27], v[124:127], v[104:107], v[24:27]\n"
    "ds_read_b64_tr_b16 v[246:247], v253 offset:53248\n"
    "s_waitcnt lgkmcnt(12)\n"
    "v_mfma_f32_16x16x32_bf16 v[32:35], v[128:131], v[104:107], v[32:35]\n"
    "ds_read_b64_tr_b16 v[120:121], v254 offset:49152\n"
    "v_add_u32 v255, 0x6000, v255\n"
    "v_mfma_f32_16x16x32_bf16 v[36:39], v[128:131], v[108:111], v[36:39]\n"
    "ds_read_b64_tr_b16 v[122:123], v254 offset:53248\n"
    "s_waitcnt lgkmcnt(12)\n"
    "v_mfma_f32_16x16x32_bf16 v[44:47], v[132:135], v[108:111], v[44:47]\n"
    "ds_read_b64_tr_b16 v[124:125], v255 offset:49152\n"
    "v_add_u32 v251, 0xfffea000, v251\n"
    "v_mfma_f32_16x16x32_bf16 v[40:43], v[132:135], v[104:107], v[40:43]\n"
    "ds_read_b64_tr_b16 v[126:127], v255 offset:53248\n"
    "v_cvt_pk_bf16_f32 v89, v90, v91\n"
    "v_cvt_pk_bf16_f32 v90, v92, v93\n"
    "v_cvt_pk_bf16_f32 v91, v94, v95\n"
    "s_waitcnt lgkmcnt(11)\n"
    "v_mfma_f32_16x16x32_bf16 v[48:51], v[232:235], v[104:107], v[48:51]\n"
    "ds_read_b64_tr_b16 v[128:129], v252 offset:50176\n"
    "buffer_store_dwordx4 v[72:75], v250, s[60:63], s95 offen sc1\n"
    "v_mfma_f32_16x16x32_bf16 v[52:55], v[232:235], v[108:111], v[52:55]\n"
    "ds_read_b64_tr_b16 v[130:131], v252 offset:54272\n"
    "s_waitcnt lgkmcnt(10)\n"
    "v_mfma_f32_16x16x32_bf16 v[60:63], v[236:239], v[108:111], v[60:63]\n"
    "ds_read_b64_tr_b16 v[132:133], v253 offset:50176\n"
    "buffer_store_dwordx4 v[80:83], v250, s[60:63], s96 offen sc1\n"
    "v_mfma_f32_16x16x32_bf16 v[56:59], v[236:239], v[104:107], v[56:59]\n"
    "ds_read_b64_tr_b16 v[134:135], v253 offset:54272\n"
    "s_waitcnt lgkmcnt(10)\n"
    "v_mfma_f32_16x16x32_bf16 v[0:3], v[240:243], v[112:115], v[0:3]\n"
    "ds_read_b64_tr_b16 v[232:233], v254 offset:50176\n"
    "buffer_store_dwordx4 v[88:91], v250, s[60:63], s97 offen sc1\n"
    "ds_read_b128 v[96:99], v251 offset:0\n"
    "v_mfma_f32_16x16x32_bf16 v[4:7], v[240:243], v[116:119], v[4:7]\n"
    "ds_read_b64_tr_b16 v[234:235], v254 offset:54272\n"
    "s_waitcnt lgkmcnt(11)\n"
    "v_mfma_f32_16x16x32_bf16 v[12:15], v[244:247], v[116:119], v[12:15]\n"
    "ds_read_b64_tr_b16 v[236:237], v255 offset:50176\n"
    "ds_read_b128 v[100:103], v251 offset:1024\n"
    "v_mfma_f32_16x16x32_bf16 v[8:11], v[244:247], v[112:115], v[8:11]\n"
    "ds_read_b64_tr_b16 v[238:239], v255 offset:54272\n"
    "s_waitcnt lgkmcnt(12)\n"
    "v_mfma_f32_16x16x32_bf16 v[16:19], v[120:123], v[112:115], v[16:19]\n"
    "s_barrier\n"
    "v_add_u32 v252, 0xfffea000, v252\n"
    "ds_read_b64_tr_b16 v[240:241], v252 offset:0\n"
    "v_mfma_f32_16x16x32_bf16 v[20:23], v[120:123], v[116:119], v[20:23]\n"
    "ds_read_b64_tr_b16 v[242:243], v252 offset:4096\n"
    "s_waitcnt lgkmcnt(12)\n"
    "v_mfma_f32_16x16x32_bf16 v[28:31], v[124:127], v[116:119], v[28:31]\n"
    "v_add_u32 v253, 0xfffea000, v253\n"
    "ds_read_b64_tr_b16 v[244:245], v253 offset:0\n"
    "v_add_u32 v254, 0xfffea000, v254\n"
    "v_mfma_f32_16x16x32_bf16 v[24:27], v[124:127], v[112:115], v[24:27]\n"
    "ds_read_b64_tr_b16 v[246:247], v253 offset:4096\n"
    "s_waitcnt lgkmcnt(12)\n"
    "v_mfma_f32_16x16x32_bf16 v[32:35], v[128:131], v[112:115], v[32:35]\n"
    "ds_read_b64_tr_b16 v[120:121], v254 offset:0\n"
    "v_add_u32 v255, 0xfffea000, v255\n"
    "v_mfma_f32_16x16x32_bf16 v[36:39], v[128:131], v[116:119], v[36:39]\n"
    "ds_read_b64_tr_b16 v[122:123], v254 offset:4096\n"
    "s_waitcnt lgkmcnt(12)\n"
    "v_mfma_f32_16x16x32_bf16 v[44:47], v[132:135], v[116:119], v[44:47]\n"
    "ds_read_b64_tr_b16 v[124:125], v255 offset:0\n"
    "v_mfma_f32_16x16x32_bf16 v[40:43], v[132:135], v[112:115], v[40:43]\n"
    "ds_read_b64_tr_b16 v[126:127], v255 offset:4096\n"
    "s_waitcnt lgkmcnt(11)\n"
    "v_mfma_f32_16x16x32_bf16 v[48:51], v[232:235], v[112:115], v[48:51]\n"
    "ds_read_b64_tr_b16 v[128:129], v252 offset:1024\n"
    "v_mfma_f32_16x16x32_bf16 v[52:55], v[232:235], v[116:119], v[52:55]\n"
    "ds_read_b64_tr_b16 v[130:131], v252 offset:5120\n"
    "s_waitcnt lgkmcnt(10)\n"
    "v_mfma_f32_16x16x32_bf16 v[60:63], v[236:239], v[116:119], v[60:63]\n"
    "ds_read_b64_tr_b16 v[132:133], v253 offset:1024\n"
    "v_mfma_f32_16x16x32_bf16 v[56:59], v[236:239], v[112:115], v[56:59]\n"
    "ds_read_b64_tr_b16 v[134:135], v253 offset:5120\n"
    "s_waitcnt lgkmcnt(10)\n"
    "v_mfma_f32_16x16x32_bf16 v[0:3], v[240:243], v[96:99], v[0:3]\n"
    "ds_read_b64_tr_b16 v[232:233], v254 offset:1024\n"
    "ds_read_b128 v[104:107], v251 offset:16384\n"
    "v_mfma_f32_16x16x32_bf16 v[4:7], v[240:243], v[100:103], v[4:7]\n"
    "ds_read_b64_tr_b16 v[234:235], v254 offset:5120\n"
    "s_waitcnt lgkmcnt(11)\n"
    "v_mfma_f32_16x16x32_bf16 v[12:15], v[244:247], v[100:103], v[12:15]\n"
    "ds_read_b64_tr_b16 v[236:237], v255 offset:1024\n"
    "ds_read_b128 v[108:111], v251 offset:17408\n"
    "v_mfma_f32_16x16x32_bf16 v[8:11], v[244:247], v[96:99], v[8:11]\n"
    "ds_read_b64_tr_b16 v[238:239], v255 offset:5120\n"
    "s_waitcnt lgkmcnt(12)\n"
    "v_mfma_f32_16x16x32_bf16 v[16:19], v[120:123], v[96:99], v[16:19]\n"
    "ds_read_b64_tr_b16 v[240:241], v252 offset:16384\n"
    "v_mfma_f32_16x16x32_bf16 v[20:23], v[120:123], v[100:103], v[20:23]\n"
    "L_st7_%=:\n"
    "s_cbranch_execz L_bk%=\n"
    "ds_read_b64_tr_b16 v[242:243], v252 offset:20480\n"
    "s_waitcnt lgkmcnt(12)\n"
    "v_mfma_f32_16x16x32_bf16 v[28:31], v[124:127], v[100:103], v[28:31]\n"
    "ds_read_b64_tr_b16 v[244:245], v253 offset:16384\n"
    "v_mfma_f32_16x16x32_bf16 v[24:27], v[124:127], v[96:99], v[24:27]\n"
    "ds_read_b64_tr_b16 v[246:247], v253 offset:20480\n"
    "s_waitcnt lgkmcnt(12)\n"
    "v_mfma_f32_16x16x32_bf16 v[32:35], v[128:131], v[96:99], v[32:35]\n"
    "ds_read_b64_tr_b16 v[120:121], v254 offset:16384\n"
    "v_mfma_f32_16x16x32_bf16 v[36:39], v[128:131], v[100:103], v[36:39]\n"
    "ds_read_b64_tr_b16 v[122:123], v254 offset:20480\n"
    "s_waitcnt lgkmcnt(12)\n"
    "v_mfma_f32_16x16x32_bf16 v[44:47], v[132:135], v[100:103], v[44:47]\n"
    "ds_read_b64_tr_b16 v[124:125], v255 offset:16384\n"
    "v_mfma_f32_16x16x32_bf16 v[40:43], v[132:135], v[96:99], v[40:43]\n"
    "ds_read_b64_tr_b16 v[126:127], v255 offset:20480\n"
    "s_waitcnt lgkmcnt(11)\n"
    "v_mfma_f32_16x16x32_bf16 v[48:51], v[232:235], v[96:99], v[48:51]\n"
    "s_barrier\n"
    "ds_read_b64_tr_b16 v[128:129], v252 offset:17408\n"
    "v_mfma_f32_16x16x32_bf16 v[52:55], v[232:235], v[100:103], v[52:55]\n"
    "ds_read_b64_tr_b16 v[130:131], v252 offset:21504\n"
    "s_waitcnt lgkmcnt(10)\n"
    "v_mfma_f32_16x16x32_bf16 v[60:63], v[236:239], v[100:103], v[60:63]\n"
    "ds_read_b64_tr_b16 v[132:133], v253 offset:17408\n"
    "v_mfma_f32_16x16x32_bf16 v[56:59], v[236:239], v[96:99], v[56:59]\n"
    "ds_read_b64_tr_b16 v[134:135], v253 offset:21504\n"
    "s_waitcnt lgkmcnt(10)\n"
    "v_mfma_f32_16x16x32_bf16 v[0:3], v[240:243], v[104:107], v[0:3]\n"
    "ds_read_b64_tr_b16 v[232:233], v254 offset:17408\n"
    "ds_read_b128 v[112:115], v251 offset:32768\n"
    "v_mfma_f32_16x16x32_bf16 v[4:7], v[240:243], v[108:111], v[4:7]\n"
    "ds_read_b64_tr_b16 v[234:235], v254 offset:21504\n"
    "s_waitcnt lgkmcnt(11)\n"
    "v_mfma_f32_16x16x32_bf16 v[12:15], v[244:247], v[108:111], v[12:15]\n"
    "ds_read_b64_tr_b16 v[236:237], v255 offset:17408\n"
    "ds_read_b128 v[116:119], v251 offset:33792\n"
    "v_mfma_f32_16x16x32_bf16 v[8:11], v[244:247], v[104:107], v[8:11]\n"
    "ds_read_b64_tr_b16 v[238:239], v255 offset:21504\n"
    "s_waitcnt lgkmcnt(12)\n"
    "v_mfma_f32_16x16x32_bf16 v[16:19], v[120:123], v[104:107], v[16:19]\n"
    "ds_read_b64_tr_b16 v[240:241], v252 offset:32768\n"
    "v_mfma_f32_16x16x32_bf16 v[20:23], v[120:123], v[108:111], v[20:23]\n"
    "ds_read_b64_tr_b16 v[242:243], v252 offset:36864\n"
    "s_waitcnt lgkmcnt(12)\n"
    "v_mfma_f32_16x16x32_bf16 v[28:31], v[124:127], v[108:111], v[28:31]\n"
    "ds_read_b64_tr_b16 v[244:245], v253 offset:32768\n"
    "v_mfma_f32_16x16x32_bf16 v[24:27], v[124:127], v[104:107], v[24:27]\n"
    "ds_read_b64_tr_b16 v[246:247], v253 offset:36864\n"
    "s_waitcnt lgkmcnt(12)\n"
    "v_mfma_f32_16x16x32_bf16 v[32:35], v[128:131], v[104:107], v[32:35]\n"
    "ds_read_b64_tr_b16 v[120:121], v254 offset:32768\n"
    "v_mfma_f32_16x16x32_bf16 v[36:39], v[128:131], v[108:111], v[36:39]\n"
    "ds_read_b64_tr_b16 v[122:123], v254 offset:36864\n"
    "s_waitcnt lgkmcnt(12)\n"
    "v_mfma_f32_16x16x32_bf16 v[44:47], v[132:135], v[108:111], v[44:47]\n"
    "ds_read_b64_tr_b16 v[124:125], v255 offset:32768\n"
    "v_mfma_f32_16x16x32_bf16 v[40:43], v[132:135], v[104:107], v[40:43]\n"
    "ds_read_b64_tr_b16 v[126:127], v255 offset:36864\n"
    "s_waitcnt lgkmcnt(11)\n"
    "v_mfma_f32_16x16x32_bf16 v[48:51], v[232:235], v[104:107], v[48:51]\n"
    "ds_read_b64_tr_b16 v[128:129], v252 offset:33792\n"
    "v_mfma_f32_16x16x32_bf16 v[52:55], v[232:235], v[108:111], v[52:55]\n"
    "ds_read_b64_tr_b16 v[130:131], v252 offset:37888\n"
    "s_waitcnt lgkmcnt(10)\n"
    "v_mfma_f32_16x16x32_bf16 v[60:63], v[236:239], v[108:111], v[60:63]\n"
    "ds_read_b64_tr_b16 v[132:133], v253 offset:33792\n"
    "v_mfma_f32_16x16x32_bf16 v[56:59], v[236:239], v[104:107], v[56:59]\n"
    "ds_read_b64_tr_b16 v[134:135], v253 offset:37888\n"
    "s_waitcnt lgkmcnt(10)\n"
    "v_mfma_f32_16x16x32_bf16 v[0:3], v[240:243], v[112:115], v[0:3]\n"
    "ds_read_b64_tr_b16 v[232:233], v254 offset:33792\n"
    "v_mfma_f32_16x16x32_bf16 v[4:7], v[240:243], v[116:119], v[4:7]\n"
    "ds_read_b64_tr_b16 v[234:235], v254 offset:37888\n"
    "s_waitcnt lgkmcnt(10)\n"
    "v_mfma_f32_16x16x32_bf16 v[12:15], v[244:247], v[116:119], v[12:15]\n"
    "ds_read_b64_tr_b16 v[236:237], v255 offset:33792\n"
    "v_mfma_f32_16x16x32_bf16 v[8:11], v[244:247], v[112:115], v[8:11]\n"
    "ds_read_b64_tr_b16 v[238:239], v255 offset:37888\n"
    "s_waitcnt lgkmcnt(10)\n"
    "v_mfma_f32_16x16x32_bf16 v[16:19], v[120:123], v[112:115], v[16:19]\n"
    "s_barrier\n"
    "ds_read_b64_tr_b16 v[240:241], v252 offset:40960\n"
    "v_mfma_f32_16x16x32_bf16 v[20:23], v[120:123], v[116:119], v[20:23]\n"
    "ds_read_b64_tr_b16 v[242:243], v252 offset:45056\n"
    "s_waitcnt lgkmcnt(10)\n"
    "v_mfma_f32_16x16x32_bf16 v[28:31], v[124:127], v[116:119], v[28:31]\n"
    "ds_read_b64_tr_b16 v[244:245], v253 offset:40960\n"
    "v_mfma_f32_16x16x32_bf16 v[24:27], v[124:127], v[112:115], v[24:27]\n"
    "ds_read_b64_tr_b16 v[246:247], v253 offset:45056\n"
    "s_waitcnt lgkmcnt(10)\n"
    "v_mfma_f32_16x16x32_bf16 v[32:35], v[128:131], v[112:115], v[32:35]\n"
    "ds_read_b64_tr_b16 v[120:121], v254 offset:40960\n"
    "v_mfma_f32_16x16x32_bf16 v[36:39], v[128:131], v[116:119], v[36:39]\n"
    "ds_read_b64_tr_b16 v[122:123], v254 offset:45056\n"
    "s_waitcnt lgkmcnt(10)\n"
    "v_mfma_f32_16x16x32_bf16 v[44:47], v[132:135], v[116:119], v[44:47]\n"
    "ds_read_b64_tr_b16 v[124:125], v255 offset:40960\n"
    "v_mfma_f32_16x16x32_bf16 v[40:43], v[132:135], v[112:115], v[40:43]\n"
    "ds_read_b64_tr_b16 v[126:127], v255 offset:45056\n"
    "s_waitcnt lgkmcnt(10)\n"
    "v_mfma_f32_16x16x32_bf16 v[48:51], v[232:235], v[112:115], v[48:51]\n"
    "ds_read_b64_tr_b16 v[128:129], v252 offset:41984\n"
    "v_mfma_f32_16x16x32_bf16 v[52:55], v[232:235], v[116:119], v[52:55]\n"
    "ds_read_b64_tr_b16 v[130:131], v252 offset:46080\n"
    "s_waitcnt lgkmcnt(10)\n"
    "v_mfma_f32_16x16x32_bf16 v[60:63], v[236:239], v[116:119], v[60:63]\n"
    "ds_read_b64_tr_b16 v[132:133], v253 offset:41984\n"
    "v_mfma_f32_16x16x32_bf16 v[56:59], v[236:239], v[112:115], v[56:59]\n"
    "ds_read_b64_tr_b16 v[134:135], v253 offset:46080\n"
    "s_waitcnt lgkmcnt(10)\n"
    "v_mfma_f32_16x16x32_bf16 v[0:3], v[240:243], v[184:187], v[0:3]\n"
    "ds_read_b64_tr_b16 v[232:233], v254 offset:41984\n"
    "ds_read_b128 v[96:99], v251 offset:57344\n"
    "v_mfma_f32_16x16x32_bf16 v[4:7], v[240:243], v[188:191], v[4:7]\n"
    "ds_read_b64_tr_b16 v[234:235], v254 offset:46080\n"
    "s_waitcnt lgkmcnt(11)\n"
    "v_mfma_f32_16x16x32_bf16 v[12:15], v[244:247], v[188:191], v[12:15]\n"
    "ds_read_b64_tr_b16 v[236:237], v255 offset:41984\n"
    "ds_read_b128 v[100:103], v251 offset:58368\n"
    "v_mfma_f32_16x16x32_bf16 v[8:11], v[244:247], v[184:187], v[8:11]\n"
    "ds_read_b64_tr_b16 v[238:239], v255 offset:46080\n"
    "s_waitcnt lgkmcnt(12)\n"
    "v_mfma_f32_16x16x32_bf16 v[16:19], v[120:123], v[184:187], v[16:19]\n"
    "ds_read_b64_tr_b16 v[240:241], v252 offset:57344\n"
    "v_mfma_f32_16x16x32_bf16 v[20:23], v[120:123], v[188:191], v[20:23]\n"
    "ds_read_b64_tr_b16 v[242:243], v252 offset:61440\n"
    "s_waitcnt lgkmcnt(12)\n"
    "v_mfma_f32_16x16x32_bf16 v[28:31], v[124:127], v[188:191], v[28:31]\n"
    "ds_read_b64_tr_b16 v[244:245], v253 offset:57344\n"
    "v_mfma_f32_16x16x32_bf16 v[24:27], v[124:127], v[184:187], v[24:27]\n"
    "ds_read_b64_tr_b16 v[246:247], v253 offset:61440\n"
    "s_waitcnt lgkmcnt(12)\n"
    "v_mfma_f32_16x16x32_bf16 v[32:35], v[128:131], v[184:187], v[32:35]\n"
    "ds_read_b64_tr_b16 v[120:121], v254 offset:57344\n"
    "v_mfma_f32_16x16x32_bf16 v[36:39], v[128:131], v[188:191], v[36:39]\n"
    "ds_read_b64_tr_b16 v[122:123], v254 offset:61440\n"
    "s_waitcnt lgkmcnt(12)\n"
    "v_mfma_f32_16x16x32_bf16 v[44:47], v[132:135], v[188:191], v[44:47]\n"
    "ds_read_b64_tr_b16 v[124:125], v255 offset:57344\n"
    "v_add_u32 v251, 0x12000, v251\n"
    "v_mfma_f32_16x16x32_bf16 v[40:43], v[132:135], v[184:187], v[40:43]\n"
    "ds_read_b64_tr_b16 v[126:127], v255 offset:61440\n"
    "s_waitcnt lgkmcnt(11)\n"
    "v_mfma_f32_16x16x32_bf16 v[48:51], v[232:235], v[184:187], v[48:51]\n"
    "s_barrier\n"
    "ds_read_b64_tr_b16 v[128:129], v252 offset:58368\n"
    "v_mfma_f32_16x16x32_bf16 v[52:55], v[232:235], v[188:191], v[52:55]\n"
    "ds_read_b64_tr_b16 v[130:131], v252 offset:62464\n"
    "s_waitcnt lgkmcnt(10)\n"
    "v_mfma_f32_16x16x32_bf16 v[60:63], v[236:239], v[188:191], v[60:63]\n"
    "ds_read_b64_tr_b16 v[132:133], v253 offset:58368\n"
    "v_add_u32 v252, 0x12000, v252\n"
    "v_mfma_f32_16x16x32_bf16 v[56:59], v[236:239], v[184:187], v[56:59]\n"
    "ds_read_b64_tr_b16 v[134:135], v253 offset:62464\n"
    "s_waitcnt lgkmcnt(10)\n"
    "v_mfma_f32_16x16x32_bf16 v[0:3], v[240:243], v[96:99], v[0:3]\n"
    "ds_read_b64_tr_b16 v[232:233], v254 offset:58368\n"
    "ds_read_b128 v[104:107], v251 offset:0\n"
    "v_mfma_f32_16x16x32_bf16 v[4:7], v[240:243], v[100:103], v[4:7]\n"
    "ds_read_b64_tr_b16 v[234:235], v254 offset:62464\n"
    "s_waitcnt lgkmcnt(11)\n"
    "v_mfma_f32_16x16x32_bf16 v[12:15], v[244:247], v[100:103], v[12:15]\n"
    "ds_read_b64_tr_b16 v[236:237], v255 offset:58368\n"
    "ds_read_b128 v[108:111], v251 offset:1024\n"
    "v_mfma_f32_16x16x32_bf16 v[8:11], v[244:247], v[96:99], v[8:11]\n"
    "ds_read_b64_tr_b16 v[238:239], v255 offset:62464\n"
    "s_waitcnt lgkmcnt(12)\n"
    "v_mfma_f32_16x16x32_bf16 v[16:19], v[120:123], v[96:99], v[16:19]\n"
    "ds_read_b64_tr_b16 v[240:241], v252 offset:0\n"
    "v_add_u32 v253, 0x12000, v253\n"
    "v_mfma_f32_16x16x32_bf16 v[20:23], v[120:123], v[100:103], v[20:23]\n"
    "ds_read_b64_tr_b16 v[242:243], v252 offset:4096\n"
    "s_waitcnt lgkmcnt(12)\n"
    "v_mfma_f32_16x16x32_bf16 v[28:31], v[124:127], v[100:103], v[28:31]\n"
    "ds_read_b64_tr_b16 v[244:245], v253 offset:0\n"
    "v_add_u32 v254, 0x12000, v254\n"
    "v_mfma_f32_16x16x32_bf16 v[24:27], v[124:127], v[96:99], v[24:27]\n"
    "ds_read_b64_tr_b16 v[246:247], v253 offset:4096\n"
    "s_waitcnt lgkmcnt(12)\n"
    "v_mfma_f32_16x16x32_bf16 v[32:35], v[128:131], v[96:99], v[32:35]\n"
    "ds_read_b64_tr_b16 v[120:121], v254 offset:0\n"
    "v_add_u32 v255, 0x12000, v255\n"
    "v_mfma_f32_16x16x32_bf16 v[36:39], v[128:131], v[100:103], v[36:39]\n"
    "ds_read_b64_tr_b16 v[122:123], v254 offset:4096\n"
    "s_waitcnt lgkmcnt(12)\n"
    "v_mfma_f32_16x16x32_bf16 v[44:47], v[132:135], v[100:103], v[44:47]\n"
    "ds_read_b64_tr_b16 v[124:125], v255 offset:0\n"
    "v_mfma_f32_16x16x32_bf16 v[40:43], v[132:135], v[96:99], v[40:43]\n"
    "ds_read_b64_tr_b16 v[126:127], v255 offset:4096\n"
    "s_waitcnt lgkmcnt(11)\n"
    "v_mfma_f32_16x16x32_bf16 v[48:51], v[232:235], v[96:99], v[48:51]\n"
    "ds_read_b64_tr_b16 v[128:129], v252 offset:1024\n"
    "v_mfma_f32_16x16x32_bf16 v[52:55], v[232:235], v[100:103], v[52:55]\n"
    "ds_read_b64_tr_b16 v[130:131], v252 offset:5120\n"
    "s_waitcnt lgkmcnt(10)\n"
    "v_mfma_f32_16x16x32_bf16 v[60:63], v[236:239], v[100:103], v[60:63]\n"
    "ds_read_b64_tr_b16 v[132:133], v253 offset:1024\n"
    "v_mfma_f32_16x16x32_bf16 v[56:59], v[236:239], v[96:99], v[56:59]\n"
    "ds_read_b64_tr_b16 v[134:135], v253 offset:5120\n"
    "s_waitcnt lgkmcnt(10)\n"
    "v_mfma_f32_16x16x32_bf16 v[0:3], v[240:243], v[104:107], v[0:3]\n"
    "ds_read_b64_tr_b16 v[232:233], v254 offset:1024\n"
    "ds_read_b128 v[112:115], v251 offset:16384\n"
    "v_mfma_f32_16x16x32_bf16 v[4:7], v[240:243], v[108:111], v[4:7]\n"
    "ds_read_b64_tr_b16 v[234:235], v254 offset:5120\n"
    "s_waitcnt lgkmcnt(11)\n"
    "v_mfma_f32_16x16x32_bf16 v[12:15], v[244:247], v[108:111], v[12:15]\n"
    "ds_read_b64_tr_b16 v[236:237], v255 offset:1024\n"
    "ds_read_b128 v[116:119], v251 offset:17408\n"
    "v_mfma_f32_16x16x32_bf16 v[8:11], v[244:247], v[104:107], v[8:11]\n"
    "ds_read_b64_tr_b16 v[238:239], v255 offset:5120\n"
    "s_waitcnt lgkmcnt(12)\n"
    "v_mfma_f32_16x16x32_bf16 v[16:19], v[120:123], v[104:107], v[16:19]\n"
    "s_barrier\n"
    "ds_read_b64_tr_b16 v[240:241], v252 offset:16384\n"
    "v_mfma_f32_16x16x32_bf16 v[20:23], v[120:123], v[108:111], v[20:23]\n"
    "ds_read_b64_tr_b16 v[242:243], v252 offset:20480\n"
    "s_waitcnt lgkmcnt(12)\n"
    "v_mfma_f32_16x16x32_bf16 v[28:31], v[124:127], v[108:111], v[28:31]\n"
    "ds_read_b64_tr_b16 v[244:245], v253 offset:16384\n"
    "v_mfma_f32_16x16x32_bf16 v[24:27], v[124:127], v[104:107], v[24:27]\n"
    "ds_read_b64_tr_b16 v[246:247], v253 offset:20480\n"
    "s_waitcnt lgkmcnt(12)\n"
    "v_mfma_f32_16x16x32_bf16 v[32:35], v[128:131], v[104:107], v[32:35]\n"
    "ds_read_b64_tr_b16 v[120:121], v254 offset:16384\n"
    "v_mfma_f32_16x16x32_bf16 v[36:39], v[128:131], v[108:111], v[36:39]\n"
    "ds_read_b64_tr_b16 v[122:123], v254 offset:20480\n"
    "s_waitcnt lgkmcnt(12)\n"
    "v_mfma_f32_16x16x32_bf16 v[44:47], v[132:135], v[108:111], v[44:47]\n"
    "ds_read_b64_tr_b16 v[124:125], v255 offset:16384\n"
    "v_mfma_f32_16x16x32_bf16 v[40:43], v[132:135], v[104:107], v[40:43]\n"
    "ds_read_b64_tr_b16 v[126:127], v255 offset:20480\n"
    "s_waitcnt lgkmcnt(11)\n"
    "v_mfma_f32_16x16x32_bf16 v[48:51], v[232:235], v[104:107], v[48:51]\n"
    "ds_read_b64_tr_b16 v[128:129], v252 offset:17408\n"
    "v_mfma_f32_16x16x32_bf16 v[52:55], v[232:235], v[108:111], v[52:55]\n"
    "ds_read_b64_tr_b16 v[130:131], v252 offset:21504\n"
    "s_waitcnt lgkmcnt(10)\n"
    "v_mfma_f32_16x16x32_bf16 v[60:63], v[236:239], v[108:111], v[60:63]\n"
    "ds_read_b64_tr_b16 v[132:133], v253 offset:17408\n"
    "v_mfma_f32_16x16x32_bf16 v[56:59], v[236:239], v[104:107], v[56:59]\n"
    "ds_read_b64_tr_b16 v[134:135], v253 offset:21504\n"
    "s_waitcnt lgkmcnt(10)\n"
    "v_mfma_f32_16x16x32_bf16 v[0:3], v[240:243], v[112:115], v[0:3]\n"
    "ds_read_b64_tr_b16 v[232:233], v254 offset:17408\n"
    "v_mfma_f32_16x16x32_bf16 v[4:7], v[240:243], v[116:119], v[4:7]\n"
    "ds_read_b64_tr_b16 v[234:235], v254 offset:21504\n"
    "s_waitcnt lgkmcnt(10)\n"
    "v_mfma_f32_16x16x32_bf16 v[12:15], v[244:247], v[116:119], v[12:15]\n"
    "ds_read_b64_tr_b16 v[236:237], v255 offset:17408\n"
    "v_mfma_f32_16x16x32_bf16 v[8:11], v[244:247], v[112:115], v[8:11]\n"
    "ds_read_b64_tr_b16 v[238:239], v255 offset:21504\n"
    "s_waitcnt lgkmcnt(10)\n"
    "v_mfma_f32_16x16x32_bf16 v[16:19], v[120:123], v[112:115], v[16:19]\n"
    "ds_read_b64_tr_b16 v[240:241], v252 offset:24576\n"
    "v_mfma_f32_16x16x32_bf16 v[20:23], v[120:123], v[116:119], v[20:23]\n"
    "ds_read_b64_tr_b16 v[242:243], v252 offset:28672\n"
    "s_waitcnt lgkmcnt(10)\n"
    "v_mfma_f32_16x16x32_bf16 v[28:31], v[124:127], v[116:119], v[28:31]\n"
    "ds_read_b64_tr_b16 v[244:245], v253 offset:24576\n"
    "v_mfma_f32_16x16x32_bf16 v[24:27], v[124:127], v[112:115], v[24:27]\n"
    "ds_read_b64_tr_b16 v[246:247], v253 offset:28672\n"
    "s_waitcnt lgkmcnt(10)\n"
    "v_mfma_f32_16x16x32_bf16 v[32:35], v[128:131], v[112:115], v[32:35]\n"
    "ds_read_b64_tr_b16 v[120:121], v254 offset:24576\n"
    "v_mfma_f32_16x16x32_bf16 v[36:39], v[128:131], v[116:119], v[36:39]\n"
    "ds_read_b64_tr_b16 v[122:123], v254 offset:28672\n"
    "s_waitcnt lgkmcnt(10)\n"
    "v_mfma_f32_16x16x32_bf16 v[44:47], v[132:135], v[116:119], v[44:47]\n"
    "ds_read_b64_tr_b16 v[124:125], v255 offset:24576\n"
    "v_mfma_f32_16x16x32_bf16 v[40:43], v[132:135], v[112:115], v[40:43]\n"
    "ds_read_b64_tr_b16 v[126:127], v255 offset:28672\n"
    "s_waitcnt lgkmcnt(10)\n"
    "v_mfma_f32_16x16x32_bf16 v[48:51], v[232:235], v[112:115], v[48:51]\n"
    "s_barrier\n"
    "ds_read_b64_tr_b16 v[128:129], v252 offset:25600\n"
    "v_mfma_f32_16x16x32_bf16 v[52:55], v[232:235], v[116:119], v[52:55]\n"
    "ds_read_b64_tr_b16 v[130:131], v252 offset:29696\n"
    "s_waitcnt lgkmcnt(10)\n"
    "v_mfma_f32_16x16x32_bf16 v[60:63], v[236:239], v[116:119], v[60:63]\n"
    "ds_read_b64_tr_b16 v[132:133], v253 offset:25600\n"
    "v_mfma_f32_16x16x32_bf16 v[56:59], v[236:239], v[112:115], v[56:59]\n"
    "ds_read_b64_tr_b16 v[134:135], v253 offset:29696\n"
    "s_waitcnt lgkmcnt(10)\n"
    "v_mfma_f32_16x16x32_bf16 v[0:3], v[240:243], v[152:155], v[0:3]\n"
    "ds_read_b64_tr_b16 v[232:233], v254 offset:25600\n"
    "ds_read_b128 v[96:99], v251 offset:40960\n"
    "v_mfma_f32_16x16x32_bf16 v[4:7], v[240:243], v[156:159], v[4:7]\n"
    "ds_read_b64_tr_b16 v[234:235], v254 offset:29696\n"
    "s_waitcnt lgkmcnt(11)\n"
    "v_mfma_f32_16x16x32_bf16 v[12:15], v[244:247], v[156:159], v[12:15]\n"
    "ds_read_b64_tr_b16 v[236:237], v255 offset:25600\n"
    "ds_read_b128 v[100:103], v251 offset:41984\n"
    "v_mfma_f32_16x16x32_bf16 v[8:11], v[244:247], v[152:155], v[8:11]\n"
    "ds_read_b64_tr_b16 v[238:239], v255 offset:29696\n"
    "s_waitcnt lgkmcnt(12)\n"
    "v_mfma_f32_16x16x32_bf16 v[16:19], v[120:123], v[152:155], v[16:19]\n"
    "ds_read_b64_tr_b16 v[240:241], v252 offset:40960\n"
    "v_mfma_f32_16x16x32_bf16 v[20:23], v[120:123], v[156:159], v[20:23]\n"
    "ds_read_b64_tr_b16 v[242:243], v252 offset:45056\n"
    "s_waitcnt lgkmcnt(12)\n"
    "v_mfma_f32_16x16x32_bf16 v[28:31], v[124:127], v[156:159], v[28:31]\n"
    "ds_read_b64_tr_b16 v[244:245], v253 offset:40960\n"
    "v_mfma_f32_16x16x32_bf16 v[24:27], v[124:127], v[152:155], v[24:27]\n"
    "ds_read_b64_tr_b16 v[246:247], v253 offset:45056\n"
    "s_waitcnt lgkmcnt(12)\n"
    "v_mfma_f32_16x16x32_bf16 v[32:35], v[128:131], v[152:155], v[32:35]\n"
    "ds_read_b64_tr_b16 v[120:121], v254 offset:40960\n"
    "v_mfma_f32_16x16x32_bf16 v[36:39], v[128:131], v[156:159], v[36:39]\n"
    "ds_read_b64_tr_b16 v[122:123], v254 offset:45056\n"
    "s_waitcnt lgkmcnt(12)\n"
    "v_mfma_f32_16x16x32_bf16 v[44:47], v[132:135], v[156:159], v[44:47]\n"
    "ds_read_b64_tr_b16 v[124:125], v255 offset:40960\n"
    "v_mfma_f32_16x16x32_bf16 v[40:43], v[132:135], v[152:155], v[40:43]\n"
    "ds_read_b64_tr_b16 v[126:127], v255 offset:45056\n"
    "s_waitcnt lgkmcnt(11)\n"
    "v_mfma_f32_16x16x32_bf16 v[48:51], v[232:235], v[152:155], v[48:51]\n"
    "ds_read_b64_tr_b16 v[128:129], v252 offset:41984\n"
    "v_mfma_f32_16x16x32_bf16 v[52:55], v[232:235], v[156:159], v[52:55]\n"
    "ds_read_b64_tr_b16 v[130:131], v252 offset:46080\n"
    "s_waitcnt lgkmcnt(10)\n"
    "v_mfma_f32_16x16x32_bf16 v[60:63], v[236:239], v[156:159], v[60:63]\n"
    "ds_read_b64_tr_b16 v[132:133], v253 offset:41984\n"
    "v_mfma_f32_16x16x32_bf16 v[56:59], v[236:239], v[152:155], v[56:59]\n"
    "ds_read_b64_tr_b16 v[134:135], v253 offset:46080\n"
    "s_waitcnt lgkmcnt(10)\n"
    "v_mfma_f32_16x16x32_bf16 v[0:3], v[240:243], v[96:99], v[0:3]\n"
    "ds_read_b64_tr_b16 v[232:233], v254 offset:41984\n"
    "ds_read_b128 v[104:107], v251 offset:57344\n"
    "v_mfma_f32_16x16x32_bf16 v[4:7], v[240:243], v[100:103], v[4:7]\n"
    "ds_read_b64_tr_b16 v[234:235], v254 offset:46080\n"
    "s_waitcnt lgkmcnt(11)\n"
    "v_mfma_f32_16x16x32_bf16 v[12:15], v[244:247], v[100:103], v[12:15]\n"
    "ds_read_b64_tr_b16 v[236:237], v255 offset:41984\n"
    "ds_read_b128 v[108:111], v251 offset:58368\n"
    "v_mfma_f32_16x16x32_bf16 v[8:11], v[244:247], v[96:99], v[8:11]\n"
    "ds_read_b64_tr_b16 v[238:239], v255 offset:46080\n"
    "s_waitcnt lgkmcnt(12)\n"
    "v_mfma_f32_16x16x32_bf16 v[16:19], v[120:123], v[96:99], v[16:19]\n"
    "s_waitcnt vmcnt(0)\n"
    "s_barrier\n"
    "ds_read_b64_tr_b16 v[240:241], v252 offset:57344\n"
    "v_mfma_f32_16x16x32_bf16 v[20:23], v[120:123], v[100:103], v[20:23]\n"
    "ds_read_b64_tr_b16 v[242:243], v252 offset:61440\n"
    "s_waitcnt lgkmcnt(12)\n"
    "v_mfma_f32_16x16x32_bf16 v[28:31], v[124:127], v[100:103], v[28:31]\n"
    "ds_read_b64_tr_b16 v[244:245], v253 offset:57344\n"
    "s_cmp_lg_u32 s66, 0\n"
    "s_cbranch_scc1 L_q14_%=\n"
    "s_mov_b64 exec, 0xffff\n"
    "buffer_store_dwordx2 v[160:161], v162, s[60:63], s43 offen offset:256 sc1\n"
    "s_mov_b64 exec, -1\n"
    "L_q14_%=:\n"
    "v_mfma_f32_16x16x32_bf16 v[24:27], v[124:127], v[96:99], v[24:27]\n"
    "ds_read_b64_tr_b16 v[246:247], v253 offset:61440\n"
    "s_waitcnt lgkmcnt(12)\n"
    "v_mfma_f32_16x16x32_bf16 v[32:35], v[128:131], v[96:99], v[32:35]\n"
    "ds_read_b64_tr_b16 v[120:121], v254 offset:57344\n"
    "v_mfma_f32_16x16x32_bf16 v[36:39], v[128:131], v[100:103], v[36:39]\n"
    "ds_read_b64_tr_b16 v[122:123], v254 offset:61440\n"
    "s_waitcnt lgkmcnt(12)\n"
    "v_mfma_f32_16x16x32_bf16 v[44:47], v[132:135], v[100:103], v[44:47]\n"
    "ds_read_b64_tr_b16 v[124:125], v255 offset:57344\n"
    "v_add_u32 v251, 0x6000, v251\n"
    "v_mfma_f32_16x16x32_bf16 v[40:43], v[132:135], v[96:99], v[40:43]\n"
    "ds_read_b64_tr_b16 v[126:127], v255 offset:61440\n"
    "s_waitcnt lgkmcnt(11)\n"
    "v_mfma_f32_16x16x32_bf16 v[48:51], v[232:235], v[96:99], v[48:51]\n"
    "ds_read_b64_tr_b16 v[128:129], v252 offset:58368\n"
    "v_mfma_f32_16x16x32_bf16 v[52:55], v[232:235], v[100:103], v[52:55]\n"
    "ds_read_b64_tr_b16 v[130:131], v252 offset:62464\n"
    "s_waitcnt lgkmcnt(10)\n"
    "v_mfma_f32_16x16x32_bf16 v[60:63], v[236:239], v[100:103], v[60:63]\n"
    "ds_read_b64_tr_b16 v[132:133], v253 offset:58368\n"
    "v_add_u32 v252, 0xfffec000, v252\n"
    "v_mfma_f32_16x16x32_bf16 v[56:59], v[236:239], v[96:99], v[56:59]\n"
    "ds_read_b64_tr_b16 v[134:135], v253 offset:62464\n"
    "s_waitcnt lgkmcnt(10)\n"
    "v_mfma_f32_16x16x32_bf16 v[0:3], v[240:243], v[104:107], v[0:3]\n"
    "ds_read_b64_tr_b16 v[232:233], v254 offset:58368\n"
    "ds_read_b128 v[112:115], v251 offset:49152\n"
    "v_mfma_f32_16x16x32_bf16 v[4:7], v[240:243], v[108:111], v[4:7]\n"
    "ds_read_b64_tr_b16 v[234:235], v254 offset:62464\n"
    "s_waitcnt lgkmcnt(11)\n"
    "v_mfma_f32_16x16x32_bf16 v[12:15], v[244:247], v[108:111], v[12:15]\n"
    "ds_read_b64_tr_b16 v[236:237], v255 offset:58368\n"
    "ds_read_b128 v[116:119], v251 offset:50176\n"
    "v_mfma_f32_16x16x32_bf16 v[8:11], v[244:247], v[104:107], v[8:11]\n"
    "ds_read_b64_tr_b16 v[238:239], v255 offset:62464\n"
    "s_waitcnt lgkmcnt(12)\n"
    "v_mfma_f32_16x16x32_bf16 v[16:19], v[120:123], v[104:107], v[16:19]\n"
    "ds_read_b64_tr_b16 v[240:241], v252 offset:0\n"
    "v_add_u32 v253, 0xfffec000, v253\n"
    "v_mfma_f32_16x16x32_bf16 v[20:23], v[120:123], v[108:111], v[20:23]\n"
    "ds_read_b64_tr_b16 v[242:243], v252 offset:4096\n"
    "s_waitcnt lgkmcnt(12)\n"
    "v_mfma_f32_16x16x32_bf16 v[28:31], v[124:127], v[108:111], v[28:31]\n"
    "ds_read_b64_tr_b16 v[244:245], v253 offset:0\n"
    "v_add_u32 v254, 0xfffec000, v254\n"
    "v_mfma_f32_16x16x32_bf16 v[24:27], v[124:127], v[104:107], v[24:27]\n"
    "ds_read_b64_tr_b16 v[246:247], v253 offset:4096\n"
    "s_waitcnt lgkmcnt(12)\n"
    "v_mfma_f32_16x16x32_bf16 v[32:35], v[128:131], v[104:107], v[32:35]\n"
    "ds_read_b64_tr_b16 v[120:121], v254 offset:0\n"
    "v_add_u32 v255, 0xfffec000, v255\n"
    "v_mfma_f32_16x16x32_bf16 v[36:39], v[128:131], v[108:111], v[36:39]\n"
    "ds_read_b64_tr_b16 v[122:123], v254 offset:4096\n"
    "s_waitcnt lgkmcnt(12)\n"
    "v_mfma_f32_16x16x32_bf16 v[44:47], v[132:135], v[108:111], v[44:47]\n"
    "ds_read_b64_tr_b16 v[124:125], v255 offset:0\n"
    "v_add_u32 v251, 0xfffea000, v251\n"
    "v_mfma_f32_16x16x32_bf16 v[40:43], v[132:135], v[104:107], v[40:43]\n"
    "ds_read_b64_tr_b16 v[126:127], v255 offset:4096\n"
    "s_waitcnt lgkmcnt(11)\n"
    "v_mfma_f32_16x16x32_bf16 v[48:51], v[232:235], v[104:107], v[48:51]\n"
    "s_barrier\n"
    "ds_read_b64_tr_b16 v[128:129], v252 offset:1024\n"
    "v_mfma_f32_16x16x32_bf16 v[52:55], v[232:235], v[108:111], v[52:55]\n"
    "ds_read_b64_tr_b16 v[130:131], v252 offset:5120\n"
    "s_waitcnt lgkmcnt(10)\n"
    "v_mfma_f32_16x16x32_bf16 v[60:63], v[236:239], v[108:111], v[60:63]\n"
    "ds_read_b64_tr_b16 v[132:133], v253 offset:1024\n"
    "v_mfma_f32_16x16x32_bf16 v[56:59], v[236:239], v[104:107], v[56:59]\n"
    "ds_read_b64_tr_b16 v[134:135], v253 offset:5120\n"
    "s_waitcnt lgkmcnt(10)\n"
    "v_mfma_f32_16x16x32_bf16 v[0:3], v[240:243], v[112:115], v[0:3]\n"
    "ds_read_b64_tr_b16 v[232:233], v254 offset:1024\n"
    "ds_read_b128 v[96:99], v251 offset:0\n"
    "v_mfma_f32_16x16x32_bf16 v[4:7], v[240:243], v[116:119], v[4:7]\n"
    "ds_read_b64_tr_b16 v[234:235], v254 offset:5120\n"
    "s_waitcnt lgkmcnt(11)\n"
    "v_mfma_f32_16x16x32_bf16 v[12:15], v[244:247], v[116:119], v[12:15]\n"
    "ds_read_b64_tr_b16 v[236:237], v255 offset:1024\n"
    "ds_read_b128 v[100:103], v251 offset:1024\n"
    "v_mfma_f32_16x16x32_bf16 v[8:11], v[244:247], v[112:115], v[8:11]\n"
    "ds_read_b64_tr_b16 v[238:239], v255 offset:5120\n"
    "s_waitcnt lgkmcnt(12)\n"
    "v_mfma_f32_16x16x32_bf16 v[16:19], v[120:123], v[112:115], v[16:19]\n"
    "ds_read_b64_tr_b16 v[240:241], v252 offset:16384\n"
    "v_mfma_f32_16x16x32_bf16 v[20:23], v[120:123], v[116:119], v[20:23]\n"
    "ds_read_b64_tr_b16 v[242:243], v252 offset:20480\n"
    "s_waitcnt lgkmcnt(12)\n"
    "v_mfma_f32_16x16x32_bf16 v[28:31], v[124:127], v[116:119], v[28:31]\n"
    "ds_read_b64_tr_b16 v[244:245], v253 offset:16384\n"
    "v_mfma_f32_16x16x32_bf16 v[24:27], v[124:127], v[112:115], v[24:27]\n"
    "ds_read_b64_tr_b16 v[246:247], v253 offset:20480\n"
    "s_waitcnt lgkmcnt(12)\n"
    "v_mfma_f32_16x16x32_bf16 v[32:35], v[128:131], v[112:115], v[32:35]\n"
    "ds_read_b64_tr_b16 v[120:121], v254 offset:16384\n"
    "v_mfma_f32_16x16x32_bf16 v[36:39], v[128:131], v[116:119], v[36:39]\n"
    "ds_read_b64_tr_b16 v[122:123], v254 offset:20480\n"
    "s_waitcnt lgkmcnt(12)\n"
    "v_mfma_f32_16x16x32_bf16 v[44:47], v[132:135], v[116:119], v[44:47]\n"
    "ds_read_b64_tr_b16 v[124:125], v255 offset:16384\n"
    "v_mfma_f32_16x16x32_bf16 v[40:43], v[132:135], v[112:115], v[40:43]\n"
    "ds_read_b64_tr_b16 v[126:127], v255 offset:20480\n"
    "s_waitcnt lgkmcnt(11)\n"
    "v_mfma_f32_16x16x32_bf16 v[48:51], v[232:235], v[112:115], v[48:51]\n"
    "ds_read_b64_tr_b16 v[128:129], v252 offset:17408\n"
    "v_mfma_f32_16x16x32_bf16 v[52:55], v[232:235], v[116:119], v[52:55]\n"
    "ds_read_b64_tr_b16 v[130:131], v252 offset:21504\n"
    "s_waitcnt lgkmcnt(10)\n"
    "v_mfma_f32_16x16x32_bf16 v[60:63], v[236:239], v[116:119], v[60:63]\n"
    "ds_read_b64_tr_b16 v[132:133], v253 offset:17408\n"
    "v_mfma_f32_16x16x32_bf16 v[56:59], v[236:239], v[112:115], v[56:59]\n"
    "ds_read_b64_tr_b16 v[134:135], v253 offset:21504\n"
    "s_waitcnt lgkmcnt(10)\n"
    "v_mfma_f32_16x16x32_bf16 v[0:3], v[240:243], v[96:99], v[0:3]\n"
    "ds_read_b64_tr_b16 v[232:233], v254 offset:17408\n"
    "ds_read_b128 v[104:107], v251 offset:16384\n"
    "v_mfma_f32_16x16x32_bf16 v[4:7], v[240:243], v[100:103], v[4:7]\n"
    "ds_read_b64_tr_b16 v[234:235], v254 offset:21504\n"
    "s_waitcnt lgkmcnt(11)\n"
    "v_mfma_f32_16x16x32_bf16 v[12:15], v[244:247], v[100:103], v[12:15]\n"
    "ds_read_b64_tr_b16 v[236:237], v255 offset:17408\n"
    "ds_read_b128 v[108:111], v251 offset:17408\n"
    "v_mfma_f32_16x16x32_bf16 v[8:11], v[244:247], v[96:99], v[8:11]\n"
    "ds_read_b64_tr_b16 v[238:239], v255 offset:21504\n"
    "s_waitcnt lgkmcnt(12)\n"
    "v_mfma_f32_16x16x32_bf16 v[16:19], v[120:123], v[96:99], v[16:19]\n"
    "s_barrier\n"
    "ds_read_b64_tr_b16 v[240:241], v252 offset:32768\n"
    "v_mfma_f32_16x16x32_bf16 v[20:23], v[120:123], v[100:103], v[20:23]\n"
    "ds_read_b64_tr_b16 v[242:243], v252 offset:36864\n"
    "s_waitcnt lgkmcnt(12)\n"
    "v_mfma_f32_16x16x32_bf16 v[28:31], v[124:127], v[100:103], v[28:31]\n"
    "ds_read_b64_tr_b16 v[244:245], v253 offset:32768\n"
    "v_mfma_f32_16x16x32_bf16 v[24:27], v[124:127], v[96:99], v[24:27]\n"
    "ds_read_b64_tr_b16 v[246:247], v253 offset:36864\n"
    "s_waitcnt lgkmcnt(12)\n"
    "v_mfma_f32_16x16x32_bf16 v[32:35], v[128:131], v[96:99], v[32:35]\n"
    "ds_read_b64_tr_b16 v[120:121], v254 offset:32768\n"
    "v_mfma_f32_16x16x32_bf16 v[36:39], v[128:131], v[100:103], v[36:39]\n"
    "ds_read_b64_tr_b16 v[122:123], v254 offset:36864\n"
    "s_waitcnt lgkmcnt(12)\n"
    "v_mfma_f32_16x16x32_bf16 v[44:47], v[132:135], v[100:103], v[44:47]\n"
    "ds_read_b64_tr_b16 v[124:125], v255 offset:32768\n"
    "v_mfma_f32_16x16x32_bf16 v[40:43], v[132:135], v[96:99], v[40:43]\n"
    "ds_read_b64_tr_b16 v[126:127], v255 offset:36864\n"
    "s_waitcnt lgkmcnt(11)\n"
    "v_mfma_f32_16x16x32_bf16 v[48:51], v[232:235], v[96:99], v[48:51]\n"
    "ds_read_b64_tr_b16 v[128:129], v252 offset:33792\n"
    "v_mfma_f32_16x16x32_bf16 v[52:55], v[232:235], v[100:103], v[52:55]\n"
    "ds_read_b64_tr_b16 v[130:131], v252 offset:37888\n"
    "s_waitcnt lgkmcnt(10)\n"
    "v_mfma_f32_16x16x32_bf16 v[60:63], v[236:239], v[100:103], v[60:63]\n"
    "ds_read_b64_tr_b16 v[132:133], v253 offset:33792\n"
    "v_mfma_f32_16x16x32_bf16 v[56:59], v[236:239], v[96:99], v[56:59]\n"
    "ds_read_b64_tr_b16 v[134:135], v253 offset:37888\n"
    "s_waitcnt lgkmcnt(10)\n"
    "v_mfma_f32_16x16x32_bf16 v[0:3], v[240:243], v[104:107], v[0:3]\n"
    "ds_read_b64_tr_b16 v[232:233], v254 offset:33792\n"
    "ds_read_b128 v[112:115], v251 offset:32768\n"
    "v_mfma_f32_16x16x32_bf16 v[4:7], v[240:243], v[108:111], v[4:7]\n"
    "ds_read_b64_tr_b16 v[234:235], v254 offset:37888\n"
    "s_waitcnt lgkmcnt(11)\n"
    "v_mfma_f32_16x16x32_bf16 v[12:15], v[244:247], v[108:111], v[12:15]\n"
    "ds_read_b64_tr_b16 v[236:237], v255 offset:33792\n"
    "ds_read_b128 v[116:119], v251 offset:33792\n"
    "v_mfma_f32_16x16x32_bf16 v[8:11], v[244:247], v[104:107], v[8:11]\n"
    "ds_read_b64_tr_b16 v[238:239], v255 offset:37888\n"
    "s_waitcnt lgkmcnt(12)\n"
    "v_mfma_f32_16x16x32_bf16 v[16:19], v[120:123], v[104:107], v[16:19]\n"
    "ds_read_b64_tr_b16 v[240:241], v252 offset:49152\n"
    "v_mfma_f32_16x16x32_bf16 v[20:23], v[120:123], v[108:111], v[20:23]\n"
    "ds_read_b64_tr_b16 v[242:243], v252 offset:53248\n"
    "s_waitcnt lgkmcnt(12)\n"
    "v_mfma_f32_16x16x32_bf16 v[28:31], v[124:127], v[108:111], v[28:31]\n"
    "ds_read_b64_tr_b16 v[244:245], v253 offset:49152\n"
    "v_mfma_f32_16x16x32_bf16 v[24:27], v[124:127], v[104:107], v[24:27]\n"
    "ds_read_b64_tr_b16 v[246:247], v253 offset:53248\n"
    "s_waitcnt lgkmcnt(12)\n"
    "v_mfma_f32_16x16x32_bf16 v[32:35], v[128:131], v[104:107], v[32:35]\n"
    "ds_read_b64_tr_b16 v[120:121], v254 offset:49152\n"
    "v_mfma_f32_16x16x32_bf16 v[36:39], v[128:131], v[108:111], v[36:39]\n"
    "ds_read_b64_tr_b16 v[122:123], v254 offset:53248\n"
    "s_waitcnt lgkmcnt(12)\n"
    "v_mfma_f32_16x16x32_bf16 v[44:47], v[132:135], v[108:111], v[44:47]\n"
    "ds_read_b64_tr_b16 v[124:125], v255 offset:49152\n"
    "v_mfma_f32_16x16x32_bf16 v[40:43], v[132:135], v[104:107], v[40:43]\n"
    "ds_read_b64_tr_b16 v[126:127], v255 offset:53248\n"
    "s_waitcnt lgkmcnt(11)\n"
    "v_mfma_f32_16x16x32_bf16 v[48:51], v[232:235], v[104:107], v[48:51]\n"
    "s_barrier\n"
    "ds_read_b64_tr_b16 v[128:129], v252 offset:50176\n"
    "v_mfma_f32_16x16x32_bf16 v[52:55], v[232:235], v[108:111], v[52:55]\n"
    "ds_read_b64_tr_b16 v[130:131], v252 offset:54272\n"
    "s_waitcnt lgkmcnt(10)\n"
    "v_mfma_f32_16x16x32_bf16 v[60:63], v[236:239], v[108:111], v[60:63]\n"
    "ds_read_b64_tr_b16 v[132:133], v253 offset:50176\n"
    "v_add_u32 v252, 0x10000, v252\n"
    "v_mfma_f32_16x16x32_bf16 v[56:59], v[236:239], v[104:107], v[56:59]\n"
    "ds_read_b64_tr_b16 v[134:135], v253 offset:54272\n"
    "s_waitcnt lgkmcnt(10)\n"
    "v_mfma_f32_16x16x32_bf16 v[0:3], v[240:243], v[112:115], v[0:3]\n"
    "ds_read_b64_tr_b16 v[232:233], v254 offset:50176\n"
    "ds_read_b128 v[96:99], v251 offset:49152\n"
    "v_mfma_f32_16x16x32_bf16 v[4:7], v[240:243], v[116:119], v[4:7]\n"
    "ds_read_b64_tr_b16 v[234:235], v254 offset:54272\n"
    "s_waitcnt lgkmcnt(11)\n"
    "v_mfma_f32_16x16x32_bf16 v[12:15], v[244:247], v[116:119], v[12:15]\n"
    "ds_read_b64_tr_b16 v[236:237], v255 offset:50176\n"
    "ds_read_b128 v[100:103], v251 offset:50176\n"
    "v_mfma_f32_16x16x32_bf16 v[8:11], v[244:247], v[112:115], v[8:11]\n"
    "ds_read_b64_tr_b16 v[238:239], v255 offset:54272\n"
    "s_waitcnt lgkmcnt(12)\n"
    "v_mfma_f32_16x16x32_bf16 v[16:19], v[120:123], v[112:115], v[16:19]\n"
    "ds_read_b64_tr_b16 v[240:241], v252 offset:0\n"
    "v_add_u32 v253, 0x10000, v253\n"
    "v_mfma_f32_16x16x32_bf16 v[20:23], v[120:123], v[116:119], v[20:23]\n"
    "L_st8_%=:\n"
    "s_cbranch_execz L_bk%=\n"
    "ds_read_b64_tr_b16 v[242:243], v252 offset:4096\n"
    "s_waitcnt lgkmcnt(12)\n"
    "v_mfma_f32_16x16x32_bf16 v[28:31], v[124:127], v[116:119], v[28:31]\n"
    "ds_read_b64_tr_b16 v[244:245], v253 offset:0\n"
    "v_add_u32 v254, 0x10000, v254\n"
    "v_mfma_f32_16x16x32_bf16 v[24:27], v[124:127], v[112:115], v[24:27]\n"
    "ds_read_b64_tr_b16 v[246:247], v253 offset:4096\n"
    "s_waitcnt lgkmcnt(12)\n"
    "v_mfma_f32_16x16x32_bf16 v[32:35], v[128:131], v[112:115], v[32:35]\n"
    "ds_read_b64_tr_b16 v[120:121], v254 offset:0\n"
    "v_add_u32 v255, 0x10000, v255\n"
    "v_mfma_f32_16x16x32_bf16 v[36:39], v[128:131], v[116:119], v[36:39]\n"
    "ds_read_b64_tr_b16 v[122:123], v254 offset:4096\n"
    "s_waitcnt lgkmcnt(12)\n"
    "v_mfma_f32_16x16x32_bf16 v[44:47], v[132:135], v[116:119], v[44:47]\n"
    "ds_read_b64_tr_b16 v[124:125], v255 offset:0\n"
    "v_add_u32 v251, 0x10000, v251\n"
    "v_mfma_f32_16x16x32_bf16 v[40:43], v[132:135], v[112:115], v[40:43]\n"
    "ds_read_b64_tr_b16 v[126:127], v255 offset:4096\n"
    "s_waitcnt lgkmcnt(11)\n"
    "v_mfma_f32_16x16x32_bf16 v[48:51], v[232:235], v[112:115], v[48:51]\n"
    "ds_read_b64_tr_b16 v[128:129], v252 offset:1024\n"
    "v_mfma_f32_16x16x32_bf16 v[52:55], v[232:235], v[116:119], v[52:55]\n"
    "ds_read_b64_tr_b16 v[130:131], v252 offset:5120\n"
    "s_waitcnt lgkmcnt(10)\n"
    "v_mfma_f32_16x16x32_bf16 v[60:63], v[236:239], v[116:119], v[60:63]\n"
    "ds_read_b64_tr_b16 v[132:133], v253 offset:1024\n"
    "v_mfma_f32_16x16x32_bf16 v[56:59], v[236:239], v[112:115], v[56:59]\n"
    "ds_read_b64_tr_b16 v[134:135], v253 offset:5120\n"
    "s_waitcnt lgkmcnt(10)\n"
    "v_mfma_f32_16x16x32_bf16 v[0:3], v[240:243], v[96:99], v[0:3]\n"
    "ds_read_b64_tr_b16 v[232:233], v254 offset:1024\n"
    "ds_read_b128 v[104:107], v251 offset:0\n"
    "v_mfma_f32_16x16x32_bf16 v[4:7], v[240:243], v[100:103], v[4:7]\n"
    "ds_read_b64_tr_b16 v[234:235], v254 offset:5120\n"
    "s_waitcnt lgkmcnt(11)\n"
    "v_mfma_f32_16x16x32_bf16 v[12:15], v[244:247], v[100:103], v[12:15]\n"
    "ds_read_b64_tr_b16 v[236:237], v255 offset:1024\n"
    "ds_read_b128 v[108:111], v251 offset:1024\n"
    "v_mfma_f32_16x16x32_bf16 v[8:11], v[244:247], v[96:99], v[8:11]\n"
    "ds_read_b64_tr_b16 v[238:239], v255 offset:5120\n"
    "s_waitcnt lgkmcnt(12)\n"
    "v_mfma_f32_16x16x32_bf16 v[16:19], v[120:123], v[96:99], v[16:19]\n"
    "s_barrier\n"
    "ds_read_b64_tr_b16 v[240:241], v252 offset:16384\n"
    "v_mfma_f32_16x16x32_bf16 v[20:23], v[120:123], v[100:103], v[20:23]\n"
    "ds_read_b64_tr_b16 v[242:243], v252 offset:20480\n"
    "s_waitcnt lgkmcnt(12)\n"
    "v_mfma_f32_16x16x32_bf16 v[28:31], v[124:127], v[100:103], v[28:31]\n"
    "ds_read_b64_tr_b16 v[244:245], v253 offset:16384\n"
    "v_mfma_f32_16x16x32_bf16 v[24:27], v[124:127], v[96:99], v[24:27]\n"
    "ds_read_b64_tr_b16 v[246:247], v253 offset:20480\n"
    "s_waitcnt lgkmcnt(12)\n"
    "v_mfma_f32_16x16x32_bf16 v[32:35], v[128:131], v[96:99], v[32:35]\n"
    "ds_read_b64_tr_b16 v[120:121], v254 offset:16384\n"
    "v_mfma_f32_16x16x32_bf16 v[36:39], v[128:131], v[100:103], v[36:39]\n"
    "ds_read_b64_tr_b16 v[122:123], v254 offset:20480\n"
    "s_waitcnt lgkmcnt(12)\n"
    "v_mfma_f32_16x16x32_bf16 v[44:47], v[132:135], v[100:103], v[44:47]\n"
    "ds_read_b64_tr_b16 v[124:125], v255 offset:16384\n"
    "v_mfma_f32_16x16x32_bf16 v[40:43], v[132:135], v[96:99], v[40:43]\n"
    "ds_read_b64_tr_b16 v[126:127], v255 offset:20480\n"
    "s_waitcnt lgkmcnt(11)\n"
    "v_mfma_f32_16x16x32_bf16 v[48:51], v[232:235], v[96:99], v[48:51]\n"
    "ds_read_b64_tr_b16 v[128:129], v252 offset:17408\n"
    "v_mfma_f32_16x16x32_bf16 v[52:55], v[232:235], v[100:103], v[52:55]\n"
    "ds_read_b64_tr_b16 v[130:131], v252 offset:21504\n"
    "s_waitcnt lgkmcnt(10)\n"
    "v_mfma_f32_16x16x32_bf16 v[60:63], v[236:239], v[100:103], v[60:63]\n"
    "ds_read_b64_tr_b16 v[132:133], v253 offset:17408\n"
    "v_mfma_f32_16x16x32_bf16 v[56:59], v[236:239], v[96:99], v[56:59]\n"
    "ds_read_b64_tr_b16 v[134:135], v253 offset:21504\n"
    "s_waitcnt lgkmcnt(10)\n"
    "v_mfma_f32_16x16x32_bf16 v[0:3], v[240:243], v[104:107], v[0:3]\n"
    "ds_read_b64_tr_b16 v[232:233], v254 offset:17408\n"
    "ds_read_b128 v[112:115], v251 offset:16384\n"
    "v_mfma_f32_16x16x32_bf16 v[4:7], v[240:243], v[108:111], v[4:7]\n"
    "ds_read_b64_tr_b16 v[234:235], v254 offset:21504\n"
    "s_waitcnt lgkmcnt(11)\n"
    "v_mfma_f32_16x16x32_bf16 v[12:15], v[244:247], v[108:111], v[12:15]\n"
    "ds_read_b64_tr_b16 v[236:237], v255 offset:17408\n"
    "ds_read_b128 v[116:119], v251 offset:17408\n"
    "v_mfma_f32_16x16x32_bf16 v[8:11], v[244:247], v[104:107], v[8:11]\n"
    "ds_read_b64_tr_b16 v[238:239], v255 offset:21504\n"
    "s_waitcnt lgkmcnt(12)\n"
    "v_mfma_f32_16x16x32_bf16 v[16:19], v[120:123], v[104:107], v[16:19]\n"
    "ds_read_b64_tr_b16 v[240:241], v252 offset:32768\n"
    "v_mfma_f32_16x16x32_bf16 v[20:23], v[120:123], v[108:111], v[20:23]\n"
    "ds_read_b64_tr_b16 v[242:243], v252 offset:36864\n"
    "s_waitcnt lgkmcnt(12)\n"
    "v_mfma_f32_16x16x32_bf16 v[28:31], v[124:127], v[108:111], v[28:31]\n"
    "ds_read_b64_tr_b16 v[244:245], v253 offset:32768\n"
    "v_mfma_f32_16x16x32_bf16 v[24:27], v[124:127], v[104:107], v[24:27]\n"
    "ds_read_b64_tr_b16 v[246:247], v253 offset:36864\n"
    "s_waitcnt lgkmcnt(12)\n"
    "v_mfma_f32_16x16x32_bf16 v[32:35], v[128:131], v[104:107], v[32:35]\n"
    "ds_read_b64_tr_b16 v[120:121], v254 offset:32768\n"
    "v_mfma_f32_16x16x32_bf16 v[36:39], v[128:131], v[108:111], v[36:39]\n"
    "ds_read_b64_tr_b16 v[122:123], v254 offset:36864\n"
    "s_waitcnt lgkmcnt(12)\n"
    "v_mfma_f32_16x16x32_bf16 v[44:47], v[132:135], v[108:111], v[44:47]\n"
    "ds_read_b64_tr_b16 v[124:125], v255 offset:32768\n"
    "v_mfma_f32_16x16x32_bf16 v[40:43], v[132:135], v[104:107], v[40:43]\n"
    "ds_read_b64_tr_b16 v[126:127], v255 offset:36864\n"
    "s_waitcnt lgkmcnt(11)\n"
    "v_mfma_f32_16x16x32_bf16 v[48:51], v[232:235], v[104:107], v[48:51]\n"
    "s_barrier\n"
    "ds_read_b64_tr_b16 v[128:129], v252 offset:33792\n"
    "v_mfma_f32_16x16x32_bf16 v[52:55], v[232:235], v[108:111], v[52:55]\n"
    "ds_read_b64_tr_b16 v[130:131], v252 offset:37888\n"
    "s_waitcnt lgkmcnt(10)\n"
    "v_mfma_f32_16x16x32_bf16 v[60:63], v[236:239], v[108:111], v[60:63]\n"
    "ds_read_b64_tr_b16 v[132:133], v253 offset:33792\n"
    "v_mfma_f32_16x16x32_bf16 v[56:59], v[236:239], v[104:107], v[56:59]\n"
    "ds_read_b64_tr_b16 v[134:135], v253 offset:37888\n"
    "s_waitcnt lgkmcnt(10)\n"
    "v_mfma_f32_16x16x32_bf16 v[0:3], v[240:243], v[112:115], v[0:3]\n"
    "ds_read_b64_tr_b16 v[232:233], v254 offset:33792\n"
    "ds_read_b128 v[96:99], v251 offset:32768\n"
    "v_mfma_f32_16x16x32_bf16 v[4:7], v[240:243], v[116:119], v[4:7]\n"
    "ds_read_b64_tr_b16 v[234:235], v254 offset:37888\n"
    "s_waitcnt lgkmcnt(11)\n"
    "v_mfma_f32_16x16x32_bf16 v[12:15], v[244:247], v[116:119], v[12:15]\n"
    "ds_read_b64_tr_b16 v[236:237], v255 offset:33792\n"
    "ds_read_b128 v[100:103], v251 offset:33792\n"
    "v_mfma_f32_16x16x32_bf16 v[8:11], v[244:247], v[112:115], v[8:11]\n"
    "ds_read_b64_tr_b16 v[238:239], v255 offset:37888\n"
    "s_waitcnt lgkmcnt(12)\n"
    "v_mfma_f32_16x16x32_bf16 v[16:19], v[120:123], v[112:115], v[16:19]\n"
    "ds_read_b64_tr_b16 v[240:241], v252 offset:49152\n"
    "v_mfma_f32_16x16x32_bf16 v[20:23], v[120:123], v[116:119], v[20:23]\n"
    "ds_read_b64_tr_b16 v[242:243], v252 offset:53248\n"
    "s_waitcnt lgkmcnt(12)\n"
    "v_mfma_f32_16x16x32_bf16 v[28:31], v[124:127], v[116:119], v[28:31]\n"
    "ds_read_b64_tr_b16 v[244:245], v253 offset:49152\n"
    "v_mfma_f32_16x16x32_bf16 v[24:27], v[124:127], v[112:115], v[24:27]\n"
    "ds_read_b64_tr_b16 v[246:247], v253 offset:53248\n"
    "s_waitcnt lgkmcnt(12)\n"
    "v_mfma_f32_16x16x32_bf16 v[32:35], v[128:131], v[112:115], v[32:35]\n"
    "ds_read_b64_tr_b16 v[120:121], v254 offset:49152\n"
    "v_mfma_f32_16x16x32_bf16 v[36:39], v[128:131], v[116:119], v[36:39]\n"
    "ds_read_b64_tr_b16 v[122:123], v254 offset:53248\n"
    "s_waitcnt lgkmcnt(12)\n"
    "v_mfma_f32_16x16x32_bf16 v[44:47], v[132:135], v[116:119], v[44:47]\n"
    "ds_read_b64_tr_b16 v[124:125], v255 offset:49152\n"
    "v_mfma_f32_16x16x32_bf16 v[40:43], v[132:135], v[112:115], v[40:43]\n"
    "ds_read_b64_tr_b16 v[126:127], v255 offset:53248\n"
    "s_waitcnt lgkmcnt(11)\n"
    "v_mfma_f32_16x16x32_bf16 v[48:51], v[232:235], v[112:115], v[48:51]\n"
    "ds_read_b64_tr_b16 v[128:129], v252 offset:50176\n"
    "v_mfma_f32_16x16x32_bf16 v[52:55], v[232:235], v[116:119], v[52:55]\n"
    "ds_read_b64_tr_b16 v[130:131], v252 offset:54272\n"
    "s_waitcnt lgkmcnt(10)\n"
    "v_mfma_f32_16x16x32_bf16 v[60:63], v[236:239], v[116:119], v[60:63]\n"
    "ds_read_b64_tr_b16 v[132:133], v253 offset:50176\n"
    "v_add_u32 v252, 0x8000, v252\n"
    "v_mfma_f32_16x16x32_bf16 v[56:59], v[236:239], v[112:115], v[56:59]\n"
    "ds_read_b64_tr_b16 v[134:135], v253 offset:54272\n"
    "s_waitcnt lgkmcnt(10)\n"
    "v_mfma_f32_16x16x32_bf16 v[0:3], v[240:243], v[96:99], v[0:3]\n"
    "ds_read_b64_tr_b16 v[232:233], v254 offset:50176\n"
    "ds_read_b128 v[104:107], v251 offset:49152\n"
    "v_mfma_f32_16x16x32_bf16 v[4:7], v[240:243], v[100:103], v[4:7]\n"
    "ds_read_b64_tr_b16 v[234:235], v254 offset:54272\n"
    "s_waitcnt lgkmcnt(11)\n"
    "v_mfma_f32_16x16x32_bf16 v[12:15], v[244:247], v[100:103], v[12:15]\n"
    "ds_read_b64_tr_b16 v[236:237], v255 offset:50176\n"
    "ds_read_b128 v[108:111], v251 offset:50176\n"
    "v_mfma_f32_16x16x32_bf16 v[8:11], v[244:247], v[96:99], v[8:11]\n"
    "ds_read_b64_tr_b16 v[238:239], v255 offset:54272\n"
    "s_waitcnt lgkmcnt(12)\n"
    "v_mfma_f32_16x16x32_bf16 v[16:19], v[120:123], v[96:99], v[16:19]\n"
    "s_barrier\n"
    "ds_read_b64_tr_b16 v[240:241], v252 offset:32768\n"
    "v_mfma_f32_16x16x32_bf16 v[20:23], v[120:123], v[100:103], v[20:23]\n"
    "ds_read_b64_tr_b16 v[242:243], v252 offset:36864\n"
    "s_waitcnt lgkmcnt(12)\n"
    "v_mfma_f32_16x16x32_bf16 v[28:31], v[124:127], v[100:103], v[28:31]\n"
    "v_add_u32 v253, 0x8000, v253\n"
    "ds_read_b64_tr_b16 v[244:245], v253 offset:32768\n"
    "v_add_u32 v254, 0x8000, v254\n"
    "v_mfma_f32_16x16x32_bf16 v[24:27], v[124:127], v[96:99], v[24:27]\n"
    "ds_read_b64_tr_b16 v[246:247], v253 offset:36864\n"
    "s_waitcnt lgkmcnt(12)\n"
    "v_mfma_f32_16x16x32_bf16 v[32:35], v[128:131], v[96:99], v[32:35]\n"
    "ds_read_b64_tr_b16 v[120:121], v254 offset:32768\n"
    "v_add_u32 v255, 0x8000, v255\n"
    "v_mfma_f32_16x16x32_bf16 v[36:39], v[128:131], v[100:103], v[36:39]\n"
    "ds_read_b64_tr_b16 v[122:123], v254 offset:36864\n"
    "s_waitcnt lgkmcnt(12)\n"
    "v_mfma_f32_16x16x32_bf16 v[44:47], v[132:135], v[100:103], v[44:47]\n"
    "ds_read_b64_tr_b16 v[124:125], v255 offset:32768\n"
    "v_add_u32 v251, 0x6000, v251\n"
    "v_mfma_f32_16x16x32_bf16 v[40:43], v[132:135], v[96:99], v[40:43]\n"
    "ds_read_b64_tr_b16 v[126:127], v255 offset:36864\n"
    "s_waitcnt lgkmcnt(11)\n"
    "v_mfma_f32_16x16x32_bf16 v[48:51], v[232:235], v[96:99], v[48:51]\n"
    "ds_read_b64_tr_b16 v[128:129], v252 offset:33792\n"
    "v_mfma_f32_16x16x32_bf16 v[52:55], v[232:235], v[100:103], v[52:55]\n"
    "ds_read_b64_tr_b16 v[130:131], v252 offset:37888\n"
    "s_waitcnt lgkmcnt(10)\n"
    "v_mfma_f32_16x16x32_bf16 v[60:63], v[236:239], v[100:103], v[60:63]\n"
    "ds_read_b64_tr_b16 v[132:133], v253 offset:33792\n"
    "v_mfma_f32_16x16x32_bf16 v[56:59], v[236:239], v[96:99], v[56:59]\n"
    "ds_read_b64_tr_b16 v[134:135], v253 offset:37888\n"
    "s_waitcnt lgkmcnt(10)\n"
    "v_mfma_f32_16x16x32_bf16 v[0:3], v[240:243], v[104:107], v[0:3]\n"
    "ds_read_b64_tr_b16 v[232:233], v254 offset:33792\n"
    "ds_read_b128 v[112:115], v251 offset:40960\n"
    "v_mfma_f32_16x16x32_bf16 v[4:7], v[240:243], v[108:111], v[4:7]\n"
    "ds_read_b64_tr_b16 v[234:235], v254 offset:37888\n"
    "s_waitcnt lgkmcnt(11)\n"
    "v_mfma_f32_16x16x32_bf16 v[12:15], v[244:247], v[108:111], v[12:15]\n"
    "ds_read_b64_tr_b16 v[236:237], v255 offset:33792\n"
    "ds_read_b128 v[116:119], v251 offset:41984\n"
    "v_mfma_f32_16x16x32_bf16 v[8:11], v[244:247], v[104:107], v[8:11]\n"
    "ds_read_b64_tr_b16 v[238:239], v255 offset:37888\n"
    "s_waitcnt lgkmcnt(12)\n"
    "v_mfma_f32_16x16x32_bf16 v[16:19], v[120:123], v[104:107], v[16:19]\n"
    "ds_read_b64_tr_b16 v[240:241], v252 offset:49152\n"
    "v_mfma_f32_16x16x32_bf16 v[20:23], v[120:123], v[108:111], v[20:23]\n"
    "ds_read_b64_tr_b16 v[242:243], v252 offset:53248\n"
    "s_waitcnt lgkmcnt(12)\n"
    "v_mfma_f32_16x16x32_bf16 v[28:31], v[124:127], v[108:111], v[28:31]\n"
    "ds_read_b64_tr_b16 v[244:245], v253 offset:49152\n"
    "v_mfma_f32_16x16x32_bf16 v[24:27], v[124:127], v[104:107], v[24:27]\n"
    "ds_read_b64_tr_b16 v[246:247], v253 offset:53248\n"
    "s_waitcnt lgkmcnt(12)\n"
    "v_mfma_f32_16x16x32_bf16 v[32:35], v[128:131], v[104:107], v[32:35]\n"
    "ds_read_b64_tr_b16 v[120:121], v254 offset:49152\n"
    "v_mfma_f32_16x16x32_bf16 v[36:39], v[128:131], v[108:111], v[36:39]\n"
    "ds_read_b64_tr_b16 v[122:123], v254 offset:53248\n"
    "s_waitcnt lgkmcnt(12)\n"
    "v_mfma_f32_16x16x32_bf16 v[44:47], v[132:135], v[108:111], v[44:47]\n"
    "ds_read_b64_tr_b16 v[124:125], v255 offset:49152\n"
    "v_add_u32 v251, 0xfffe8000, v251\n"
    "v_mfma_f32_16x16x32_bf16 v[40:43], v[132:135], v[104:107], v[40:43]\n"
    "ds_read_b64_tr_b16 v[126:127], v255 offset:53248\n"
    "s_waitcnt lgkmcnt(11)\n"
    "v_mfma_f32_16x16x32_bf16 v[48:51], v[232:235], v[104:107], v[48:51]\n"
    "s_barrier\n"
    "ds_read_b64_tr_b16 v[128:129], v252 offset:50176\n"
    "v_mfma_f32_16x16x32_bf16 v[52:55], v[232:235], v[108:111], v[52:55]\n"
    "ds_read_b64_tr_b16 v[130:131], v252 offset:54272\n"
    "s_waitcnt lgkmcnt(10)\n"
    "v_mfma_f32_16x16x32_bf16 v[60:63], v[236:239], v[108:111], v[60:63]\n"
    "ds_read_b64_tr_b16 v[132:133], v253 offset:50176\n"
    "v_mfma_f32_16x16x32_bf16 v[56:59], v[236:239], v[104:107], v[56:59]\n"
    "ds_read_b64_tr_b16 v[134:135], v253 offset:54272\n"
    "s_waitcnt lgkmcnt(10)\n"
    "v_mfma_f32_16x16x32_bf16 v[0:3], v[240:243], v[112:115], v[0:3]\n"
    "ds_read_b64_tr_b16 v[232:233], v254 offset:50176\n"
    "ds_read_b128 v[96:99], v251 offset:0\n"
    "v_mfma_f32_16x16x32_bf16 v[4:7], v[240:243], v[116:119], v[4:7]\n"
    "ds_read_b64_tr_b16 v[234:235], v254 offset:54272\n"
    "s_waitcnt lgkmcnt(11)\n"
    "v_mfma_f32_16x16x32_bf16 v[12:15], v[244:247], v[116:119], v[12:15]\n"
    "ds_read_b64_tr_b16 v[236:237], v255 offset:50176\n"
    "ds_read_b128 v[100:103], v251 offset:1024\n"
    "v_mfma_f32_16x16x32_bf16 v[8:11], v[244:247], v[112:115], v[8:11]\n"
    "ds_read_b64_tr_b16 v[238:239], v255 offset:54272\n"
    "s_waitcnt lgkmcnt(12)\n"
    "v_mfma_f32_16x16x32_bf16 v[16:19], v[120:123], v[112:115], v[16:19]\n"
    "ds_read_b64_tr_b16 v[240:241], v248 offset:8192\n"
    "v_mfma_f32_16x16x32_bf16 v[20:23], v[120:123], v[116:119], v[20:23]\n"
    "ds_read_b64_tr_b16 v[242:243], v248 offset:12800\n"
    "s_waitcnt lgkmcnt(12)\n"
    "v_mfma_f32_16x16x32_bf16 v[28:31], v[124:127], v[116:119], v[28:31]\n"
    "ds_read_b64_tr_b16 v[244:245], v248 offset:8224\n"
    "v_mfma_f32_16x16x32_bf16 v[24:27], v[124:127], v[112:115], v[24:27]\n"
    "ds_read_b64_tr_b16 v[246:247], v248 offset:12832\n"
    "s_waitcnt lgkmcnt(12)\n"
    "v_mfma_f32_16x16x32_bf16 v[32:35], v[128:131], v[112:115], v[32:35]\n"
    "ds_read_b64_tr_b16 v[120:121], v248 offset:8256\n"
    "v_mfma_f32_16x16x32_bf16 v[36:39], v[128:131], v[116:119], v[36:39]\n"
    "ds_read_b64_tr_b16 v[122:123], v248 offset:12864\n"
    "s_waitcnt lgkmcnt(12)\n"
    "v_mfma_f32_16x16x32_bf16 v[44:47], v[132:135], v[116:119], v[44:47]\n"
    "ds_read_b64_tr_b16 v[124:125], v248 offset:8288\n"
    "v_mfma_f32_16x16x32_bf16 v[40:43], v[132:135], v[112:115], v[40:43]\n"
    "ds_read_b64_tr_b16 v[126:127], v248 offset:12896\n"
    "s_waitcnt lgkmcnt(11)\n"
    "v_mfma_f32_16x16x32_bf16 v[48:51], v[232:235], v[112:115], v[48:51]\n"
    "ds_read_b64_tr_b16 v[128:129], v248 offset:8320\n"
    "v_mfma_f32_16x16x32_bf16 v[52:55], v[232:235], v[116:119], v[52:55]\n"
    "ds_read_b64_tr_b16 v[130:131], v248 offset:12928\n"
    "s_waitcnt lgkmcnt(10)\n"
    "v_mfma_f32_16x16x32_bf16 v[60:63], v[236:239], v[116:119], v[60:63]\n"
    "ds_read_b64_tr_b16 v[132:133], v248 offset:8352\n"
    "v_mfma_f32_16x16x32_bf16 v[56:59], v[236:239], v[112:115], v[56:59]\n"
    "ds_read_b64_tr_b16 v[134:135], v248 offset:12960\n"
    "s_waitcnt lgkmcnt(10)\n"
    "v_mfma_f32_16x16x32_bf16 v[0:3], v[240:243], v[96:99], v[0:3]\n"
    "ds_read_b64_tr_b16 v[232:233], v248 offset:8384\n"
    "v_mfma_f32_16x16x32_bf16 v[4:7], v[240:243], v[100:103], v[4:7]\n"
    "ds_read_b64_tr_b16 v[234:235], v248 offset:12992\n"
    "s_waitcnt lgkmcnt(10)\n"
    "v_mfma_f32_16x16x32_bf16 v[12:15], v[244:247], v[100:103], v[12:15]\n"
    "ds_read_b64_tr_b16 v[236:237], v248 offset:8416\n"
    "ds_read_b128 v[104:107], v251 offset:17408\n"
    "v_mfma_f32_16x16x32_bf16 v[8:11], v[244:247], v[96:99], v[8:11]\n"
    "ds_read_b64_tr_b16 v[238:239], v248 offset:13024\n"
    "s_waitcnt lgkmcnt(11)\n"
    "v_mfma_f32_16x16x32_bf16 v[16:19], v[120:123], v[96:99], v[16:19]\n"
    "s_barrier\n"
    "ds_read_b64_tr_b16 v[240:241], v248 offset:8448\n"
    "ds_read_b128 v[108:111], v251 offset:18432\n"
    "v_mfma_f32_16x16x32_bf16 v[20:23], v[120:123], v[100:103], v[20:23]\n"
    "ds_read_b64_tr_b16 v[242:243], v248 offset:13056\n"
    "s_waitcnt lgkmcnt(12)\n"
    "v_mfma_f32_16x16x32_bf16 v[28:31], v[124:127], v[100:103], v[28:31]\n"
    "ds_read_b64_tr_b16 v[244:245], v248 offset:25600\n"
    "v_mfma_f32_16x16x32_bf16 v[24:27], v[124:127], v[96:99], v[24:27]\n"
    "ds_read_b64_tr_b16 v[246:247], v248 offset:30208\n"
    "s_waitcnt lgkmcnt(12)\n"
    "v_mfma_f32_16x16x32_bf16 v[32:35], v[128:131], v[96:99], v[32:35]\n"
    "ds_read_b64_tr_b16 v[120:121], v248 offset:25632\n"
    "v_mfma_f32_16x16x32_bf16 v[36:39], v[128:131], v[100:103], v[36:39]\n"
    "ds_read_b64_tr_b16 v[122:123], v248 offset:30240\n"
    "s_waitcnt lgkmcnt(12)\n"
    "v_mfma_f32_16x16x32_bf16 v[44:47], v[132:135], v[100:103], v[44:47]\n"
    "ds_read_b64_tr_b16 v[124:125], v248 offset:25664\n"
    "v_mfma_f32_16x16x32_bf16 v[40:43], v[132:135], v[96:99], v[40:43]\n"
    "ds_read_b64_tr_b16 v[126:127], v248 offset:30272\n"
    "s_waitcnt lgkmcnt(12)\n"
    "v_mfma_f32_16x16x32_bf16 v[48:51], v[232:235], v[96:99], v[48:51]\n"
    "ds_read_b64_tr_b16 v[128:129], v248 offset:25696\n"
    "v_mfma_f32_16x16x32_bf16 v[52:55], v[232:235], v[100:103], v[52:55]\n"
    "ds_read_b64_tr_b16 v[130:131], v248 offset:30304\n"
    "s_waitcnt lgkmcnt(11)\n"
    "v_mfma_f32_16x16x32_bf16 v[60:63], v[236:239], v[100:103], v[60:63]\n"
    "ds_read_b64_tr_b16 v[132:133], v248 offset:25728\n"
    "v_mfma_f32_16x16x32_bf16 v[56:59], v[236:239], v[96:99], v[56:59]\n"
    "ds_read_b64_tr_b16 v[134:135], v248 offset:30336\n"
    "s_waitcnt lgkmcnt(10)\n"
    "v_mfma_f32_16x16x32_bf16 v[64:67], v[240:243], v[96:99], v[64:67]\n"
    "ds_read_b64_tr_b16 v[232:233], v248 offset:25760\n"
    "v_mfma_f32_16x16x32_bf16 v[68:71], v[240:243], v[100:103], v[68:71]\n"
    "ds_read_b64_tr_b16 v[234:235], v248 offset:30368\n"
    "s_waitcnt lgkmcnt(10)\n"
    "v_mfma_f32_16x16x32_bf16 v[0:3], v[244:247], v[104:107], v[0:3]\n"
    "ds_read_b64_tr_b16 v[236:237], v248 offset:25792\n"
    "v_mfma_f32_16x16x32_bf16 v[4:7], v[244:247], v[108:111], v[4:7]\n"
    "ds_read_b64_tr_b16 v[238:239], v248 offset:30400\n"
    "s_waitcnt lgkmcnt(10)\n"
    "v_mfma_f32_16x16x32_bf16 v[12:15], v[120:123], v[108:111], v[12:15]\n"
    "ds_read_b64_tr_b16 v[240:241], v248 offset:25824\n"
    "ds_read_b128 v[112:115], v251 offset:34816\n"
    "v_mfma_f32_16x16x32_bf16 v[8:11], v[120:123], v[104:107], v[8:11]\n"
    "ds_read_b64_tr_b16 v[242:243], v248 offset:30432\n"
    "s_waitcnt lgkmcnt(11)\n"
    "v_mfma_f32_16x16x32_bf16 v[16:19], v[124:127], v[104:107], v[16:19]\n"
    "ds_read_b64_tr_b16 v[244:245], v248 offset:25856\n"
    "ds_read_b128 v[116:119], v251 offset:35840\n"
    "v_mfma_f32_16x16x32_bf16 v[20:23], v[124:127], v[108:111], v[20:23]\n"
    "ds_read_b64_tr_b16 v[246:247], v248 offset:30464\n"
    "s_waitcnt lgkmcnt(12)\n"
    "v_mfma_f32_16x16x32_bf16 v[28:31], v[128:131], v[108:111], v[28:31]\n"
    "ds_read_b64_tr_b16 v[120:121], v248 offset:43008\n"
    "v_mfma_f32_16x16x32_bf16 v[24:27], v[128:131], v[104:107], v[24:27]\n"
    "ds_read_b64_tr_b16 v[122:123], v248 offset:47616\n"
    "s_waitcnt lgkmcnt(12)\n"
    "v_mfma_f32_16x16x32_bf16 v[32:35], v[132:135], v[104:107], v[32:35]\n"
    "ds_read_b64_tr_b16 v[124:125], v248 offset:43040\n"
    "v_mfma_f32_16x16x32_bf16 v[36:39], v[132:135], v[108:111], v[36:39]\n"
    "ds_read_b64_tr_b16 v[126:127], v248 offset:47648\n"
    "s_waitcnt lgkmcnt(12)\n"
    "v_mfma_f32_16x16x32_bf16 v[44:47], v[232:235], v[108:111], v[44:47]\n"
    "s_barrier\n"
    "ds_read_b64_tr_b16 v[128:129], v248 offset:43072\n"
    "v_mfma_f32_16x16x32_bf16 v[40:43], v[232:235], v[104:107], v[40:43]\n"
    "ds_read_b64_tr_b16 v[130:131], v248 offset:47680\n"
    "s_waitcnt lgkmcnt(12)\n"
    "v_mfma_f32_16x16x32_bf16 v[48:51], v[236:239], v[104:107], v[48:51]\n"
    "ds_read_b64_tr_b16 v[132:133], v248 offset:43104\n"
    "v_mfma_f32_16x16x32_bf16 v[52:55], v[236:239], v[108:111], v[52:55]\n"
    "ds_read_b64_tr_b16 v[134:135], v248 offset:47712\n"
    "s_waitcnt lgkmcnt(11)\n"
    "v_mfma_f32_16x16x32_bf16 v[60:63], v[240:243], v[108:111], v[60:63]\n"
    "ds_read_b64_tr_b16 v[232:233], v248 offset:43136\n"
    "v_mfma_f32_16x16x32_bf16 v[56:59], v[240:243], v[104:107], v[56:59]\n"
    "ds_read_b64_tr_b16 v[234:235], v248 offset:47744\n"
    "s_waitcnt lgkmcnt(10)\n"
    "v_mfma_f32_16x16x32_bf16 v[64:67], v[244:247], v[104:107], v[64:67]\n"
    "ds_read_b64_tr_b16 v[236:237], v248 offset:43168\n"
    "v_mfma_f32_16x16x32_bf16 v[68:71], v[244:247], v[108:111], v[68:71]\n"
    "ds_read_b64_tr_b16 v[238:239], v248 offset:47776\n"
    "s_waitcnt lgkmcnt(10)\n"
    "v_mfma_f32_16x16x32_bf16 v[0:3], v[120:123], v[112:115], v[0:3]\n"
    "ds_read_b64_tr_b16 v[240:241], v248 offset:43200\n"
    "v_mfma_f32_16x16x32_bf16 v[4:7], v[120:123], v[116:119], v[4:7]\n"
    "ds_read_b64_tr_b16 v[242:243], v248 offset:47808\n"
    "s_waitcnt lgkmcnt(10)\n"
    "v_mfma_f32_16x16x32_bf16 v[12:15], v[124:127], v[116:119], v[12:15]\n"
    "ds_read_b64_tr_b16 v[244:245], v248 offset:43232\n"
    "ds_read_b128 v[96:99], v251 offset:52224\n"
    "v_mfma_f32_16x16x32_bf16 v[8:11], v[124:127], v[112:115], v[8:11]\n"
    "ds_read_b64_tr_b16 v[246:247], v248 offset:47840\n"
    "s_waitcnt lgkmcnt(11)\n"
    "v_mfma_f32_16x16x32_bf16 v[16:19], v[128:131], v[112:115], v[16:19]\n"
    "ds_read_b64_tr_b16 v[120:121], v248 offset:43264\n"
    "ds_read_b128 v[100:103], v251 offset:53248\n"
    "v_mfma_f32_16x16x32_bf16 v[20:23], v[128:131], v[116:119], v[20:23]\n"
    "ds_read_b64_tr_b16 v[122:123], v248 offset:47872\n"
    "s_waitcnt lgkmcnt(12)\n"
    "v_mfma_f32_16x16x32_bf16 v[28:31], v[132:135], v[116:119], v[28:31]\n"
    "ds_read_b64_tr_b16 v[124:125], v248 offset:60416\n"
    "v_add_u32 v248, 0xec00, v248\n"
    "v_mfma_f32_16x16x32_bf16 v[24:27], v[132:135], v[112:115], v[24:27]\n"
    "ds_read_b64_tr_b16 v[126:127], v248 offset:4608\n"
    "s_waitcnt lgkmcnt(12)\n"
    "v_mfma_f32_16x16x32_bf16 v[32:35], v[232:235], v[112:115], v[32:35]\n"
    "ds_read_b64_tr_b16 v[128:129], v248 offset:32\n"
    "v_mfma_f32_16x16x32_bf16 v[36:39], v[232:235], v[116:119], v[36:39]\n"
    "ds_read_b64_tr_b16 v[130:131], v248 offset:4640\n"
    "s_waitcnt lgkmcnt(12)\n"
    "v_mfma_f32_16x16x32_bf16 v[44:47], v[236:239], v[116:119], v[44:47]\n"
    "ds_read_b64_tr_b16 v[132:133], v248 offset:64\n"
    "v_mfma_f32_16x16x32_bf16 v[40:43], v[236:239], v[112:115], v[40:43]\n"
    "ds_read_b64_tr_b16 v[134:135], v248 offset:4672\n"
    "s_waitcnt lgkmcnt(12)\n"
    "v_mfma_f32_16x16x32_bf16 v[48:51], v[240:243], v[112:115], v[48:51]\n"
    "ds_read_b64_tr_b16 v[232:233], v248 offset:96\n"
    "v_mfma_f32_16x16x32_bf16 v[52:55], v[240:243], v[116:119], v[52:55]\n"
    "ds_read_b64_tr_b16 v[234:235], v248 offset:4704\n"
    "s_waitcnt lgkmcnt(11)\n"
    "v_mfma_f32_16x16x32_bf16 v[60:63], v[244:247], v[116:119], v[60:63]\n"
    "ds_read_b64_tr_b16 v[236:237], v248 offset:128\n"
    "v_add_u32 v249, 0x11000, v249\n"
    "v_mfma_f32_16x16x32_bf16 v[56:59], v[244:247], v[112:115], v[56:59]\n"
    "ds_read_b64_tr_b16 v[238:239], v248 offset:4736\n"
    "s_waitcnt lgkmcnt(10)\n"
    "v_mfma_f32_16x16x32_bf16 v[64:67], v[120:123], v[112:115], v[64:67]\n"
    "s_barrier\n"
    "ds_read_b64_tr_b16 v[240:241], v248 offset:160\n"
    "v_mfma_f32_16x16x32_bf16 v[68:71], v[120:123], v[116:119], v[68:71]\n"
    "ds_read_b64_tr_b16 v[242:243], v248 offset:4768\n"
    "s_waitcnt lgkmcnt(10)\n"
    "v_mfma_f32_16x16x32_bf16 v[0:3], v[124:127], v[96:99], v[0:3]\n"
    "ds_read_b64_tr_b16 v[244:245], v248 offset:192\n"
    "v_mfma_f32_16x16x32_bf16 v[4:7], v[124:127], v[100:103], v[4:7]\n"
    "ds_read_b64_tr_b16 v[246:247], v248 offset:4800\n"
    "s_waitcnt lgkmcnt(10)\n"
    "v_mfma_f32_16x16x32_bf16 v[12:15], v[128:131], v[100:103], v[12:15]\n"
    "ds_read_b64_tr_b16 v[120:121], v248 offset:224\n"
    "ds_read_b64 v[104:105], v249 offset:0\n"
    "v_mfma_f32_16x16x32_bf16 v[8:11], v[128:131], v[96:99], v[8:11]\n"
    "ds_read_b64_tr_b16 v[122:123], v248 offset:4832\n"
    "s_waitcnt lgkmcnt(11)\n"
    "v_mfma_f32_16x16x32_bf16 v[16:19], v[132:135], v[96:99], v[16:19]\n"
    "ds_read_b64_tr_b16 v[124:125], v248 offset:256\n"
    "ds_read_b64 v[108:109], v249 offset:512\n"
    "v_mfma_f32_16x16x32_bf16 v[20:23], v[132:135], v[100:103], v[20:23]\n"
    "ds_read_b64_tr_b16 v[126:127], v248 offset:4864\n"
    "s_waitcnt lgkmcnt(12)\n"
    "v_mfma_f32_16x16x32_bf16 v[28:31], v[232:235], v[100:103], v[28:31]\n"
    "ds_read_b64_tr_b16 v[128:129], v248 offset:13568\n"
    "v_mov_b32 v106, 0\n"
    "v_mfma_f32_16x16x32_bf16 v[24:27], v[232:235], v[96:99], v[24:27]\n"
    "ds_read_b64_tr_b16 v[130:131], v248 offset:13568\n"
    "s_waitcnt lgkmcnt(12)\n"
    "v_mfma_f32_16x16x32_bf16 v[32:35], v[236:239], v[96:99], v[32:35]\n"
    "ds_read_b64_tr_b16 v[132:133], v248 offset:13312\n"
    "v_mov_b32 v107, 0\n"
    "v_mfma_f32_16x16x32_bf16 v[36:39], v[236:239], v[100:103], v[36:39]\n"
    "ds_read_b64_tr_b16 v[134:135], v248 offset:13312\n"
    "s_waitcnt lgkmcnt(12)\n"
    "v_mfma_f32_16x16x32_bf16 v[44:47], v[240:243], v[100:103], v[44:47]\n"
    "ds_read_b64_tr_b16 v[232:233], v248 offset:13344\n"
    "v_mov_b32 v110, 0\n"
    "v_mfma_f32_16x16x32_bf16 v[40:43], v[240:243], v[96:99], v[40:43]\n"
    "ds_read_b64_tr_b16 v[234:235], v248 offset:13344\n"
    "s_waitcnt lgkmcnt(12)\n"
    "v_mfma_f32_16x16x32_bf16 v[48:51], v[244:247], v[96:99], v[48:51]\n"
    "ds_read_b64_tr_b16 v[236:237], v248 offset:13376\n"
    "v_mov_b32 v111, 0\n"
    "v_mfma_f32_16x16x32_bf16 v[52:55], v[244:247], v[100:103], v[52:55]\n"
    "ds_read_b64_tr_b16 v[238:239], v248 offset:13376\n"
    "s_waitcnt lgkmcnt(11)\n"
    "v_mfma_f32_16x16x32_bf16 v[60:63], v[120:123], v[100:103], v[60:63]\n"
    "ds_read_b64_tr_b16 v[240:241], v248 offset:13408\n"
    "v_mfma_f32_16x16x32_bf16 v[56:59], v[120:123], v[96:99], v[56:59]\n"
    "ds_read_b64_tr_b16 v[242:243], v248 offset:13408\n"
    "s_waitcnt lgkmcnt(10)\n"
    "v_mfma_f32_16x16x32_bf16 v[64:67], v[124:127], v[96:99], v[64:67]\n"
    "ds_read_b64_tr_b16 v[244:245], v248 offset:13440\n"
    "v_mfma_f32_16x16x32_bf16 v[68:71], v[124:127], v[100:103], v[68:71]\n"
    "ds_read_b64_tr_b16 v[246:247], v248 offset:13440\n"
    "s_waitcnt lgkmcnt(10)\n"
    "v_mfma_f32_16x16x32_bf16 v[64:67], v[128:131], v[104:107], v[64:67]\n"
    "ds_read_b64_tr_b16 v[120:121], v248 offset:13472\n"
    "v_add_u32 v252, 0xffffb400, v252\n"
    "v_mfma_f32_16x16x32_bf16 v[68:71], v[128:131], v[108:111], v[68:71]\n"
    "ds_read_b64_tr_b16 v[122:123], v248 offset:13472\n"
    "s_waitcnt lgkmcnt(10)\n"
    "v_mfma_f32_16x16x32_bf16 v[4:7], v[132:135], v[108:111], v[4:7]\n"
    "ds_read_b64_tr_b16 v[124:125], v248 offset:13504\n"
    "v_mfma_f32_16x16x32_bf16 v[0:3], v[132:135], v[104:107], v[0:3]\n"
    "ds_read_b64_tr_b16 v[126:127], v248 offset:13504\n"
    "s_waitcnt lgkmcnt(10)\n"
    "v_mfma_f32_16x16x32_bf16 v[8:11], v[232:235], v[104:107], v[8:11]\n"
    "s_barrier\n"
    "ds_read_b64_tr_b16 v[128:129], v248 offset:13536\n"
    "v_mfma_f32_16x16x32_bf16 v[12:15], v[232:235], v[108:111], v[12:15]\n"
    "ds_read_b64_tr_b16 v[130:131], v248 offset:13536\n"
    "s_waitcnt lgkmcnt(10)\n"
    "v_mfma_f32_16x16x32_bf16 v[20:23], v[236:239], v[108:111], v[20:23]\n"
    "ds_read_b64_tr_b16 v[132:133], v252 offset:0\n"
    "v_cvt_pk_bf16_f32 v64, v64, v65\n"
    "v_mfma_f32_16x16x32_bf16 v[16:19], v[236:239], v[104:107], v[16:19]\n"
    "ds_read_b64_tr_b16 v[134:135], v252 offset:4096\n"
    "s_waitcnt lgkmcnt(10)\n"
    "v_mfma_f32_16x16x32_bf16 v[24:27], v[240:243], v[104:107], v[24:27]\n"
    "ds_read_b64_tr_b16 v[232:233], v252 offset:1024\n"
    "v_cvt_pk_bf16_f32 v65, v66, v67\n"
    "v_mfma_f32_16x16x32_bf16 v[28:31], v[240:243], v[108:111], v[28:31]\n"
    "ds_read_b64_tr_b16 v[234:235], v252 offset:5120\n"
    "s_waitcnt lgkmcnt(10)\n"
    "v_mfma_f32_16x16x32_bf16 v[36:39], v[244:247], v[108:111], v[36:39]\n"
    "ds_read_b64_tr_b16 v[236:237], v252 offset:8192\n"
    "v_cvt_pk_bf16_f32 v66, v68, v69\n"
    "v_mfma_f32_16x16x32_bf16 v[32:35], v[244:247], v[104:107], v[32:35]\n"
    "ds_read_b64_tr_b16 v[238:239], v252 offset:12288\n"
    "s_waitcnt lgkmcnt(10)\n"
    "v_mfma_f32_16x16x32_bf16 v[40:43], v[120:123], v[104:107], v[40:43]\n"
    "ds_read_b64_tr_b16 v[240:241], v252 offset:9216\n"
    "v_add_u32 v253, 0xffffb400, v253\n"
    "v_mfma_f32_16x16x32_bf16 v[44:47], v[120:123], v[108:111], v[44:47]\n"
    "ds_read_b64_tr_b16 v[242:243], v252 offset:13312\n"
    "s_waitcnt lgkmcnt(10)\n"
    "v_mfma_f32_16x16x32_bf16 v[52:55], v[124:127], v[108:111], v[52:55]\n"
    "ds_read_b64_tr_b16 v[244:245], v252 offset:16384\n"
    "v_cvt_pk_bf16_f32 v67, v70, v71\n"
    "v_mfma_f32_16x16x32_bf16 v[48:51], v[124:127], v[104:107], v[48:51]\n"
    "ds_read_b64_tr_b16 v[246:247], v252 offset:20480\n"
    "s_waitcnt lgkmcnt(10)\n"
    "v_mfma_f32_16x16x32_bf16 v[56:59], v[128:131], v[104:107], v[56:59]\n"
    "ds_read_b64_tr_b16 v[120:121], v252 offset:17408\n"
    "buffer_store_dwordx4 v[64:67], v250, s[60:63], s94 offen sc1\n"
    "v_mfma_f32_16x16x32_bf16 v[60:63], v[128:131], v[108:111], v[60:63]\n"
    "ds_read_b64_tr_b16 v[122:123], v252 offset:21504\n"
    "s_waitcnt lgkmcnt(10)\n"
    "v_mfma_f32_16x16x32_bf16 v[0:3], v[132:135], v[192:195], v[0:3]\n"
    "ds_read_b64_tr_b16 v[124:125], v253 offset:0\n"
    "v_mfma_f32_16x16x32_bf16 v[4:7], v[132:135], v[196:199], v[4:7]\n"
    "ds_read_b64_tr_b16 v[126:127], v253 offset:4096\n"
    "s_waitcnt lgkmcnt(10)\n"
    "v_mfma_f32_16x16x32_bf16 v[0:3], v[232:235], v[200:203], v[0:3]\n"
    "ds_read_b64_tr_b16 v[128:129], v253 offset:1024\n"
    "v_mfma_f32_16x16x32_bf16 v[4:7], v[232:235], v[204:207], v[4:7]\n"
    "ds_read_b64_tr_b16 v[130:131], v253 offset:5120\n"
    "s_waitcnt lgkmcnt(10)\n"
    "v_mfma_f32_16x16x32_bf16 v[0:3], v[236:239], v[208:211], v[0:3]\n"
    "ds_read_b64_tr_b16 v[132:133], v253 offset:8192\n"
    "v_mfma_f32_16x16x32_bf16 v[4:7], v[236:239], v[212:215], v[4:7]\n"
    "ds_read_b64_tr_b16 v[134:135], v253 offset:12288\n"
    "s_waitcnt lgkmcnt(10)\n"
    "v_mfma_f32_16x16x32_bf16 v[0:3], v[240:243], v[216:219], v[0:3]\n"
    "ds_read_b64_tr_b16 v[232:233], v253 offset:9216\n"
    "v_add_u32 v254, 0xffffb400, v254\n"
    "v_mfma_f32_16x16x32_bf16 v[4:7], v[240:243], v[220:223], v[4:7]\n"
    "ds_read_b64_tr_b16 v[234:235], v253 offset:13312\n"
    "s_waitcnt lgkmcnt(10)\n"
    "v_mfma_f32_16x16x32_bf16 v[0:3], v[244:247], v[224:227], v[0:3]\n"
    "ds_read_b64_tr_b16 v[236:237], v253 offset:16384\n"
    "v_mfma_f32_16x16x32_bf16 v[4:7], v[244:247], v[228:231], v[4:7]\n"
    "ds_read_b64_tr_b16 v[238:239], v253 offset:20480\n"
    "s_waitcnt lgkmcnt(10)\n"
    "v_mfma_f32_16x16x32_bf16 v[0:3], v[120:123], v[144:147], v[0:3]\n"
    "s_barrier\n"
    "L_st9_%=:\n"
    "s_cbranch_execz L_bk%=\n"
    "ds_read_b64_tr_b16 v[240:241], v253 offset:17408\n"
    "v_mfma_f32_16x16x32_bf16 v[4:7], v[120:123], v[148:151], v[4:7]\n"
    "ds_read_b64_tr_b16 v[242:243], v253 offset:21504\n"
    "s_waitcnt lgkmcnt(10)\n"
    "v_mfma_f32_16x16x32_bf16 v[8:11], v[124:127], v[192:195], v[8:11]\n"
    "ds_read_b64_tr_b16 v[244:245], v254 offset:0\n"
    "v_mfma_f32_16x16x32_bf16 v[12:15], v[124:127], v[196:199], v[12:15]\n"
    "ds_read_b64_tr_b16 v[246:247], v254 offset:4096\n"
    "s_waitcnt lgkmcnt(10)\n"
    "v_mfma_f32_16x16x32_bf16 v[8:11], v[128:131], v[200:203], v[8:11]\n"
    "ds_read_b64_tr_b16 v[120:121], v254 offset:1024\n"
    "v_cvt_pk_bf16_f32 v0, v0, v1\n"
    "v_mfma_f32_16x16x32_bf16 v[12:15], v[128:131], v[204:207], v[12:15]\n"
    "ds_read_b64_tr_b16 v[122:123], v254 offset:5120\n"
    "s_waitcnt lgkmcnt(10)\n"
    "v_mfma_f32_16x16x32_bf16 v[8:11], v[132:135], v[208:211], v[8:11]\n"
    "ds_read_b64_tr_b16 v[124:125], v254 offset:8192\n"
    "v_cvt_pk_bf16_f32 v1, v2, v3\n"
    "v_mfma_f32_16x16x32_bf16 v[12:15], v[132:135], v[212:215], v[12:15]\n"
    "ds_read_b64_tr_b16 v[126:127], v254 offset:12288\n"
    "s_waitcnt lgkmcnt(10)\n"
    "v_mfma_f32_16x16x32_bf16 v[8:11], v[232:235], v[216:219], v[8:11]\n"
    "ds_read_b64_tr_b16 v[128:129], v254 offset:9216\n"
    "v_add_u32 v255, 0xffffb400, v255\n"
    "v_mfma_f32_16x16x32_bf16 v[12:15], v[232:235], v[220:223], v[12:15]\n"
    "ds_read_b64_tr_b16 v[130:131], v254 offset:13312\n"
    "s_waitcnt lgkmcnt(10)\n"
    "v_mfma_f32_16x16x32_bf16 v[8:11], v[236:239], v[224:227], v[8:11]\n"
    "ds_read_b64_tr_b16 v[132:133], v254 offset:16384\n"
    "v_cvt_pk_bf16_f32 v2, v4, v5\n"
    "v_mfma_f32_16x16x32_bf16 v[12:15], v[236:239], v[228:231], v[12:15]\n"
    "ds_read_b64_tr_b16 v[134:135], v254 offset:20480\n"
    "s_waitcnt lgkmcnt(10)\n"
    "v_mfma_f32_16x16x32_bf16 v[8:11], v[240:243], v[144:147], v[8:11]\n"
    "ds_read_b64_tr_b16 v[232:233], v254 offset:17408\n"
    "v_cvt_pk_bf16_f32 v3, v6, v7\n"
    "v_mfma_f32_16x16x32_bf16 v[12:15], v[240:243], v[148:151], v[12:15]\n"
    "ds_read_b64_tr_b16 v[234:235], v254 offset:21504\n"
    "s_waitcnt lgkmcnt(10)\n"
    "v_mfma_f32_16x16x32_bf16 v[16:19], v[244:247], v[192:195], v[16:19]\n"
    "ds_read_b64_tr_b16 v[236:237], v255 offset:0\n"
    "buffer_store_dwordx4 v[0:3], v250, s[60:63], s73 offen sc1\n"
    "v_mfma_f32_16x16x32_bf16 v[20:23], v[244:247], v[196:199], v[20:23]\n"
    "ds_read_b64_tr_b16 v[238:239], v255 offset:4096\n"
    "s_waitcnt lgkmcnt(10)\n"
    "v_mfma_f32_16x16x32_bf16 v[16:19], v[120:123], v[200:203], v[16:19]\n"
    "ds_read_b64_tr_b16 v[240:241], v255 offset:1024\n"
    "v_cvt_pk_bf16_f32 v8, v8, v9\n"
    "v_mfma_f32_16x16x32_bf16 v[20:23], v[120:123], v[204:207], v[20:23]\n"
    "ds_read_b64_tr_b16 v[242:243], v255 offset:5120\n"
    "s_waitcnt lgkmcnt(10)\n"
    "v_mfma_f32_16x16x32_bf16 v[16:19], v[124:127], v[208:211], v[16:19]\n"
    "ds_read_b64_tr_b16 v[244:245], v255 offset:8192\n"
    "v_cvt_pk_bf16_f32 v9, v10, v11\n"
    "v_mfma_f32_16x16x32_bf16 v[20:23], v[124:127], v[212:215], v[20:23]\n"
    "ds_read_b64_tr_b16 v[246:247], v255 offset:12288\n"
    "s_waitcnt lgkmcnt(10)\n"
    "v_mfma_f32_16x16x32_bf16 v[16:19], v[128:131], v[216:219], v[16:19]\n"
    "ds_read_b64_tr_b16 v[120:121], v255 offset:9216\n"
    "v_cvt_pk_bf16_f32 v10, v12, v13\n"
    "v_mfma_f32_16x16x32_bf16 v[20:23], v[128:131], v[220:223], v[20:23]\n"
    "ds_read_b64_tr_b16 v[122:123], v255 offset:13312\n"
    "s_waitcnt lgkmcnt(10)\n"
    "v_mfma_f32_16x16x32_bf16 v[16:19], v[132:135], v[224:227], v[16:19]\n"
    "ds_read_b64_tr_b16 v[124:125], v255 offset:16384\n"
    "v_cvt_pk_bf16_f32 v11, v14, v15\n"
    "v_mfma_f32_16x16x32_bf16 v[20:23], v[132:135], v[228:231], v[20:23]\n"
    "ds_read_b64_tr_b16 v[126:127], v255 offset:20480\n"
    "s_waitcnt lgkmcnt(10)\n"
    "v_mfma_f32_16x16x32_bf16 v[16:19], v[232:235], v[144:147], v[16:19]\n"
    "s_barrier\n"
    "ds_read_b64_tr_b16 v[128:129], v255 offset:17408\n"
    "v_mfma_f32_16x16x32_bf16 v[20:23], v[232:235], v[148:151], v[20:23]\n"
    "ds_read_b64_tr_b16 v[130:131], v255 offset:21504\n"
    "s_waitcnt lgkmcnt(10)\n"
    "v_mfma_f32_16x16x32_bf16 v[24:27], v[236:239], v[192:195], v[24:27]\n"
    "ds_read_b64_tr_b16 v[132:133], v252 offset:24576\n"
    "buffer_store_dwordx4 v[8:11], v250, s[60:63], s85 offen sc1\n"
    "v_mfma_f32_16x16x32_bf16 v[28:31], v[236:239], v[196:199], v[28:31]\n"
    "ds_read_b64_tr_b16 v[134:135], v252 offset:28672\n"
    "s_waitcnt lgkmcnt(10)\n"
    "v_mfma_f32_16x16x32_bf16 v[24:27], v[240:243], v[200:203], v[24:27]\n"
    "ds_read_b64_tr_b16 v[232:233], v252 offset:25600\n"
    "v_cvt_pk_bf16_f32 v16, v16, v17\n"
    "v_mfma_f32_16x16x32_bf16 v[28:31], v[240:243], v[204:207], v[28:31]\n"
    "ds_read_b64_tr_b16 v[234:235], v252 offset:29696\n"
    "s_waitcnt lgkmcnt(10)\n"
    "v_mfma_f32_16x16x32_bf16 v[24:27], v[244:247], v[208:211], v[24:27]\n"
    "ds_read_b64_tr_b16 v[236:237], v252 offset:32768\n"
    "v_cvt_pk_bf16_f32 v17, v18, v19\n"
    "v_mfma_f32_16x16x32_bf16 v[28:31], v[244:247], v[212:215], v[28:31]\n"
    "ds_read_b64_tr_b16 v[238:239], v252 offset:36864\n"
    "s_waitcnt lgkmcnt(10)\n"
    "v_mfma_f32_16x16x32_bf16 v[24:27], v[120:123], v[216:219], v[24:27]\n"
    "ds_read_b64_tr_b16 v[240:241], v252 offset:33792\n"
    "v_cvt_pk_bf16_f32 v18, v20, v21\n"
    "v_mfma_f32_16x16x32_bf16 v[28:31], v[120:123], v[220:223], v[28:31]\n"
    "ds_read_b64_tr_b16 v[242:243], v252 offset:37888\n"
    "s_waitcnt lgkmcnt(10)\n"
    "v_mfma_f32_16x16x32_bf16 v[24:27], v[124:127], v[224:227], v[24:27]\n"
    "ds_read_b64_tr_b16 v[244:245], v252 offset:40960\n"
    "v_cvt_pk_bf16_f32 v19, v22, v23\n"
    "v_mfma_f32_16x16x32_bf16 v[28:31], v[124:127], v[228:231], v[28:31]\n"
    "ds_read_b64_tr_b16 v[246:247], v252 offset:45056\n"
    "s_waitcnt lgkmcnt(10)\n"
    "v_mfma_f32_16x16x32_bf16 v[24:27], v[128:131], v[144:147], v[24:27]\n"
    "ds_read_b64_tr_b16 v[120:121], v252 offset:41984\n"
    "buffer_store_dwordx4 v[16:19], v250, s[60:63], s86 offen sc1\n"
    "v_mfma_f32_16x16x32_bf16 v[28:31], v[128:131], v[148:151], v[28:31]\n"
    "ds_read_b64_tr_b16 v[122:123], v252 offset:46080\n"
    "s_waitcnt lgkmcnt(10)\n"
    "v_mfma_f32_16x16x32_bf16 v[32:35], v[132:135], v[192:195], v[32:35]\n"
    "ds_read_b64_tr_b16 v[124:125], v253 offset:24576\n"
    "v_mfma_f32_16x16x32_bf16 v[36:39], v[132:135], v[196:199], v[36:39]\n"
    "ds_read_b64_tr_b16 v[126:127], v253 offset:28672\n"
    "s_waitcnt lgkmcnt(10)\n"
    "v_mfma_f32_16x16x32_bf16 v[32:35], v[232:235], v[200:203], v[32:35]\n"
    "ds_read_b64_tr_b16 v[128:129], v253 offset:25600\n"
    "v_cvt_pk_bf16_f32 v24, v24, v25\n"
    "v_mfma_f32_16x16x32_bf16 v[36:39], v[232:235], v[204:207], v[36:39]\n"
    "ds_read_b64_tr_b16 v[130:131], v253 offset:29696\n"
    "s_waitcnt lgkmcnt(10)\n"
    "v_mfma_f32_16x16x32_bf16 v[32:35], v[236:239], v[208:211], v[32:35]\n"
    "ds_read_b64_tr_b16 v[132:133], v253 offset:32768\n"
    "v_cvt_pk_bf16_f32 v25, v26, v27\n"
    "v_mfma_f32_16x16x32_bf16 v[36:39], v[236:239], v[212:215], v[36:39]\n"
    "ds_read_b64_tr_b16 v[134:135], v253 offset:36864\n"
    "s_waitcnt lgkmcnt(10)\n"
    "v_mfma_f32_16x16x32_bf16 v[32:35], v[240:243], v[216:219], v[32:35]\n"
    "ds_read_b64_tr_b16 v[232:233], v253 offset:33792\n"
    "v_cvt_pk_bf16_f32 v26, v28, v29\n"
    "v_mfma_f32_16x16x32_bf16 v[36:39], v[240:243], v[220:223], v[36:39]\n"
    "ds_read_b64_tr_b16 v[234:235], v253 offset:37888\n"
    "s_waitcnt lgkmcnt(10)\n"
    "v_mfma_f32_16x16x32_bf16 v[32:35], v[244:247], v[224:227], v[32:35]\n"
    "ds_read_b64_tr_b16 v[236:237], v253 offset:40960\n"
    "v_cvt_pk_bf16_f32 v27, v30, v31\n"
    "v_mfma_f32_16x16x32_bf16 v[36:39], v[244:247], v[228:231], v[36:39]\n"
    "ds_read_b64_tr_b16 v[238:239], v253 offset:45056\n"
    "s_waitcnt lgkmcnt(10)\n"
    "v_mfma_f32_16x16x32_bf16 v[32:35], v[120:123], v[144:147], v[32:35]\n"
    "ds_read_b64_tr_b16 v[240:241], v253 offset:41984\n"
    "buffer_store_dwordx4 v[24:27], v250, s[60:63], s87 offen sc1\n"
    "v_mfma_f32_16x16x32_bf16 v[36:39], v[120:123], v[148:151], v[36:39]\n"
    "ds_read_b64_tr_b16 v[242:243], v253 offset:46080\n"
    "s_waitcnt lgkmcnt(10)\n"
    "v_mfma_f32_16x16x32_bf16 v[40:43], v[124:127], v[192:195], v[40:43]\n"
    "ds_read_b64_tr_b16 v[244:245], v254 offset:24576\n"
    "v_mfma_f32_16x16x32_bf16 v[44:47], v[124:127], v[196:199], v[44:47]\n"
    "ds_read_b64_tr_b16 v[246:247], v254 offset:28672\n"
    "s_waitcnt lgkmcnt(10)\n"
    "v_mfma_f32_16x16x32_bf16 v[40:43], v[128:131], v[200:203], v[40:43]\n"
    "ds_read_b64_tr_b16 v[120:121], v254 offset:25600\n"
    "v_cvt_pk_bf16_f32 v32, v32, v33\n"
    "v_mfma_f32_16x16x32_bf16 v[44:47], v[128:131], v[204:207], v[44:47]\n"
    "ds_read_b64_tr_b16 v[122:123], v254 offset:29696\n"
    "s_waitcnt lgkmcnt(10)\n"
    "v_mfma_f32_16x16x32_bf16 v[40:43], v[132:135], v[208:211], v[40:43]\n"
    "ds_read_b64_tr_b16 v[124:125], v254 offset:32768\n"
    "v_cvt_pk_bf16_f32 v33, v34, v35\n"
    "v_mfma_f32_16x16x32_bf16 v[44:47], v[132:135], v[212:215], v[44:47]\n"
    "ds_read_b64_tr_b16 v[126:127], v254 offset:36864\n"
    "s_waitcnt lgkmcnt(10)\n"
    "v_mfma_f32_16x16x32_bf16 v[40:43], v[232:235], v[216:219], v[40:43]\n"
    "ds_read_b64_tr_b16 v[128:129], v254 offset:33792\n"
    "v_cvt_pk_bf16_f32 v34, v36, v37\n"
    "v_mfma_f32_16x16x32_bf16 v[44:47], v[232:235], v[220:223], v[44:47]\n"
    "ds_read_b64_tr_b16 v[130:131], v254 offset:37888\n"
    "s_waitcnt lgkmcnt(10)\n"
    "v_mfma_f32_16x16x32_bf16 v[40:43], v[236:239], v[224:227], v[40:43]\n"
    "ds_read_b64_tr_b16 v[132:133], v254 offset:40960\n"
    "v_cvt_pk_bf16_f32 v35, v38, v39\n"
    "v_mfma_f32_16x16x32_bf16 v[44:47], v[236:239], v[228:231], v[44:47]\n"
    "ds_read_b64_tr_b16 v[134:135], v254 offset:45056\n"
    "s_waitcnt lgkmcnt(10)\n"
    "v_mfma_f32_16x16x32_bf16 v[40:43], v[240:243], v[144:147], v[40:43]\n"
    "ds_read_b64_tr_b16 v[232:233], v254 offset:41984\n"
    "buffer_store_dwordx4 v[32:35], v250, s[60:63], s88 offen sc1\n"
    "v_mfma_f32_16x16x32_bf16 v[44:47], v[240:243], v[148:151], v[44:47]\n"
    "ds_read_b64_tr_b16 v[234:235], v254 offset:46080\n"
    "s_waitcnt lgkmcnt(10)\n"
    "v_mfma_f32_16x16x32_bf16 v[48:51], v[244:247], v[192:195], v[48:51]\n"
    "ds_read_b64_tr_b16 v[236:237], v255 offset:24576\n"
    "v_mfma_f32_16x16x32_bf16 v[52:55], v[244:247], v[196:199], v[52:55]\n"
    "ds_read_b64_tr_b16 v[238:239], v255 offset:28672\n"
    "s_waitcnt lgkmcnt(10)\n"
    "v_mfma_f32_16x16x32_bf16 v[48:51], v[120:123], v[200:203], v[48:51]\n"
    "ds_read_b64_tr_b16 v[240:241], v255 offset:25600\n"
    "v_cvt_pk_bf16_f32 v40, v40, v41\n"
    "v_mfma_f32_16x16x32_bf16 v[52:55], v[120:123], v[204:207], v[52:55]\n"
    "ds_read_b64_tr_b16 v[242:243], v255 offset:29696\n"
    "s_waitcnt lgkmcnt(10)\n"
    "v_mfma_f32_16x16x32_bf16 v[48:51], v[124:127], v[208:211], v[48:51]\n"
    "ds_read_b64_tr_b16 v[244:245], v255 offset:32768\n"
    "v_cvt_pk_bf16_f32 v41, v42, v43\n"
    "v_mfma_f32_16x16x32_bf16 v[52:55], v[124:127], v[212:215], v[52:55]\n"
    "ds_read_b64_tr_b16 v[246:247], v255 offset:36864\n"
    "s_waitcnt lgkmcnt(10)\n"
    "v_mfma_f32_16x16x32_bf16 v[48:51], v[128:131], v[216:219], v[48:51]\n"
    "ds_read_b64_tr_b16 v[120:121], v255 offset:33792\n"
    "v_cvt_pk_bf16_f32 v42, v44, v45\n"
    "v_mfma_f32_16x16x32_bf16 v[52:55], v[128:131], v[220:223], v[52:55]\n"
    "ds_read_b64_tr_b16 v[122:123], v255 offset:37888\n"
    "s_waitcnt lgkmcnt(10)\n"
    "v_mfma_f32_16x16x32_bf16 v[48:51], v[132:135], v[224:227], v[48:51]\n"
    "s_barrier\n"
    "ds_read_b64_tr_b16 v[124:125], v255 offset:40960\n"
    "v_mfma_f32_16x16x32_bf16 v[52:55], v[132:135], v[228:231], v[52:55]\n"
    "ds_read_b64_tr_b16 v[126:127], v255 offset:45056\n"
    "s_waitcnt lgkmcnt(10)\n"
    "v_mfma_f32_16x16x32_bf16 v[48:51], v[232:235], v[144:147], v[48:51]\n"
    "ds_read_b64_tr_b16 v[128:129], v255 offset:41984\n"
    "v_cvt_pk_bf16_f32 v43, v46, v47\n"
    "v_mfma_f32_16x16x32_bf16 v[52:55], v[232:235], v[148:151], v[52:55]\n"
    "ds_read_b64_tr_b16 v[130:131], v255 offset:46080\n"
    "s_waitcnt lgkmcnt(10)\n"
    "v_mfma_f32_16x16x32_bf16 v[56:59], v[236:239], v[192:195], v[56:59]\n"
    "buffer_store_dwordx4 v[40:43], v250, s[60:63], s89 offen sc1\n"
    "v_mfma_f32_16x16x32_bf16 v[60:63], v[236:239], v[196:199], v[60:63]\n"
    "s_waitcnt lgkmcnt(8)\n"
    "v_mfma_f32_16x16x32_bf16 v[56:59], v[240:243], v[200:203], v[56:59]\n"
    "v_cvt_pk_bf16_f32 v48, v48, v49\n"
    "v_mfma_f32_16x16x32_bf16 v[60:63], v[240:243], v[204:207], v[60:63]\n"
    "s_waitcnt lgkmcnt(6)\n"
    "v_mfma_f32_16x16x32_bf16 v[56:59], v[244:247], v[208:211], v[56:59]\n"
    "v_cvt_pk_bf16_f32 v49, v50, v51\n"
    "v_mfma_f32_16x16x32_bf16 v[60:63], v[244:247], v[212:215], v[60:63]\n"
    "s_waitcnt lgkmcnt(4)\n"
    "v_mfma_f32_16x16x32_bf16 v[56:59], v[120:123], v[216:219], v[56:59]\n"
    "v_cvt_pk_bf16_f32 v50, v52, v53\n"
    "v_mfma_f32_16x16x32_bf16 v[60:63], v[120:123], v[220:223], v[60:63]\n"
    "s_waitcnt lgkmcnt(2)\n"
    "v_mfma_f32_16x16x32_bf16 v[56:59], v[124:127], v[224:227], v[56:59]\n"
    "v_cvt_pk_bf16_f32 v51, v54, v55\n"
    "v_mfma_f32_16x16x32_bf16 v[60:63], v[124:127], v[228:231], v[60:63]\n"
    "s_waitcnt lgkmcnt(0)\n"
    "v_mfma_f32_16x16x32_bf16 v[56:59], v[128:131], v[144:147], v[56:59]\n"
    "buffer_store_dwordx4 v[48:51], v250, s[60:63], s90 offen sc1\n"
    "v_mfma_f32_16x16x32_bf16 v[60:63], v[128:131], v[148:151], v[60:63]\n"
    "s_nop 5\n"
    "v_cvt_pk_bf16_f32 v56, v56, v57\n"
    "v_cvt_pk_bf16_f32 v57, v58, v59\n"
    "v_cvt_pk_bf16_f32 v58, v60, v61\n"
    "v_cvt_pk_bf16_f32 v59, v62, v63\n"
    "buffer_store_dwordx4 v[56:59], v250, s[60:63], s91 offen sc1\n"
    "s_cmp_lg_u32 s42, 0\n"
    "s_cbranch_scc1 L_no%=\n"
    "v_add_u32 v163, 0x0, v250\n"
    "ds_write_b128 v163, v[64:67] offset:8192\n"
    "ds_write_b128 v163, v[0:3] offset:0\n"
    "ds_write_b128 v163, v[8:11] offset:1024\n"
    "ds_write_b128 v163, v[16:19] offset:2048\n"
    "ds_write_b128 v163, v[24:27] offset:3072\n"
    "ds_write_b128 v163, v[32:35] offset:4096\n"
    "ds_write_b128 v163, v[40:43] offset:5120\n"
    "ds_write_b128 v163, v[48:51] offset:6144\n"
    "ds_write_b128 v163, v[56:59] offset:7168\n"
    "s_waitcnt lgkmcnt(0)\n"
    "L_no%=:\n"
    "s_barrier\n"
    "s_branch L_end%=\n"
    "L_bk%=:\n"
    "s_setpc_b64 s[24:25]\n"
    "L_aux%=:\n"
    "s_setprio 3\n"
    "v_mov_b32 v0, 0x3f803f80\n"
    "v_lshlrev_b32 v1, 2, v255\n"
    "v_add_u32 v1, 0x27e00, v1\n"
    "ds_write_b32 v1, v0\n"
    "s_getpc_b64 s[30:31]\n"
    "L_pc%=:\n"
    "s_sub_u32 s30, s30, L_pc%=-L_beg%=\n"
    "s_subb_u32 s31, s31, 0\n"
    "s_lshl_b32 s34, s71, 2\n"
    "s_add_u32 s34, s34, s66\n"
    "s_mul_i32 s34, s34, 5\n"
    "v_add_u32 v249, s34, v255\n"
    "v_lshlrev_b32 v249, 7, v249\n"
    "v_min_u32 v249, L_end%=-L_beg%=-4, v249\n"
    "s_mov_b64 exec, 0x1f\n"
    "global_load_dword v238, v249, s[30:31]\n"
    "s_mov_b64 exec, -1\n"
    "s_lshl_b32 s30, s64, 4\n"
    "s_add_u32 s30, s30, s65\n"
    "s_lshl_b32 s30, s30, 2\n"
    "s_add_u32 s30, s30, s66\n"
    "s_mul_i32 s35, s30, 2304\n"
    "v_lshlrev_b32 v198, 7, v255\n"
    "s_mov_b32 m0, 162816\n"
    "s_waitcnt lgkmcnt(0)\n"
    "s_mov_b32 s36, s4\n"
    "s_and_b32 s37, s5, 0xffff\n"
    "s_mov_b64 exec, 0x3ffff\n"
    "buffer_load_dword v198, s[36:39], s35 offen lds\n"
    "s_mov_b64 exec, -1\n"
    "s_setprio 0\n"
    "s_mov_b32 s48, s10\n"
    "s_and_b32 s49, s11, 0xffff\n"
    "s_mov_b32 s50, 0x510000\n"
    "s_mov_b32 s51, 0x20000\n"
    "s_mov_b32 s40, s6\n"
    "s_and_b32 s41, s7, 0xffff\n"
    "s_mov_b32 s42, 0x1e6000\n"
    "s_mov_b32 s43, 0x20000\n"
    "s_mov_b32 s60, s16\n"
    "s_and_b32 s61, s17, 0xffff\n"
    "s_mov_b32 s62, 0x3000000\n"
    "s_mov_b32 s63, 0x20000\n"
    "s_mov_b32 s44, s8\n"
    "s_and_b32 s45, s9, 0xffff\n"
    "s_mov_b32 s46, 0xa2000\n"
    "s_mov_b32 s47, 0x20000\n"
    "s_mov_b32 s52, s12\n"
    "s_and_b32 s53, s13, 0xffff\n"
    "s_mov_b32 s54, 0x510000\n"
    "s_mov_b32 s55, 0x20000\n"
    "s_mov_b32 s56, s14\n"
    "s_and_b32 s57, s15, 0xffff\n"
    "s_mov_b32 s58, 0x240000\n"
    "s_mov_b32 s59, 0x20000\n"
    "s_lshl_b32 s33, s64, 2\n"
    "s_and_b32 s30, s65, 3\n"
    "s_add_u32 s33, s33, s30\n"
    "s_lshl_b32 s33, s33, 2\n"
    "s_add_u32 s33, s33, s66\n"
    "s_lshr_b32 s34, s65, 2\n"
    "s_mul_i32 s30, s33, 9\n"
    "s_add_u32 s30, s30, 0\n"
    "v_min_u32 v208, 8, v255\n"
    "v_add_u32 v208, s30, v208\n"
    "v_min_u32 v208, 2303, v208\n"
    "v_and_b32 v249, 3, v208\n"
    "v_lshrrev_b32 v208, 2, v208\n"
    "v_mul_u32_u24 v249, 576, v249\n"
    "v_add_u32 v249, 64, v249\n"
    "v_and_b32 v249, 0xffffff80, v249\n"
    "v_mul_u32_u24 v208, 9216, v208\n"
    "s_mul_i32 s30, s34, 2304\n"
    "v_add3_u32 v208, v208, v249, s30\n"
    "s_mul_i32 s30, s33, 21\n"
    "s_add_u32 s30, s30, 0\n"
    "v_min_u32 v209, 20, v255\n"
    "v_add_u32 v209, s30, v209\n"
    "v_min_u32 v209, 5183, v209\n"
    "v_mul_u32_u24 v249, 7282, v209\n"
    "v_lshrrev_b32 v249, 16, v249\n"
    "v_mul_u32_u24 v248, 9, v249\n"
    "v_sub_u32 v209, v209, v248\n"
    "v_mul_u32_u24 v248, 22, v209\n"
    "v_lshrrev_b32 v248, 6, v248\n"
    "v_mul_u32_u24 v248, 768, v248\n"
    "v_lshl_add_u32 v209, v209, 7, v248\n"
    "v_mul_u32_u24 v249, 3456, v249\n"
    "s_lshl_b32 s30, s34, 8\n"
    "v_add3_u32 v209, v209, v249, s30\n"
    "s_lshl_b32 s30, s64, 4\n"
    "s_add_u32 s31, s30, s65\n"
    "s_andn2_b32 s33, s31, 3\n"
    "s_sub_u32 s33, s33, 48\n"
    "v_add_u32 v249, s30, v255\n"
    "v_lshrrev_b32 v247, 2, v255\n"
    "v_lshl_add_u32 v247, v247, 4, s33\n"
    "v_and_b32 v248, 3, v255\n"
    "v_add_u32 v247, v247, v248\n"
    "v_cmp_gt_u32 vcc, 16, v255\n"
    "s_nop 1\n"
    "v_cndmask_b32 v249, v247, v249, vcc\n"
    "v_cmp_gt_u32 vcc, 0x100, v249\n"
    "v_lshlrev_b32 v249, 10, v249\n"
    "v_mov_b32 v247, 0x40000000\n"
    "s_and_b64 vcc, vcc, 0xffffff\n"
    "v_cndmask_b32 v241, v247, v249, vcc\n"
    "s_lshl_b32 s30, s31, 10\n"
    "v_lshl_add_u32 v240, v255, 3, s30\n"
    "v_add_u32 v198, s30, v198\n"
    "v_mov_b32 v199, v241\n"
    "v_lshlrev_b32 v250, 4, v255\n"
    "s_lshl_b32 s85, s66, 11\n"
    "v_lshrrev_b32 v249, 3, v255\n"
    "v_bfe_u32 v248, v255, 4, 2\n"
    "v_lshlrev_b32 v248, 1, v248\n"
    "v_and_b32 v247, 7, v255\n"
    "v_xor_b32 v248, v248, v247\n"
    "v_lshlrev_b32 v248, 4, v248\n"
    "s_lshl_b32 s67, s66, 3\n"
    "v_add_u32 v249, s67, v249\n"
    "v_mul_u32_u24 v254, 9216, v249\n"
    "v_add_u32 v254, v254, v248\n"
    "s_mul_i32 s30, s31, 40960\n"
    "s_add_u32 s82, s30, 0x2000000\n"
    "s_lshr_b32 s30, s65, 2\n"
    "s_mul_i32 s30, s30, 18432\n"
    "s_add_u32 s30, s30, 15360\n"
    "s_and_b32 s33, s65, 3\n"
    "s_lshl_b32 s34, s33, 12\n"
    "s_add_u32 s34, s34, s30\n"
    "s_lshl_b32 s35, s66, 1\n"
    "v_bfe_u32 v4, v255, 1, 3\n"
    "v_lshrrev_b32 v5, 5, v255\n"
    "v_add_u32 v5, s35, v5\n"
    "v_xor_b32 v5, v5, v4\n"
    "v_and_b32 v6, 15, v255\n"
    "v_lshlrev_b32 v6, 7, v6\n"
    "v_bfe_u32 v7, v255, 4, 1\n"
    "v_mul_u32_u24 v7, 73728, v7\n"
    "v_add_u32 v6, v6, v7\n"
    "v_lshl_add_u32 v2, v5, 4, v6\n"
    "v_add_u32 v2, s34, v2\n"
    "s_lshl_b32 s33, s33, 1\n"
    "s_lshr_b32 s34, s66, 1\n"
    "s_add_u32 s33, s33, s34\n"
    "s_and_b32 s34, s66, 1\n"
    "s_lshl_b32 s34, s34, 3\n"
    "s_add_u32 s30, s30, s34\n"
    "v_xor_b32 v5, s33, v4\n"
    "v_lshl_add_u32 v3, v5, 4, v6\n"
    "v_add_u32 v3, s30, v3\n"
    "s_and_b32 s34, s65, 1\n"
    "s_mul_i32 s35, s65, 288\n"
    "s_lshl_b32 s31, s34, 5\n"
    "s_add_u32 s31, s31, s35\n"
    "s_mov_b32 s30, 0\n"
    "s_mul_i32 s77, s30, 1152\n"
    "s_add_u32 s30, s30, s31\n"
    "s_lshl_b32 s72, s30, 1\n"
    "s_mov_b32 s30, 64\n"
    "s_mul_i32 s78, s30, 1152\n"
    "s_add_u32 s30, s30, s31\n"
    "s_lshl_b32 s73, s30, 1\n"
    "s_mov_b32 s30, 128\n"
    "s_mul_i32 s79, s30, 1152\n"
    "s_add_u32 s30, s30, s31\n"
    "s_lshl_b32 s74, s30, 1\n"
    "s_mov_b32 s30, 192\n"
    "s_mul_i32 s80, s30, 1152\n"
    "s_add_u32 s30, s30, s31\n"
    "s_lshl_b32 s75, s30, 1\n"
    "s_xor_b32 s30, s34, 1\n"
    "s_lshl_b32 s30, s30, 8\n"
    "s_add_u32 s30, s30, s35\n"
    "s_lshl_b32 s76, s30, 1\n"
    "s_lshr_b32 s32, s65, 3\n"
    "s_xor_b32 s33, s32, 1\n"
    "s_mul_i32 s32, s32, 3\n"
    "s_and_b32 s30, s65, 3\n"
    "s_add_u32 s31, s33, 0\n"
    "s_sub_u32 s33, s30, s33\n"
    "s_cmp_eq_u32 s30, s32\n"
    "s_cselect_b32 s30, 0, s33\n"
    "s_cselect_b32 s31, s32, s31\n"
    "s_andn2_b32 s32, s65, 3\n"
    "s_add_u32 s31, s31, s32\n"
    "s_mul_i32 s31, s31, 72\n"
    "s_andn2_b32 s31, s31, 15\n"
    "s_mul_i32 s30, s30, 1152\n"
    "s_add_u32 s30, s30, s31\n"
    "s_sub_u32 s26, s30, 0\n"
    "s_lshr_b32 s32, s65, 3\n"
    "s_xor_b32 s33, s32, 1\n"
    "s_mul_i32 s32, s32, 3\n"
    "s_and_b32 s30, s65, 3\n"
    "s_add_u32 s31, s33, 1\n"
    "s_sub_u32 s33, s30, s33\n"
    "s_cmp_eq_u32 s30, s32\n"
    "s_cselect_b32 s30, 1, s33\n"
    "s_cselect_b32 s31, s32, s31\n"
    "s_andn2_b32 s32, s65, 3\n"
    "s_add_u32 s31, s31, s32\n"
    "s_mul_i32 s31, s31, 72\n"
    "s_andn2_b32 s31, s31, 15\n"
    "s_mul_i32 s30, s30, 1152\n"
    "s_add_u32 s30, s30, s31\n"
    "s_sub_u32 s27, s30, 80\n"
    "s_lshr_b32 s32, s65, 3\n"
    "s_xor_b32 s33, s32, 1\n"
    "s_mul_i32 s32, s32, 3\n"
    "s_and_b32 s30, s65, 3\n"
    "s_add_u32 s31, s33, 2\n"
    "s_sub_u32 s33, s30, s33\n"
    "s_cmp_eq_u32 s30, s32\n"
    "s_cselect_b32 s30, 2, s33\n"
    "s_cselect_b32 s31, s32, s31\n"
    "s_andn2_b32 s32, s65, 3\n"
    "s_add_u32 s31, s31, s32\n"
    "s_mul_i32 s31, s31, 72\n"
    "s_andn2_b32 s31, s31, 15\n"
    "s_mul_i32 s30, s30, 1152\n"
    "s_add_u32 s30, s30, s31\n"
    "s_sub_u32 s28, s30, 160\n"
    "v_lshrrev_b32 v249, 4, v255\n"
    "s_lshl_b32 s30, s66, 3\n"
    "s_add_u32 s30, s30, 0\n"
    "v_add_u32 v249, s30, v249\n"
    "v_lshlrev_b32 v248, 1, v249\n"
    "v_sub_u32 v248, v255, v248\n"
    "v_and_b32 v248, 15, v248\n"
    "v_min_u32 v248, 14, v248\n"
    "v_mov_b32 v247, s26\n"
    "v_mov_b32 v246, s27\n"
    "v_cmp_le_u32 vcc, 5, v248\n"
    "s_nop 1\n"
    "v_cndmask_b32 v247, v247, v246, vcc\n"
    "v_mov_b32 v246, s28\n"
    "v_cmp_le_u32 vcc, 10, v248\n"
    "s_nop 1\n"
    "v_cndmask_b32 v247, v247, v246, vcc\n"
    "v_mul_u32_u24 v251, 3456, v249\n"
    "v_lshl_add_u32 v251, v248, 4, v251\n"
    "v_add_u32 v251, v251, v247\n"
    "v_lshrrev_b32 v249, 4, v255\n"
    "s_lshl_b32 s30, s66, 3\n"
    "s_add_u32 s30, s30, 4\n"
    "v_add_u32 v249, s30, v249\n"
    "v_lshlrev_b32 v248, 1, v249\n"
    "v_sub_u32 v248, v255, v248\n"
    "v_and_b32 v248, 15, v248\n"
    "v_min_u32 v248, 14, v248\n"
    "v_mov_b32 v247, s26\n"
    "v_mov_b32 v246, s27\n"
    "v_cmp_le_u32 vcc, 5, v248\n"
    "s_nop 1\n"
    "v_cndmask_b32 v247, v247, v246, vcc\n"
    "v_mov_b32 v246, s28\n"
    "v_cmp_le_u32 vcc, 10, v248\n"
    "s_nop 1\n"
    "v_cndmask_b32 v247, v247, v246, vcc\n"
    "v_mul_u32_u24 v216, 3456, v249\n"
    "v_lshl_add_u32 v216, v248, 4, v216\n"
    "v_add_u32 v216, v216, v247\n"
    "v_subrev_u32 v216, 1024, v216\n"
    "v_mov_b32 v228, 0xff81ff81\n"
    "v_mov_b32 v229, 0xff81ff81\n"
    "v_mov_b32 v230, 0xff81ff81\n"
    "v_mov_b32 v231, 0xff81ff81\n"
    "s_and_b32 s30, s65, 3\n"
    "s_lshr_b32 s33, s64, 2\n"
    "s_lshl_b32 s33, s33, 2\n"
    "s_add_u32 s33, s33, s30\n"
    "s_lshl_b32 s33, s33, 2\n"
    "s_add_u32 s33, s33, s66\n"
    "s_lshr_b32 s34, s65, 2\n"
    "s_mul_i32 s30, s33, 36\n"
    "s_add_u32 s30, s30, 0\n"
    "v_min_u32 v210, 35, v255\n"
    "v_add_u32 v210, s30, v210\n"
    "v_min_u32 v210, 2303, v210\n"
    "v_and_b32 v249, 3, v210\n"
    "v_lshrrev_b32 v210, 2, v210\n"
    "v_mul_u32_u24 v249, 576, v249\n"
    "v_add_u32 v249, 64, v249\n"
    "v_and_b32 v249, 0xffffff80, v249\n"
    "v_mul_u32_u24 v210, 9216, v210\n"
    "s_mul_i32 s30, s34, 2304\n"
    "v_add3_u32 v210, v210, v249, s30\n"
    "s_and_b32 s35, s33, 15\n"
    "s_mul_i32 s30, s35, 41\n"
    "s_add_u32 s30, s30, 0\n"
    "v_min_u32 v212, 40, v255\n"
    "v_add_u32 v212, s30, v212\n"
    "v_min_u32 v212, 647, v212\n"
    "s_lshl_b32 s30, s34, 2\n"
    "s_lshr_b32 s35, s33, 4\n"
    "s_add_u32 s30, s30, s35\n"
    "s_mul_i32 s30, s30, 331776\n"
    "v_lshl_add_u32 v212, v212, 7, s30\n"
    "s_mul_i32 s30, s33, 6\n"
    "s_add_u32 s30, s30, 0\n"
    "v_min_u32 v214, 5, v255\n"
    "v_add_u32 v214, s30, v214\n"
    "v_min_u32 v214, 323, v214\n"
    "s_mul_i32 s30, s34, 165888\n"
    "v_lshl_add_u32 v214, v214, 7, s30\n"
    "s_and_b32 s71, s64, 3\n"
    "s_add_u32 m0, s70, 73728\n"
    "s_add_u32 s67, s68, 18432\n"
    "buffer_load_dwordx4 v170, s[36:39], s67 offen offset:0 lds\n"
    "buffer_load_dwordx4 v171, s[36:39], s67 offen offset:1024 lds\n"
    "s_add_u32 m0, s70, 75776\n"
    "s_add_u32 s67, s68, 18560\n"
    "buffer_load_dwordx4 v170, s[36:39], s67 offen offset:0 lds\n"
    "buffer_load_dwordx4 v171, s[36:39], s67 offen offset:1024 lds\n"
    "s_add_u32 m0, s70, 77824\n"
    "s_add_u32 s67, s68, 18688\n"
    "buffer_load_dwordx4 v170, s[36:39], s67 offen offset:0 lds\n"
    "buffer_load_dwordx4 v171, s[36:39], s67 offen offset:1024 lds\n"
    "s_add_u32 m0, s70, 79872\n"
    "s_add_u32 s67, s68, 18816\n"
    "buffer_load_dwordx4 v170, s[36:39], s67 offen offset:0 lds\n"
    "buffer_load_dwordx4 v171, s[36:39], s67 offen offset:1024 lds\n"
    "s_add_u32 m0, s70, 81920\n"
    "s_add_u32 s67, s68, 18944\n"
    "buffer_load_dwordx4 v170, s[36:39], s67 offen offset:0 lds\n"
    "buffer_load_dwordx4 v171, s[36:39], s67 offen offset:1024 lds\n"
    "s_add_u32 m0, s70, 83968\n"
    "s_add_u32 s67, s68, 19072\n"
    "buffer_load_dwordx4 v170, s[36:39], s67 offen offset:0 lds\n"
    "buffer_load_dwordx4 v171, s[36:39], s67 offen offset:1024 lds\n"
    "s_add_u32 m0, s70, 86016\n"
    "s_add_u32 s67, s68, 19200\n"
    "buffer_load_dwordx4 v170, s[36:39], s67 offen offset:0 lds\n"
    "buffer_load_dwordx4 v171, s[36:39], s67 offen offset:1024 lds\n"
    "s_add_u32 m0, s70, 88064\n"
    "s_add_u32 s67, s68, 19328\n"
    "buffer_load_dwordx4 v170, s[36:39], s67 offen offset:0 lds\n"
    "buffer_load_dwordx4 v171, s[36:39], s67 offen offset:1024 lds\n"
    "s_add_u32 m0, s70, 90112\n"
    "s_add_u32 s67, s68, 19456\n"
    "buffer_load_dwordx4 v170, s[36:39], s67 offen offset:0 lds\n"
    "buffer_load_dwordx4 v171, s[36:39], s67 offen offset:1024 lds\n"
    "s_mov_b32 m0, 162816\n"
    "s_mov_b64 exec, 0x1fffff\n"
    "buffer_load_dword v209, s[40:43], 0 offen lds\n"
    "s_mov_b64 exec, -1\n"
    "s_add_u32 m0, s85, 0\n"
    "s_mov_b32 s67, 0\n"
    "buffer_load_dwordx4 v251, s[40:43], s67 offen offset:0 lds\n"
    "buffer_load_dwordx4 v216, s[40:43], s67 offen offset:1024 lds\n"
    "s_waitcnt vmcnt(9)\n"
    "s_barrier\n"
    "s_setprio 2\n"
    "s_mov_b32 exec_lo, 0xffff\n"
    "s_mov_b32 exec_hi, 0xffff\n"
    "ds_read_b128 v[180:183], v2\n"
    "ds_read_b128 v[184:187], v2 offset:2048\n"
    "ds_read_b64 v[188:189], v3 offset:16384\n"
    "s_mov_b64 exec, -1\n"
    "s_setprio 0\n"
    "s_waitcnt lgkmcnt(0)\n"
    "s_barrier\n"
    "s_add_u32 m0, s85, 8192\n"
    "s_mov_b32 s67, 110592\n"
    "buffer_load_dwordx4 v251, s[40:43], s67 offen offset:0 lds\n"
    "buffer_load_dwordx4 v216, s[40:43], s67 offen offset:1024 lds\n"
    "s_add_u32 m0, s85, 16384\n"
    "s_mov_b32 s67, 221184\n"
    "buffer_load_dwordx4 v251, s[40:43], s67 offen offset:0 lds\n"
    "buffer_load_dwordx4 v216, s[40:43], s67 offen offset:1024 lds\n"
    "s_lshl_b32 s26, s66, 3\n"
    "s_lshr_b32 s26, 0x02010900, s26\n"
    "s_and_b32 s26, s26, 0xff\n"
    "s_lshl_b32 s28, s26, 12\n"
    "s_getpc_b64 s[26:27]\n"
    "L_hp0_%=:\n"
    "s_sub_u32 s26, s26, L_hp0_%=-L_st1_%=\n"
    "s_subb_u32 s27, s27, 0\n"
    "s_add_u32 s26, s26, s28\n"
    "s_addc_u32 s27, s27, 0\n"
    "s_mov_b64 exec, 0\n"
    "s_swappc_b64 s[24:25], s[26:27]\n"
    "s_mov_b64 exec, -1\n"
    "s_waitcnt vmcnt(7)\n"
    "s_barrier\n"
    "s_setprio 2\n"
    "s_mov_b32 exec_lo, 0xffff0000\n"
    "s_mov_b32 exec_hi, 0xffff0000\n"
    "ds_read_b128 v[180:183], v2\n"
    "ds_read_b128 v[184:187], v2 offset:2048\n"
    "ds_read_b64 v[188:189], v3 offset:16384\n"
    "s_mov_b64 exec, -1\n"
    "s_setprio 0\n"
    "s_add_u32 m0, s85, 24576\n"
    "s_mov_b32 s67, 331776\n"
    "s_nop 0\n"
    "L_st10_%=:\n"
    "s_cbranch_execz L_bk%=\n"
    "buffer_load_dwordx4 v251, s[40:43], s67 offen offset:0 lds\n"
    "buffer_load_dwordx4 v216, s[40:43], s67 offen offset:1024 lds\n"
    "s_add_u32 m0, s85, 32768\n"
    "s_mov_b32 s67, 442368\n"
    "buffer_load_dwordx4 v251, s[40:43], s67 offen offset:0 lds\n"
    "buffer_load_dwordx4 v216, s[40:43], s67 offen offset:1024 lds\n"
    "s_add_u32 m0, s85, 40960\n"
    "s_mov_b32 s67, 552960\n"
    "buffer_load_dwordx4 v251, s[40:43], s67 offen offset:0 lds\n"
    "buffer_load_dwordx4 v216, s[40:43], s67 offen offset:1024 lds\n"
    "s_waitcnt lgkmcnt(0)\n"
    "s_waitcnt vmcnt(6)\n"
    "s_barrier\n"
    "s_add_u32 m0, s85, 49152\n"
    "s_mov_b32 s67, 663552\n"
    "buffer_load_dwordx4 v251, s[40:43], s67 offen offset:0 lds\n"
    "buffer_load_dwordx4 v216, s[40:43], s67 offen offset:1024 lds\n"
    "s_lshl_b32 s30, s65, 3\n"
    "s_cmp_eq_u32 s66, 0\n"
    "s_cselect_b64 vcc, 0xffff, 0\n"
    "v_mov_b32 v247, 0x40000000\n"
    "v_add_u32 v249, s30, v241\n"
    "v_cndmask_b32 v243, v247, v249, vcc\n"
    "s_and_b32 s31, s65, 3\n"
    "s_lshl_b32 s30, s31, 2\n"
    "s_add_u32 s30, s30, s66\n"
    "s_lshl_b32 s30, s30, 3\n"
    "s_andn2_b32 s33, s65, 3\n"
    "s_lshl_b32 vcc_lo, 15, s33\n"
    "s_mov_b32 vcc_hi, 0\n"
    "v_add_u32 v249, s30, v241\n"
    "v_cndmask_b32 v236, v247, v249, vcc\n"
    "s_lshl_b32 s30, s31, 3\n"
    "s_add_u32 s30, s30, 128\n"
    "s_cmp_eq_u32 s66, 0\n"
    "s_cselect_b32 vcc_lo, vcc_lo, 0\n"
    "v_add_u32 v249, s30, v241\n"
    "v_cndmask_b32 v237, v247, v249, vcc\n"
    "s_add_u32 m0, s85, 57344\n"
    "s_mov_b32 s67, 774144\n"
    "buffer_load_dwordx4 v251, s[40:43], s67 offen offset:0 lds\n"
    "buffer_load_dwordx4 v216, s[40:43], s67 offen offset:1024 lds\n"
    "s_and_b32 s30, s65, 3\n"
    "s_lshl_b32 s30, s30, 3\n"
    "s_add_u32 s30, s30, 160\n"
    "v_lshrrev_b32 v248, 2, v255\n"
    "v_max_u32 v248, 3, v248\n"
    "v_lshlrev_b32 v248, 5, v248\n"
    "v_sub_u32 v248, s30, v248\n"
    "v_add_u32 v248, v248, v241\n"
    "s_andn2_b32 s33, s65, 3\n"
    "s_lshl_b32 s30, 15, s33\n"
    "s_or_b32 s30, s30, 0xff0000\n"
    "s_cmp_eq_u32 s66, 0\n"
    "s_cselect_b32 vcc_lo, s30, 0\n"
    "s_mov_b32 vcc_hi, 0\n"
    "v_cndmask_b32 v242, v247, v248, vcc\n"
    "s_lshl_b32 vcc_lo, 1, s65\n"
    "v_cndmask_b32 v241, v247, v241, vcc\n"
    "s_lshl_b32 s30, s64, 2\n"
    "s_min_u32 s30, s30, 8\n"
    "s_sub_u32 s30, 8, s30\n"
    "s_lshl_b32 s96, -1, s30\n"
    "s_and_b32 s96, s96, 0xfff\n"
    "s_mov_b32 s97, 0\n"
    "s_add_u32 m0, s85, 65536\n"
    "s_mov_b32 s67, 884736\n"
    "buffer_load_dwordx4 v251, s[40:43], s67 offen offset:0 lds\n"
    "buffer_load_dwordx4 v216, s[40:43], s67 offen offset:1024 lds\n"
    "v_lshrrev_b32 v249, 2, v255\n"
    "s_lshl_b32 s30, s66, 4\n"
    "v_add_u32 v249, s30, v249\n"
    "v_mul_u32_u24 v252, 9216, v249\n"
    "v_bfe_u32 v249, v255, 4, 1\n"
    "v_lshlrev_b32 v249, 1, v249\n"
    "v_and_b32 v248, 3, v255\n"
    "v_xor_b32 v249, v249, v248\n"
    "v_lshl_add_u32 v252, v249, 4, v252\n"
    "s_lshl_b32 s83, s64, 2\n"
    "s_add_u32 s83, s83, s66\n"
    "s_lshl_b32 s30, s65, 6\n"
    "s_add_u32 s30, s30, s83\n"
    "s_mul_i32 s30, s30, 2304\n"
    "s_add_u32 s68, s30, 0x600000\n"
    "s_lshl_b32 s30, s30, 1\n"
    "s_add_u32 s81, s30, 0x100000\n"
    "s_mul_i32 s70, s66, 4608\n"
    "s_mul_i32 s84, s66, 1152\n"
    "s_waitcnt vmcnt(8)\n"
    "s_barrier\n"
    "s_add_u32 m0, s85, 73728\n"
    "s_mov_b32 s67, 995328\n"
    "buffer_load_dwordx4 v251, s[40:43], s67 offen offset:0 lds\n"
    "buffer_load_dwordx4 v216, s[40:43], s67 offen offset:1024 lds\n"
    "s_add_u32 s30, s65, 0\n"
    "s_and_b32 s30, s30, 3\n"
    "s_andn2_b32 s31, s65, 3\n"
    "s_add_u32 s30, s30, s31\n"
    "s_and_b32 s31, s30, 1\n"
    "s_mul_i32 s33, s30, 288\n"
    "s_lshl_b32 s34, s31, 5\n"
    "s_add_u32 s34, s34, s33\n"
    "s_mul_i32 s4, s34, 1152\n"
    "s_xor_b32 s31, s31, 1\n"
    "s_lshl_b32 s31, s31, 8\n"
    "s_add_u32 s31, s31, s33\n"
    "s_mul_i32 s8, s31, 1152\n"
    "s_mul_i32 s31, s30, 36\n"
    "s_lshr_b32 s33, s66, 1\n"
    "s_lshl_b32 s33, s33, 2\n"
    "s_add_u32 s31, s31, s33\n"
    "s_lshl_b32 s33, s66, 3\n"
    "s_add_u32 s31, s31, s33\n"
    "s_mul_i32 s12, s31, 1152\n"
    "s_lshl_b32 s31, s64, 4\n"
    "s_add_u32 s31, s31, s30\n"
    "s_mul_i32 s31, s31, 81920\n"
    "s_add_u32 s31, s31, s85\n"
    "s_add_u32 s31, s31, s85\n"
    "s_add_u32 s16, s31, 0x900000\n"
    "s_and_b32 s30, s65, 3\n"
    "s_cmp_ge_u32 0, s30\n"
    "s_cselect_b32 s30, 1, 0\n"
    "s_andn2_b32 s31, s65, 3\n"
    "s_add_u32 s30, s30, s31\n"
    "s_and_b32 s31, s30, 1\n"
    "s_mul_i32 s33, s30, 288\n"
    "s_lshl_b32 s34, s31, 5\n"
    "s_add_u32 s34, s34, s33\n"
    "s_mul_i32 s5, s34, 1152\n"
    "s_xor_b32 s31, s31, 1\n"
    "s_lshl_b32 s31, s31, 8\n"
    "s_add_u32 s31, s31, s33\n"
    "s_mul_i32 s9, s31, 1152\n"
    "s_mul_i32 s31, s30, 36\n"
    "s_lshr_b32 s33, s66, 1\n"
    "s_lshl_b32 s33, s33, 2\n"
    "s_add_u32 s31, s31, s33\n"
    "s_lshl_b32 s33, s66, 3\n"
    "s_add_u32 s31, s31, s33\n"
    "s_mul_i32 s13, s31, 1152\n"
    "s_lshl_b32 s31, s64, 4\n"
    "s_add_u32 s31, s31, s30\n"
    "s_mul_i32 s31, s31, 81920\n"
    "s_add_u32 s31, s31, s85\n"
    "s_add_u32 s31, s31, s85\n"
    "s_add_u32 s17, s31, 0x900000\n"
    "s_andn2_b32 s26, s65, 3\n"
    "s_lshl_b32 s26, 15, s26\n"
    "s_or_b32 s26, s26, 0xff0000\n"
    "s_lshl_b32 s27, 1, s65\n"
    "s_cmp_eq_u32 s66, 0\n"
    "s_cselect_b32 exec_lo, s26, s27\n"
    "s_mov_b32 exec_hi, 0\n"
    "buffer_load_dwordx2 v[244:245], v199, s[60:63], 0 offen sc1\n"
    "s_mov_b64 exec, -1\n"
    "s_cmp_lg_u32 s66, 1\n"
    "s_cbranch_scc1 L_q1_%=\n"
    "s_mov_b32 m0, 162816\n"
    "s_mov_b64 exec, 0xf\n"
    "buffer_load_dword v198, s[60:63], 0 offen sc1 lds\n"
    "s_mov_b64 exec, -1\n"
    "L_q1_%=:\n"
    "s_mov_b32 m0, 162816\n"
    "s_mov_b64 exec, 0x1ff\n"
    "buffer_load_dword v208, s[48:51], 0 offen lds\n"
    "s_mov_b64 exec, -1\n"
    "s_waitcnt vmcnt(8)\n"
    "s_barrier\n"
    "s_add_u32 m0, s85, 81920\n"
    "s_mov_b32 s67, 1105920\n"
    "buffer_load_dwordx4 v251, s[40:43], s67 offen offset:0 lds\n"
    "buffer_load_dwordx4 v216, s[40:43], s67 offen offset:1024 lds\n"
    "s_and_b32 s30, s65, 3\n"
    "s_cmp_ge_u32 1, s30\n"
    "s_cselect_b32 s30, 2, 1\n"
    "s_andn2_b32 s31, s65, 3\n"
    "s_add_u32 s30, s30, s31\n"
    "s_and_b32 s31, s30, 1\n"
    "s_mul_i32 s33, s30, 288\n"
    "s_lshl_b32 s34, s31, 5\n"
    "s_add_u32 s34, s34, s33\n"
    "s_mul_i32 s6, s34, 1152\n"
    "s_xor_b32 s31, s31, 1\n"
    "s_lshl_b32 s31, s31, 8\n"
    "s_add_u32 s31, s31, s33\n"
    "s_mul_i32 s10, s31, 1152\n"
    "s_mul_i32 s31, s30, 36\n"
    "s_lshr_b32 s33, s66, 1\n"
    "s_lshl_b32 s33, s33, 2\n"
    "s_add_u32 s31, s31, s33\n"
    "s_lshl_b32 s33, s66, 3\n"
    "s_add_u32 s31, s31, s33\n"
    "s_mul_i32 s14, s31, 1152\n"
    "s_lshl_b32 s31, s64, 4\n"
    "s_add_u32 s31, s31, s30\n"
    "s_mul_i32 s31, s31, 81920\n"
    "s_add_u32 s31, s31, s85\n"
    "s_add_u32 s31, s31, s85\n"
    "s_add_u32 s18, s31, 0x900000\n"
    "s_add_u32 m0, s85, 90112\n"
    "s_mov_b32 s67, 1216512\n"
    "buffer_load_dwordx4 v251, s[40:43], s67 offen offset:0 lds\n"
    "buffer_load_dwordx4 v216, s[40:43], s67 offen offset:1024 lds\n"
    "s_and_b32 s30, s65, 3\n"
    "s_cmp_ge_u32 2, s30\n"
    "s_cselect_b32 s30, 3, 2\n"
    "s_andn2_b32 s31, s65, 3\n"
    "s_add_u32 s30, s30, s31\n"
    "s_and_b32 s31, s30, 1\n"
    "s_mul_i32 s33, s30, 288\n"
    "s_lshl_b32 s34, s31, 5\n"
    "s_add_u32 s34, s34, s33\n"
    "s_mul_i32 s7, s34, 1152\n"
    "s_xor_b32 s31, s31, 1\n"
    "s_lshl_b32 s31, s31, 8\n"
    "s_add_u32 s31, s31, s33\n"
    "s_mul_i32 s11, s31, 1152\n"
    "s_mul_i32 s31, s30, 36\n"
    "s_lshr_b32 s33, s66, 1\n"
    "s_lshl_b32 s33, s33, 2\n"
    "s_add_u32 s31, s31, s33\n"
    "s_lshl_b32 s33, s66, 3\n"
    "s_add_u32 s31, s31, s33\n"
    "s_mul_i32 s15, s31, 1152\n"
    "s_lshl_b32 s31, s64, 4\n"
    "s_add_u32 s31, s31, s30\n"
    "s_mul_i32 s31, s31, 81920\n"
    "s_add_u32 s31, s31, s85\n"
    "s_add_u32 s31, s31, s85\n"
    "s_add_u32 s19, s31, 0x900000\n"
    "s_lshl_b32 s26, s66, 3\n"
    "s_lshr_b32 s26, 0x0b04030a, s26\n"
    "s_and_b32 s26, s26, 0xff\n"
    "s_lshl_b32 s28, s26, 12\n"
    "s_getpc_b64 s[26:27]\n"
    "L_hp1_%=:\n"
    "s_sub_u32 s26, s26, L_hp1_%=-L_st1_%=\n"
    "s_subb_u32 s27, s27, 0\n"
    "s_add_u32 s26, s26, s28\n"
    "s_addc_u32 s27, s27, 0\n"
    "s_mov_b64 exec, 0\n"
    "s_swappc_b64 s[24:25], s[26:27]\n"
    "s_mov_b64 exec, -1\n"
    "s_waitcnt vmcnt(8)\n"
    "s_barrier\n"
    "s_add_u32 m0, s85, 98304\n"
    "s_mov_b32 s67, 1327104\n"
    "buffer_load_dwordx4 v251, s[40:43], s67 offen offset:0 lds\n"
    "buffer_load_dwordx4 v216, s[40:43], s67 offen offset:1024 lds\n"
    "s_andn2_b32 s30, s65, 3\n"
    "s_mul_i32 s30, s30, 36\n"
    "s_mul_i32 s31, s66, 36\n"
    "s_add_u32 s30, s30, s31\n"
    "s_add_u32 s30, s30, 16\n"
    "s_mul_i32 s86, s30, 1152\n"
    "s_lshl_b32 s30, s64, 2\n"
    "s_lshr_b32 s31, s65, 2\n"
    "s_add_u32 s30, s30, s31\n"
    "s_lshl_b32 s30, s30, 12\n"
    "s_add_u32 s30, s30, s69\n"
    "s_add_u32 s87, s30, 0x80000\n"
    "s_add_u32 m0, s85, 106496\n"
    "s_mov_b32 s67, 1437696\n"
    "buffer_load_dwordx4 v251, s[40:43], s67 offen offset:0 lds\n"
    "buffer_load_dwordx4 v216, s[40:43], s67 offen offset:1024 lds\n"
    "v_lshrrev_b32 v249, 3, v255\n"
    "v_bfe_u32 v248, v255, 4, 2\n"
    "v_lshlrev_b32 v248, 1, v248\n"
    "v_and_b32 v247, 7, v255\n"
    "v_xor_b32 v248, v248, v247\n"
    "v_lshlrev_b32 v248, 4, v248\n"
    "s_mul_i32 s31, s66, 9216\n"
    "v_mul_u32_u24 v249, 1152, v249\n"
    "v_add3_u32 v249, v249, v248, s31\n"
    "s_and_b32 s30, s65, 3\n"
    "s_lshl_b32 s30, s30, 8\n"
    "v_add_u32 v220, s30, v249\n"
    "s_lshr_b32 s30, s30, 3\n"
    "v_xor_b32 v249, s30, v249\n"
    "v_add_u32 v221, 0x400, v249\n"
    "s_and_b32 s30, s65, 3\n"
    "s_mul_i32 s31, s30, 224\n"
    "s_sub_u32 s31, 768, s31\n"
    "s_lshl_b32 s30, s30, 8\n"
    "v_add_u32 v249, 0, v255\n"
    "v_mul_u32_u24 v248, 3641, v249\n"
    "v_lshrrev_b32 v248, 16, v248\n"
    "v_mul_u32_u24 v247, 18, v248\n"
    "v_sub_u32 v249, v249, v247\n"
    "v_lshrrev_b32 v249, 4, v249\n"
    "v_mul_u32_u24 v249, s31, v249\n"
    "v_mul_u32_u24 v248, 864, v248\n"
    "v_add3_u32 v248, v248, v250, s30\n"
    "v_add_u32 v222, v249, v248\n"
    "s_waitcnt vmcnt(10)\n"
    "s_barrier\n"
    "s_add_u32 m0, s85, 114688\n"
    "s_mov_b32 s67, 1548288\n"
    "buffer_load_dwordx4 v251, s[40:43], s67 offen offset:0 lds\n"
    "buffer_load_dwordx4 v216, s[40:43], s67 offen offset:1024 lds\n"
    "s_and_b32 s30, s65, 3\n"
    "s_mul_i32 s31, s30, 224\n"
    "s_sub_u32 s31, 768, s31\n"
    "s_lshl_b32 s30, s30, 8\n"
    "v_add_u32 v249, 64, v255\n"
    "v_mul_u32_u24 v248, 3641, v249\n"
    "v_lshrrev_b32 v248, 16, v248\n"
    "v_mul_u32_u24 v247, 18, v248\n"
    "v_sub_u32 v249, v249, v247\n"
    "v_lshrrev_b32 v249, 4, v249\n"
    "v_mul_u32_u24 v249, s31, v249\n"
    "v_mul_u32_u24 v248, 864, v248\n"
    "v_add3_u32 v248, v248, v250, s30\n"
    "v_add_u32 v223, v249, v248\n"
    "s_add_u32 m0, s85, 122880\n"
    "s_mov_b32 s67, 1658880\n"
    "buffer_load_dwordx4 v251, s[40:43], s67 offen offset:0 lds\n"
    "buffer_load_dwordx4 v216, s[40:43], s67 offen offset:1024 lds\n"
    "s_and_b32 s30, s65, 3\n"
    "s_mul_i32 s31, s30, 224\n"
    "s_sub_u32 s31, 768, s31\n"
    "s_lshl_b32 s30, s30, 8\n"
    "v_add_u32 v249, 128, v255\n"
    "v_mul_u32_u24 v248, 3641, v249\n"
    "v_lshrrev_b32 v248, 16, v248\n"
    "v_mul_u32_u24 v247, 18, v248\n"
    "v_sub_u32 v249, v249, v247\n"
    "v_lshrrev_b32 v249, 4, v249\n"
    "v_mul_u32_u24 v249, s31, v249\n"
    "v_mul_u32_u24 v248, 864, v248\n"
    "v_add3_u32 v248, v248, v250, s30\n"
    "v_add_u32 v197, v249, v248\n"
    "s_cmp_lg_u32 s71, 0\n"
    "s_cbranch_scc1 L_q5_%=\n"
    "s_mov_b32 m0, 162816\n"
    "s_mov_b32 exec_lo, 0xffffffff\n"
    "s_mov_b32 exec_hi, 0xf\n"
    "s_mov_b32 s67, 128\n"
    "buffer_load_dword v210, s[48:51], s67 offen lds\n"
    "s_mov_b64 exec, -1\n"
    "L_q5_%=:\n"
    "s_cmp_lg_u32 s71, 1\n"
    "s_cbranch_scc1 L_q6_%=\n"
    "s_mov_b32 m0, 162816\n"
    "s_mov_b32 exec_lo, 0xffffffff\n"
    "s_mov_b32 exec_hi, 0xf\n"
    "s_mov_b32 s67, 256\n"
    "buffer_load_dword v210, s[48:51], s67 offen lds\n"
    "s_mov_b64 exec, -1\n"
    "L_q6_%=:\n"
    "s_waitcnt vmcnt(8)\n"
    "s_barrier\n"
    "s_add_u32 m0, s85, 131072\n"
    "s_mov_b32 s67, 1769472\n"
    "buffer_load_dwordx4 v251, s[40:43], s67 offen offset:0 lds\n"
    "buffer_load_dwordx4 v216, s[40:43], s67 offen offset:1024 lds\n"
    "v_and_b32 v249, 15, v255\n"
    "v_lshrrev_b32 v248, 4, v255\n"
    "v_lshlrev_b32 v247, 2, v248\n"
    "v_sub_u32 v247, v249, v247\n"
    "v_cmp_le_i32 s[88:89], 0, v247\n"
    "v_cmp_le_i32 s[90:91], 1, v247\n"
    "v_cmp_le_i32 s[92:93], 2, v247\n"
    "v_cmp_le_i32 s[94:95], 3, v247\n"
    "v_and_b32 v246, 1, v255\n"
    "v_lshlrev_b32 v246, 4, v246\n"
    "v_mov_b32 v156, 0x3f80\n"
    "v_lshlrev_b32 v246, v246, v156\n"
    "v_ashrrev_i32 v247, 1, v247\n"
    "v_mov_b32 v164, 0\n"
    "v_mov_b32 v165, 0\n"
    "v_mov_b32 v168, 0\n"
    "v_mov_b32 v169, 0\n"
    "v_mov_b32 v51, 0\n"
    "v_mov_b32 v52, 0\n"
    "v_mov_b32 v59, 0\n"
    "v_mov_b32 v60, 0\n"
    "v_mov_b32 v160, 0\n"
    "v_mov_b32 v161, 0\n"
    "v_mov_b32 v162, 0\n"
    "v_mov_b32 v163, 0\n"
    "v_cmp_eq_u32 vcc, 0, v247\n"
    "s_nop 1\n"
    "v_cndmask_b32 v166, 0, v246, vcc\n"
    "s_waitcnt vmcnt(6)\n"
    "s_barrier\n"
    "s_add_u32 m0, s85, 139264\n"
    "s_mov_b32 s67, 1880064\n"
    "buffer_load_dwordx4 v251, s[40:43], s67 offen offset:0 lds\n"
    "buffer_load_dwordx4 v216, s[40:43], s67 offen offset:1024 lds\n"
    "v_cmp_eq_u32 vcc, 1, v247\n"
    "s_nop 1\n"
    "v_cndmask_b32 v167, 0, v246, vcc\n"
    "s_add_u32 m0, s69, 147456\n"
    "s_add_u32 s67, s72, 0\n"
    "buffer_load_dwordx4 v254, s[48:51], s67 offen offset:0 lds\n"
    "s_mov_b32 vcc_lo, 0x30003\n"
    "s_mov_b32 vcc_hi, 0x30003\n"
    "v_cndmask_b32 v192, 0, v246, vcc\n"
    "s_mov_b32 vcc_lo, 0xc000c\n"
    "s_mov_b32 vcc_hi, 0xc000c\n"
    "v_cndmask_b32 v193, 0, v246, vcc\n"
    "s_add_u32 m0, s69, 151552\n"
    "s_add_u32 s67, s72, 294912\n"
    "buffer_load_dwordx4 v254, s[48:51], s67 offen offset:0 lds\n"
    "s_mov_b32 vcc_lo, 0x300030\n"
    "s_mov_b32 vcc_hi, 0x300030\n"
    "v_cndmask_b32 v194, 0, v246, vcc\n"
    "s_add_u32 m0, s69, 155648\n"
    "s_add_u32 s67, s72, 589824\n"
    "buffer_load_dwordx4 v254, s[48:51], s67 offen offset:0 lds\n"
    "s_mov_b32 vcc_lo, 0xc000c0\n"
    "s_mov_b32 vcc_hi, 0xc000c0\n"
    "v_cndmask_b32 v195, 0, v246, vcc\n"
    "v_mov_b32 v247, 0xf149f2ca\n"
    "v_cndmask_b32 v80, v247, 0, s[88:89]\n"
    "v_cndmask_b32 v81, v247, 0, s[90:91]\n"
    "v_cndmask_b32 v82, v247, 0, s[92:93]\n"
    "v_cndmask_b32 v83, v247, 0, s[94:95]\n"
    "s_add_u32 m0, s69, 0\n"
    "s_add_u32 s67, s72, 884736\n"
    "buffer_load_dwordx4 v254, s[48:51], s67 offen offset:0 lds\n"
    "s_and_b32 s30, s65, 3\n"
    "s_lshl_b32 s30, s30, 7\n"
    "v_lshl_add_u32 v235, v249, 3, s30\n"
    "v_mov_b32 v247, 0x40000000\n"
    "s_mov_b64 vcc, 0xffff\n"
    "v_cndmask_b32 v235, v247, v235, vcc\n"
    "s_lshl_b32 s30, s64, 2\n"
    "s_lshr_b32 s33, s65, 2\n"
    "s_add_u32 s30, s30, s33\n"
    "s_mul_i32 s30, s30, 36864\n"
    "s_and_b32 s31, s65, 3\n"
    "s_mul_i32 s31, s31, 256\n"
    "s_add_u32 s30, s30, s31\n"
    "s_lshl_b32 s31, s66, 5\n"
    "s_add_u32 s30, s30, s31\n"
    "v_and_b32 v247, 31, v255\n"
    "v_mul_u32_u24 v234, 1152, v247\n"
    "v_lshrrev_b32 v246, 5, v255\n"
    "v_lshlrev_b32 v246, 4, v246\n"
    "v_add3_u32 v234, v234, v246, s30\n"
    "v_add_co_u32 v244, vcc, 1, v244\n"
    "s_nop 1\n"
    "v_addc_co_u32 v245, vcc, 0, v245, vcc\n"
    "v_readlane_b32 s98, v244, s65\n"
    "v_readlane_b32 s99, v245, s65\n"
    "s_waitcnt vmcnt(10)\n"
    "s_barrier\n"
    "s_add_u32 m0, s69, 4096\n"
    "s_add_u32 s67, s72, 1179648\n"
    "buffer_load_dwordx4 v254, s[48:51], s67 offen offset:0 lds\n"
    "s_add_u32 m0, s69, 8192\n"
    "s_add_u32 s67, s72, 1474560\n"
    "buffer_load_dwordx4 v254, s[48:51], s67 offen offset:0 lds\n"
    "s_and_b32 s31, s65, 3\n"
    "s_mul_i32 s31, s31, 224\n"
    "s_sub_u32 s30, 1024, s31\n"
    "s_mul_i32 s31, s66, 24\n"
    "s_sub_u32 s30, s30, s31\n"
    "v_add_u32 v233, s30, v234\n"
    "v_mov_b32 v246, 0x40000000\n"
    "s_mov_b32 vcc_lo, -1\n"
    "s_mov_b32 vcc_hi, 0\n"
    "v_cndmask_b32 v233, v246, v233, vcc\n"
    "s_add_u32 m0, s69, 12288\n"
    "s_add_u32 s67, s72, 1769472\n"
    "buffer_load_dwordx4 v254, s[48:51], s67 offen offset:0 lds\n"
    "s_lshl_b32 s30, s66, 8\n"
    "v_lshlrev_b32 v232, 10, v248\n"
    "v_lshl_add_u32 v232, v249, 4, v232\n"
    "v_add_u32 v232, s30, v232\n"
    "s_add_u32 m0, s69, 16384\n"
    "s_add_u32 s67, s72, 2064384\n"
    "buffer_load_dwordx4 v254, s[48:51], s67 offen offset:0 lds\n"
    "s_lshl_b32 s30, s64, 4\n"
    "s_add_u32 s30, s30, s65\n"
    "s_lshl_b32 s30, s30, 10\n"
    "s_add_u32 s30, s30, 0xc0000\n"
    "s_lshr_b32 s31, s65, 2\n"
    "s_lshl_b32 s31, s31, 8\n"
    "s_add_u32 s31, s31, s30\n"
    "s_lshl_b32 s33, s66, 8\n"
    "s_add_u32 s30, s30, s33\n"
    "v_lshl_add_u32 v206, v249, 4, s30\n"
    "v_lshl_add_u32 v217, v249, 4, s31\n"
    "v_bfe_u32 v246, v255, 4, 1\n"
    "v_lshl_add_u32 v217, v246, 2, v217\n"
    "s_add_u32 m0, s69, 20480\n"
    "s_add_u32 s67, s72, 2359296\n"
    "buffer_load_dwordx4 v254, s[48:51], s67 offen offset:0 lds\n"
    "s_add_u32 s30, s70, 107520\n"
    "v_lshrrev_b32 v247, 1, v255\n"
    "v_and_b32 v247, 8, v247\n"
    "v_xor_b32 v247, v247, v255\n"
    "v_lshlrev_b32 v205, 4, v247\n"
    "v_add_u32 v200, s30, v205\n"
    "v_add_u32 v201, 8, v200\n"
    "s_cmp_lg_u32 s71, 2\n"
    "s_cbranch_scc1 L_q7_%=\n"
    "s_mov_b32 m0, 162816\n"
    "s_mov_b32 exec_lo, 0xffffffff\n"
    "s_mov_b32 exec_hi, 0xf\n"
    "s_mov_b32 s67, 384\n"
    "buffer_load_dword v210, s[48:51], s67 offen lds\n"
    "s_mov_b64 exec, -1\n"
    "L_q7_%=:\n"
    "s_cmp_lg_u32 s71, 3\n"
    "s_cbranch_scc1 L_q8_%=\n"
    "s_mov_b32 m0, 162816\n"
    "s_mov_b32 exec_lo, 0xffffffff\n"
    "s_mov_b32 exec_hi, 0xf\n"
    "s_mov_b32 s67, 512\n"
    "buffer_load_dword v210, s[48:51], s67 offen lds\n"
    "s_mov_b64 exec, -1\n"
    "L_q8_%=:\n"
    "s_waitcnt vmcnt(11)\n"
    "s_barrier\n"
    "s_add_u32 m0, s69, 24576\n"
    "s_add_u32 s67, s72, 2654208\n"
    "buffer_load_dwordx4 v254, s[48:51], s67 offen offset:0 lds\n"
    "s_add_u32 m0, s69, 28672\n"
    "s_add_u32 s67, s72, 2949120\n"
    "buffer_load_dwordx4 v254, s[48:51], s67 offen offset:0 lds\n"
    "s_add_u32 m0, s69, 32768\n"
    "s_add_u32 s67, s72, 3244032\n"
    "buffer_load_dwordx4 v254, s[48:51], s67 offen offset:0 lds\n"
    "s_add_u32 m0, s69, 36864\n"
    "s_add_u32 s67, s72, 3538944\n"
    "buffer_load_dwordx4 v254, s[48:51], s67 offen offset:0 lds\n"
    "s_add_u32 m0, s69, 40960\n"
    "s_add_u32 s67, s72, 3833856\n"
    "buffer_load_dwordx4 v254, s[48:51], s67 offen offset:0 lds\n"
    "s_add_u32 s30, s70, 109824\n"
    "v_lshrrev_b32 v247, 2, v255\n"
    "v_and_b32 v246, 1, v255\n"
    "v_lshlrev_b32 v246, 3, v246\n"
    "v_xor_b32 v246, v246, v247\n"
    "v_and_b32 v248, 3, v255\n"
    "v_lshl_add_u32 v246, v248, 4, v246\n"
    "v_lshl_add_u32 v202, v246, 4, s30\n"
    "s_add_u32 s30, s30, 38912\n"
    "v_lshl_add_u32 v203, v247, 4, s30\n"
    "v_mov_b32 v246, 0x27e00\n"
    "s_mov_b32 vcc_lo, 0xaaaaaaaa\n"
    "s_mov_b32 vcc_hi, 0xaaaaaaaa\n"
    "v_cndmask_b32 v203, v203, v246, vcc\n"
    "v_mov_b32 v204, 4608\n"
    "v_cndmask_b32 v204, v204, 0, vcc\n"
    "s_waitcnt vmcnt(11)\n"
    "s_barrier\n"
    "s_add_u32 m0, s69, 45056\n"
    "s_add_u32 s67, s72, 4128768\n"
    "buffer_load_dwordx4 v254, s[48:51], s67 offen offset:0 lds\n"
    "s_add_u32 m0, s69, 49152\n"
    "s_add_u32 s67, s72, 4423680\n"
    "buffer_load_dwordx4 v254, s[48:51], s67 offen offset:0 lds\n"
    "s_add_u32 m0, s69, 53248\n"
    "s_add_u32 s67, s72, 4718592\n"
    "buffer_load_dwordx4 v254, s[48:51], s67 offen offset:0 lds\n"
    "s_add_u32 m0, s69, 57344\n"
    "s_add_u32 s67, s72, 5013504\n"
    "buffer_load_dwordx4 v254, s[48:51], s67 offen offset:0 lds\n"
    "s_add_u32 m0, s69, 61440\n"
    "s_add_u32 s67, s73, 0\n"
    "buffer_load_dwordx4 v254, s[48:51], s67 offen offset:0 lds\n"
    "s_add_u32 m0, s69, 65536\n"
    "s_add_u32 s67, s73, 294912\n"
    "buffer_load_dwordx4 v254, s[48:51], s67 offen offset:0 lds\n"
    "s_lshl_b32 s26, s66, 3\n"
    "s_lshr_b32 s26, 0x07060c05, s26\n"
    "s_and_b32 s26, s26, 0xff\n"
    "s_lshl_b32 s28, s26, 12\n"
    "s_getpc_b64 s[26:27]\n"
    "L_hp2_%=:\n"
    "s_sub_u32 s26, s26, L_hp2_%=-L_st1_%=\n"
    "s_subb_u32 s27, s27, 0\n"
    "s_add_u32 s26, s26, s28\n"
    "s_addc_u32 s27, s27, 0\n"
    "s_mov_b64 exec, 0\n"
    "s_swappc_b64 s[24:25], s[26:27]\n"
    "s_mov_b64 exec, -1\n"
    "s_waitcnt vmcnt(11)\n"
    "s_barrier\n"
    "s_add_u32 m0, s69, 69632\n"
    "s_add_u32 s67, s73, 589824\n"
    "buffer_load_dwordx4 v254, s[48:51], s67 offen offset:0 lds\n"
    "s_add_u32 m0, s69, 73728\n"
    "s_add_u32 s67, s73, 884736\n"
    "buffer_load_dwordx4 v254, s[48:51], s67 offen offset:0 lds\n"
    "s_add_u32 m0, s69, 77824\n"
    "s_add_u32 s67, s73, 1179648\n"
    "buffer_load_dwordx4 v254, s[48:51], s67 offen offset:0 lds\n"
    "s_add_u32 m0, s69, 81920\n"
    "s_add_u32 s67, s73, 1474560\n"
    "buffer_load_dwordx4 v254, s[48:51], s67 offen offset:0 lds\n"
    "s_waitcnt vmcnt(11)\n"
    "s_barrier\n"
    "s_waitcnt vmcnt(6)\n"
    "s_barrier\n"
    "s_add_u32 m0, s69, 86016\n"
    "s_add_u32 s67, s73, 1769472\n"
    "buffer_load_dwordx4 v254, s[48:51], s67 offen offset:0 lds\n"
    "s_add_u32 m0, s69, 90112\n"
    "s_add_u32 s67, s73, 2064384\n"
    "buffer_load_dwordx4 v254, s[48:51], s67 offen offset:0 lds\n"
    "s_add_u32 m0, s69, 94208\n"
    "s_add_u32 s67, s73, 2359296\n"
    "buffer_load_dwordx4 v254, s[48:51], s67 offen offset:0 lds\n"
    "s_waitcnt vmcnt(6)\n"
    "s_barrier\n"
    "s_add_u32 m0, s69, 98304\n"
    "s_add_u32 s67, s73, 2654208\n"
    "buffer_load_dwordx4 v254, s[48:51], s67 offen offset:0 lds\n"
    "s_add_u32 m0, s69, 102400\n"
    "s_add_u32 s67, s73, 2949120\n"
    "buffer_load_dwordx4 v254, s[48:51], s67 offen offset:0 lds\n"
    "s_add_u32 m0, s69, 106496\n"
    "s_add_u32 s67, s73, 3244032\n"
    "buffer_load_dwordx4 v254, s[48:51], s67 offen offset:0 lds\n"
    "s_add_u32 m0, s69, 110592\n"
    "s_add_u32 s67, s73, 3538944\n"
    "buffer_load_dwordx4 v254, s[48:51], s67 offen offset:0 lds\n"
    "s_add_u32 m0, s69, 114688\n"
    "s_add_u32 s67, s73, 3833856\n"
    "buffer_load_dwordx4 v254, s[48:51], s67 offen offset:0 lds\n"
    "s_add_u32 m0, s69, 118784\n"
    "s_add_u32 s67, s73, 4128768\n"
    "buffer_load_dwordx4 v254, s[48:51], s67 offen offset:0 lds\n"
    "s_add_u32 m0, s69, 122880\n"
    "s_add_u32 s67, s73, 4423680\n"
    "buffer_load_dwordx4 v254, s[48:51], s67 offen offset:0 lds\n"
    "s_lshl_b32 s26, s66, 3\n"
    "s_lshr_b32 s26, 0x0d0d0d08, s26\n"
    "s_and_b32 s26, s26, 0xff\n"
    "s_lshl_b32 s28, s26, 12\n"
    "s_getpc_b64 s[26:27]\n"
    "L_hp3_%=:\n"
    "s_sub_u32 s26, s26, L_hp3_%=-L_st1_%=\n"
    "s_subb_u32 s27, s27, 0\n"
    "s_add_u32 s26, s26, s28\n"
    "s_addc_u32 s27, s27, 0\n"
    "s_mov_b64 exec, 0\n"
    "s_swappc_b64 s[24:25], s[26:27]\n"
    "s_mov_b64 exec, -1\n"
    "s_waitcnt vmcnt(7)\n"
    "s_barrier\n"
    "s_cmp_lg_u32 s66, 0\n"
    "s_cbranch_scc1 L_q0_%=\n"
    "s_mov_b64 exec, 0xffffff\n"
    "buffer_store_dwordx2 v[244:245], v242, s[60:63], 0 offen offset:128 sc1\n"
    "s_mov_b64 exec, -1\n"
    "L_q0_%=:\n"
    "s_add_u32 m0, s69, 126976\n"
    "s_add_u32 s67, s73, 4718592\n"
    "buffer_load_dwordx4 v254, s[48:51], s67 offen offset:0 lds\n"
    "s_add_u32 m0, s69, 131072\n"
    "s_add_u32 s67, s73, 5013504\n"
    "buffer_load_dwordx4 v254, s[48:51], s67 offen offset:0 lds\n"
    "s_add_u32 m0, s69, 0\n"
    "s_add_u32 s67, s74, 0\n"
    "buffer_load_dwordx4 v254, s[48:51], s67 offen offset:0 lds\n"
    "s_add_u32 m0, s69, 4096\n"
    "s_add_u32 s67, s74, 294912\n"
    "buffer_load_dwordx4 v254, s[48:51], s67 offen offset:0 lds\n"
    "s_add_u32 m0, s69, 8192\n"
    "s_add_u32 s67, s74, 589824\n"
    "buffer_load_dwordx4 v254, s[48:51], s67 offen offset:0 lds\n"
    "s_add_u32 m0, s69, 12288\n"
    "s_add_u32 s67, s74, 884736\n"
    "buffer_load_dwordx4 v254, s[48:51], s67 offen offset:0 lds\n"
    "s_add_u32 m0, s69, 16384\n"
    "s_add_u32 s67, s74, 1179648\n"
    "buffer_load_dwordx4 v254, s[48:51], s67 offen offset:0 lds\n"
    "s_add_u32 m0, s69, 20480\n"
    "s_add_u32 s67, s74, 1474560\n"
    "buffer_load_dwordx4 v254, s[48:51], s67 offen offset:0 lds\n"
    "s_waitcnt vmcnt(11)\n"
    "s_barrier\n"
    "s_add_u32 m0, s69, 24576\n"
    "s_add_u32 s67, s74, 1769472\n"
    "buffer_load_dwordx4 v254, s[48:51], s67 offen offset:0 lds\n"
    "s_add_u32 m0, s69, 28672\n"
    "s_add_u32 s67, s74, 2064384\n"
    "buffer_load_dwordx4 v254, s[48:51], s67 offen offset:0 lds\n"
    "s_waitcnt vmcnt(8)\n"
    "s_barrier\n"
    "s_mov_b64 exec, 0xfff\n"
    "buffer_load_dwordx2 v[238:239], v240, s[60:63], 0 offen offset:128 sc1\n"
    "s_mov_b64 exec, -1\n"
    "s_add_u32 m0, s69, 32768\n"
    "s_add_u32 s67, s74, 2359296\n"
    "buffer_load_dwordx4 v254, s[48:51], s67 offen offset:0 lds\n"
    "s_add_u32 m0, s69, 36864\n"
    "s_add_u32 s67, s74, 2654208\n"
    "buffer_load_dwordx4 v254, s[48:51], s67 offen offset:0 lds\n"
    "s_add_u32 m0, s69, 40960\n"
    "s_add_u32 s67, s74, 2949120\n"
    "buffer_load_dwordx4 v254, s[48:51], s67 offen offset:0 lds\n"
    "s_waitcnt vmcnt(9)\n"
    "L_st11_%=:\n"
    "s_cbranch_execz L_bk%=\n"
    "s_barrier\n"
    "s_waitcnt vmcnt(3)\n"
    "v_cmp_ne_u64 vcc, s[98:99], v[238:239]\n"
    "s_nop 1\n"
    "s_and_b64 vcc, vcc, s[96:97]\n"
    "s_cbranch_vccnz L_sl0_%=\n"
    "L_rt0_%=:\n"
    "buffer_load_dwordx4 v[96:99], v205, s[60:63], s68 offen offset:0 sc1\n"
    "buffer_load_dwordx4 v[100:103], v205, s[60:63], s68 offen offset:1024 sc1\n"
    "s_mov_b64 exec, 0xffff\n"
    "buffer_load_dwordx4 v[160:163], v250, s[60:63], s68 offen offset:2048 sc1\n"
    "s_mov_b64 exec, -1\n"
    "buffer_load_dwordx2 v[218:219], v206, s[60:63], 0 offen sc1\n"
    "buffer_load_dword v217, v217, s[60:63], 0 offen sc1\n"
    "s_add_u32 m0, s70, 144384\n"
    "s_sub_u32 s67, s81, 0\n"
    "buffer_load_dwordx4 v250, s[60:63], s67 offen offset:0 sc1 lds\n"
    "buffer_load_dwordx4 v250, s[60:63], s67 offen offset:1024 sc1 lds\n"
    "buffer_load_dwordx4 v250, s[60:63], s67 offen offset:2048 sc1 lds\n"
    "buffer_load_dwordx4 v250, s[60:63], s67 offen offset:3072 sc1 lds\n"
    "s_add_u32 m0, m0, 4096\n"
    "s_add_u32 s67, s67, 4096\n"
    "s_mov_b32 exec_hi, 0\n"
    "buffer_load_dwordx4 v250, s[60:63], s67 offen offset:0 sc1 lds\n"
    "s_mov_b32 exec_hi, -1\n"
    "s_add_u32 m0, s69, 45056\n"
    "s_add_u32 s67, s74, 3244032\n"
    "buffer_load_dwordx4 v254, s[48:51], s67 offen offset:0 lds\n"
    "s_add_u32 m0, s69, 49152\n"
    "s_add_u32 s67, s74, 3538944\n"
    "buffer_load_dwordx4 v254, s[48:51], s67 offen offset:0 lds\n"
    "s_add_u32 m0, s69, 53248\n"
    "s_add_u32 s67, s74, 3833856\n"
    "buffer_load_dwordx4 v254, s[48:51], s67 offen offset:0 lds\n"
    "s_waitcnt vmcnt(15)\n"
    "s_barrier\n"
    "s_add_u32 m0, s70, 125952\n"
    "s_sub_u32 s67, s81, 18432\n"
    "s_cmp_ge_u32 s64, 1\n"
    "s_cselect_b32 s67, s67, 0x40000000\n"
    "buffer_load_dwordx4 v250, s[60:63], s67 offen offset:0 sc1 lds\n"
    "buffer_load_dwordx4 v250, s[60:63], s67 offen offset:1024 sc1 lds\n"
    "buffer_load_dwordx4 v250, s[60:63], s67 offen offset:2048 sc1 lds\n"
    "buffer_load_dwordx4 v250, s[60:63], s67 offen offset:3072 sc1 lds\n"
    "s_add_u32 m0, m0, 4096\n"
    "s_add_u32 s67, s67, 4096\n"
    "s_mov_b32 exec_hi, 0\n"
    "buffer_load_dwordx4 v250, s[60:63], s67 offen offset:0 sc1 lds\n"
    "s_mov_b32 exec_hi, -1\n"
    "s_add_u32 m0, s69, 57344\n"
    "s_add_u32 s67, s74, 4128768\n"
    "buffer_load_dwordx4 v254, s[48:51], s67 offen offset:0 lds\n"
    "s_add_u32 m0, s69, 61440\n"
    "s_add_u32 s67, s74, 4423680\n"
    "buffer_load_dwordx4 v254, s[48:51], s67 offen offset:0 lds\n"
    "s_add_u32 m0, s69, 65536\n"
    "s_add_u32 s67, s74, 4718592\n"
    "buffer_load_dwordx4 v254, s[48:51], s67 offen offset:0 lds\n"
    "s_add_u32 m0, s69, 69632\n"
    "s_add_u32 s67, s74, 5013504\n"
    "buffer_load_dwordx4 v254, s[48:51], s67 offen offset:0 lds\n"
    "s_waitcnt vmcnt(10)\n"
    "s_barrier\n"
    "s_add_u32 m0, s70, 107520\n"
    "s_sub_u32 s67, s81, 36864\n"
    "s_cmp_ge_u32 s64, 2\n"
    "s_cselect_b32 s67, s67, 0x40000000\n"
    "buffer_load_dwordx4 v250, s[60:63], s67 offen offset:0 sc1 lds\n"
    "buffer_load_dwordx4 v250, s[60:63], s67 offen offset:1024 sc1 lds\n"
    "buffer_load_dwordx4 v250, s[60:63], s67 offen offset:2048 sc1 lds\n"
    "buffer_load_dwordx4 v250, s[60:63], s67 offen offset:3072 sc1 lds\n"
    "s_add_u32 m0, m0, 4096\n"
    "s_add_u32 s67, s67, 4096\n"
    "s_mov_b32 exec_hi, 0\n"
    "buffer_load_dwordx4 v250, s[60:63], s67 offen offset:0 sc1 lds\n"
    "s_mov_b32 exec_hi, -1\n"
    "s_setprio 2\n"
    "ds_read2st64_b64 v[0:3], v200 offset0:72 offset1:76\n"
    "ds_read_b64 v[4:5], v200 offset:37888\n"
    "ds_read2st64_b64 v[6:9], v201 offset0:72 offset1:76\n"
    "ds_read_b64 v[10:11], v200 offset:37896\n"
    "ds_read_b64_tr_b16 v[24:25], v202 offset:36864\n"
    "ds_read_b64_tr_b16 v[26:27], v202 offset:36872\n"
    "ds_read_b64_tr_b16 v[28:29], v202 offset:37888\n"
    "ds_read_b64_tr_b16 v[30:31], v202 offset:37896\n"
    "ds_read_b64_tr_b16 v[32:33], v203 offset:0\n"
    "ds_read_b64_tr_b16 v[34:35], v203 offset:8\n"
    "s_setprio 0\n"
    "v_mov_b32 v48, v96\n"
    "v_mov_b32 v49, v97\n"
    "v_mov_b32 v50, v160\n"
    "v_mov_b32 v53, v161\n"
    "v_mov_b32 v54, v100\n"
    "v_mov_b32 v55, v101\n"
    "v_mov_b32 v56, v98\n"
    "v_mov_b32 v57, v99\n"
    "v_mov_b32 v58, v162\n"
    "v_mov_b32 v61, v163\n"
    "v_mov_b32 v62, v102\n"
    "v_mov_b32 v63, v103\n"
    "v_mul_f32 v217, 0x3fdb80d1, v217\n"
    "v_rcp_f32 v218, v218\n"
    "v_rcp_f32 v219, v219\n"
    "s_setprio 2\n"
    "s_waitcnt lgkmcnt(9)\n"
    "v_mfma_f32_16x16x32_bf16 v[120:123], v[0:3], v[48:51], v[80:83]\n"
    "s_waitcnt lgkmcnt(7)\n"
    "v_mfma_f32_16x16x32_bf16 v[128:131], v[6:9], v[56:59], v[80:83]\n"
    "v_mfma_f32_16x16x32_bf16 v[132:135], v[0:3], v[56:59], 0\n"
    "v_mfma_f32_16x16x32_bf16 v[136:139], v[2:5], v[52:55], v[80:83]\n"
    "s_waitcnt lgkmcnt(6)\n"
    "v_mfma_f32_16x16x32_bf16 v[144:147], v[8:11], v[60:63], v[80:83]\n"
    "v_mfma_f32_16x16x32_bf16 v[148:151], v[2:5], v[60:63], 0\n"
    "s_setprio 0\n"
    "s_add_u32 m0, s69, 73728\n"
    "s_add_u32 s67, s75, 0\n"
    "buffer_load_dwordx4 v254, s[48:51], s67 offen offset:0 lds\n"
    "v_max3_f32 v152, v120, v121, v122\n"
    "v_max3_f32 v153, v128, v129, v130\n"
    "v_max3_f32 v154, v136, v137, v138\n"
    "v_max3_f32 v155, v144, v145, v146\n"
    "v_max_f32 v152, v152, v123\n"
    "v_max3_f32 v156, v131, v132, v133\n"
    "v_max3_f32 v153, v153, v134, v135\n"
    "v_max_f32 v153, v153, v156\n"
    "v_max_f32 v154, v154, v139\n"
    "v_max3_f32 v156, v147, v148, v149\n"
    "v_max3_f32 v155, v155, v150, v151\n"
    "v_max_f32 v155, v155, v156\n"
    "v_mov_b32 v156, v152\n"
    "v_mov_b32 v157, v153\n"
    "v_mov_b32 v158, v154\n"
    "v_mov_b32 v159, v155\n"
    "s_add_u32 m0, s69, 77824\n"
    "s_add_u32 s67, s75, 294912\n"
    "buffer_load_dwordx4 v254, s[48:51], s67 offen offset:0 lds\n"
    "v_permlane32_swap_b32 v152, v156\n"
    "v_permlane32_swap_b32 v153, v157\n"
    "v_permlane32_swap_b32 v154, v158\n"
    "v_permlane32_swap_b32 v155, v159\n"
    "v_max_f32 v152, v152, v156\n"
    "v_max_f32 v153, v153, v157\n"
    "v_max_f32 v154, v154, v158\n"
    "v_max_f32 v155, v155, v159\n"
    "v_mov_b32 v156, v152\n"
    "v_mov_b32 v157, v153\n"
    "v_mov_b32 v158, v154\n"
    "v_mov_b32 v159, v155\n"
    "v_permlane16_swap_b32 v152, v156\n"
    "v_permlane16_swap_b32 v153, v157\n"
    "v_permlane16_swap_b32 v154, v158\n"
    "v_permlane16_swap_b32 v155, v159\n"
    "v_max_f32 v152, v152, v156\n"
    "v_max_f32 v153, v153, v157\n"
    "v_max_f32 v154, v154, v158\n"
    "v_max_f32 v155, v155, v159\n"
    "s_add_u32 m0, s69, 81920\n"
    "s_add_u32 s67, s75, 589824\n"
    "buffer_load_dwordx4 v254, s[48:51], s67 offen offset:0 lds\n"
    "v_sub_f32 v64, 0, v152\n"
    "v_sub_f32 v65, 0, v152\n"
    "v_sub_f32 v66, 0, v152\n"
    "v_sub_f32 v67, 0, v152\n"
    "v_sub_f32 v68, 0, v153\n"
    "v_sub_f32 v69, 0, v153\n"
    "v_sub_f32 v70, 0, v153\n"
    "v_sub_f32 v71, 0, v153\n"
    "v_sub_f32 v72, 0, v154\n"
    "v_sub_f32 v73, 0, v154\n"
    "v_sub_f32 v74, 0, v154\n"
    "v_sub_f32 v75, 0, v154\n"
    "v_sub_f32 v76, 0, v155\n"
    "v_sub_f32 v77, 0, v155\n"
    "v_sub_f32 v78, 0, v155\n"
    "v_sub_f32 v79, 0, v155\n"
    "v_mov_b32 v247, 0xf149f2ca\n"
    "v_cndmask_b32 v80, v247, v64, s[88:89]\n"
    "v_cndmask_b32 v81, v247, v64, s[90:91]\n"
    "v_cndmask_b32 v82, v247, v64, s[92:93]\n"
    "v_cndmask_b32 v83, v247, v64, s[94:95]\n"
    "s_waitcnt vmcnt(8)\n"
    "s_barrier\n"
    "s_add_u32 m0, s69, 86016\n"
    "s_add_u32 s67, s75, 884736\n"
    "buffer_load_dwordx4 v254, s[48:51], s67 offen offset:0 lds\n"
    "v_cndmask_b32 v84, v247, v68, s[88:89]\n"
    "v_cndmask_b32 v85, v247, v68, s[90:91]\n"
    "v_cndmask_b32 v86, v247, v68, s[92:93]\n"
    "v_cndmask_b32 v87, v247, v68, s[94:95]\n"
    "v_cndmask_b32 v88, v247, v72, s[88:89]\n"
    "v_cndmask_b32 v89, v247, v72, s[90:91]\n"
    "v_cndmask_b32 v90, v247, v72, s[92:93]\n"
    "v_cndmask_b32 v91, v247, v72, s[94:95]\n"
    "v_cndmask_b32 v92, v247, v76, s[88:89]\n"
    "v_cndmask_b32 v93, v247, v76, s[90:91]\n"
    "v_cndmask_b32 v94, v247, v76, s[92:93]\n"
    "v_cndmask_b32 v95, v247, v76, s[94:95]\n"
    "s_setprio 2\n"
    "v_mfma_f32_16x16x32_bf16 v[96:99], v[0:3], v[48:51], v[80:83]\n"
    "s_setprio 0\n"
    "s_add_u32 m0, s69, 90112\n"
    "s_add_u32 s67, s75, 1179648\n"
    "buffer_load_dwordx4 v254, s[48:51], s67 offen offset:0 lds\n"
    "s_setprio 2\n"
    "v_mfma_f32_16x16x32_bf16 v[104:107], v[0:3], v[56:59], v[68:71]\n"
    "ds_read2st64_b64 v[12:15], v200 offset0:63 offset1:67\n"
    "s_nop 0\n"
    "v_exp_f32 v96, v96\n"
    "v_mfma_f32_16x16x32_bf16 v[108:111], v[6:9], v[56:59], v[84:87]\n"
    "ds_read_b64 v[16:17], v200 offset:33280\n"
    "ds_read2st64_b64 v[18:21], v201 offset0:63 offset1:67\n"
    "s_setprio 0\n"
    "v_exp_f32 v97, v97\n"
    "v_exp_f32 v98, v98\n"
    "v_exp_f32 v99, v99\n"
    "v_cvt_pk_bf16_f32 v112, v96, v97\n"
    "v_cvt_pk_bf16_f32 v113, v98, v99\n"
    "v_mov_b32 v114, 0\n"
    "v_mov_b32 v115, 0\n"
    "s_setprio 2\n"
    "v_mfma_f32_16x16x32_bf16 v[96:99], v[2:5], v[52:55], v[88:91]\n"
    "ds_read_b64 v[22:23], v200 offset:33288\n"
    "v_exp_f32 v104, v104\n"
    "s_waitcnt lgkmcnt(8)\n"
    "v_mfma_f32_16x16x32_bf16 v[120:123], v[24:27], v[112:115], 0\n"
    "ds_read_b64_tr_b16 v[36:37], v202 offset:32256\n"
    "v_exp_f32 v105, v105\n"
    "s_waitcnt lgkmcnt(5)\n"
    "v_mfma_f32_16x16x32_bf16 v[136:139], v[32:35], v[112:115], 0\n"
    "ds_read_b64_tr_b16 v[38:39], v202 offset:32264\n"
    "s_setprio 0\n"
    "s_add_u32 m0, s69, 94208\n"
    "s_add_u32 s67, s75, 1474560\n"
    "buffer_load_dwordx4 v254, s[48:51], s67 offen offset:0 lds\n"
    "v_exp_f32 v106, v106\n"
    "v_exp_f32 v107, v107\n"
    "v_exp_f32 v108, v108\n"
    "v_exp_f32 v109, v109\n"
    "v_exp_f32 v110, v110\n"
    "v_exp_f32 v111, v111\n"
    "v_cvt_pk_bf16_f32 v116, v104, v105\n"
    "v_cvt_pk_bf16_f32 v117, v106, v107\n"
    "v_cvt_pk_bf16_f32 v118, v108, v109\n"
    "v_cvt_pk_bf16_f32 v119, v110, v111\n"
    "s_setprio 2\n"
    "v_mfma_f32_16x16x32_bf16 v[104:107], v[2:5], v[60:63], v[76:79]\n"
    "ds_read_b64_tr_b16 v[40:41], v202 offset:33280\n"
    "v_exp_f32 v96, v96\n"
    "v_mfma_f32_16x16x32_bf16 v[108:111], v[8:11], v[60:63], v[92:95]\n"
    "ds_read_b64_tr_b16 v[42:43], v202 offset:33288\n"
    "v_exp_f32 v97, v97\n"
    "v_mfma_f32_16x16x32_bf16 v[124:127], v[24:27], v[116:119], 0\n"
    "v_exp_f32 v98, v98\n"
    "v_mfma_f32_16x16x32_bf16 v[140:143], v[32:35], v[116:119], 0\n"
    "s_setprio 0\n"
    "s_add_u32 m0, s69, 98304\n"
    "s_add_u32 s67, s75, 1769472\n"
    "buffer_load_dwordx4 v254, s[48:51], s67 offen offset:0 lds\n"
    "v_exp_f32 v99, v99\n"
    "v_cvt_pk_bf16_f32 v112, v96, v97\n"
    "v_cvt_pk_bf16_f32 v113, v98, v99\n"
    "v_mov_b32 v114, 0\n"
    "v_mov_b32 v115, 0\n"
    "s_cmp_eq_u32 s83, 0\n"
    "s_cbranch_scc1 L_k0_%=\n"
    "L_kr0_%=:\n"
    "v_sub_u32 v203, v203, v204\n"
    "s_setprio 2\n"
    "s_waitcnt lgkmcnt(7)\n"
    "v_mfma_f32_16x16x32_bf16 v[96:99], v[12:15], v[48:51], v[64:67]\n"
    "ds_read_b64_tr_b16 v[44:45], v203 offset:0\n"
    "v_exp_f32 v104, v104\n"
    "s_waitcnt lgkmcnt(6)\n"
    "v_mfma_f32_16x16x32_bf16 v[100:103], v[18:21], v[48:51], v[64:67]\n"
    "ds_read_b64_tr_b16 v[46:47], v203 offset:8\n"
    "v_exp_f32 v105, v105\n"
    "v_mfma_f32_16x16x32_bf16 v[128:131], v[28:31], v[112:115], 0\n"
    "v_exp_f32 v106, v106\n"
    "v_mfma_f32_16x16x32_bf16 v[144:147], v[32:35], v[112:115], 0\n"
    "s_setprio 0\n"
    "s_add_u32 m0, s69, 102400\n"
    "s_add_u32 s67, s75, 2064384\n"
    "buffer_load_dwordx4 v254, s[48:51], s67 offen offset:0 lds\n"
    "v_exp_f32 v107, v107\n"
    "v_exp_f32 v108, v108\n"
    "v_exp_f32 v109, v109\n"
    "v_exp_f32 v110, v110\n"
    "v_exp_f32 v111, v111\n"
    "v_cvt_pk_bf16_f32 v116, v104, v105\n"
    "v_cvt_pk_bf16_f32 v117, v106, v107\n"
    "v_cvt_pk_bf16_f32 v118, v108, v109\n"
    "v_cvt_pk_bf16_f32 v119, v110, v111\n"
    "s_setprio 2\n"
    "v_mfma_f32_16x16x32_bf16 v[104:107], v[12:15], v[56:59], v[68:71]\n"
    "ds_read2st64_b64 v[0:3], v200 offset0:54 offset1:58\n"
    "v_exp_f32 v96, v96\n"
    "v_mfma_f32_16x16x32_bf16 v[108:111], v[18:21], v[56:59], v[68:71]\n"
    "ds_read_b64 v[4:5], v200 offset:28672\n"
    "v_exp_f32 v97, v97\n"
    "v_mfma_f32_16x16x32_bf16 v[132:135], v[28:31], v[116:119], 0\n"
    "ds_read2st64_b64 v[6:9], v201 offset0:54 offset1:58\n"
    "v_exp_f32 v98, v98\n"
    "v_mfma_f32_16x16x32_bf16 v[148:151], v[32:35], v[116:119], 0\n"
    "s_setprio 0\n"
    "s_add_u32 m0, s69, 0\n"
    "s_add_u32 s67, s75, 2359296\n"
    "buffer_load_dwordx4 v254, s[48:51], s67 offen offset:0 lds\n"
    "v_exp_f32 v99, v99\n"
    "v_exp_f32 v100, v100\n"
    "v_exp_f32 v101, v101\n"
    "v_exp_f32 v102, v102\n"
    "v_exp_f32 v103, v103\n"
    "v_cvt_pk_bf16_f32 v112, v96, v97\n"
    "v_cvt_pk_bf16_f32 v113, v98, v99\n"
    "v_cvt_pk_bf16_f32 v114, v100, v101\n"
    "v_cvt_pk_bf16_f32 v115, v102, v103\n"
    "s_setprio 2\n"
    "v_mfma_f32_16x16x32_bf16 v[96:99], v[14:17], v[52:55], v[72:75]\n"
    "ds_read_b64 v[10:11], v200 offset:28680\n"
    "v_exp_f32 v104, v104\n"
    "s_waitcnt lgkmcnt(10)\n"
    "v_mfma_f32_16x16x32_bf16 v[100:103], v[20:23], v[52:55], v[72:75]\n"
    "ds_read_b64_tr_b16 v[24:25], v202 offset:27648\n"
    "v_exp_f32 v105, v105\n"
    "s_waitcnt lgkmcnt(9)\n"
    "v_mfma_f32_16x16x32_bf16 v[120:123], v[36:39], v[112:115], v[120:123]\n"
    "ds_read_b64_tr_b16 v[26:27], v202 offset:27656\n"
    "v_exp_f32 v106, v106\n"
    "s_waitcnt lgkmcnt(6)\n"
    "v_mfma_f32_16x16x32_bf16 v[136:139], v[44:47], v[112:115], v[136:139]\n"
    "s_setprio 0\n"
    "s_cmp_lg_u32 s71, 0\n"
    "s_cbranch_scc1 L_q9_%=\n"
    "s_mov_b32 m0, 162816\n"
    "s_mov_b32 exec_lo, 0xffffffff\n"
    "s_mov_b32 exec_hi, 0x1ff\n"
    "s_mov_b32 s67, 0\n"
    "buffer_load_dword v212, s[52:55], s67 offen lds\n"
    "s_mov_b32 exec_lo, 0x3f\n"
    "s_mov_b32 exec_hi, 0x0\n"
    "s_mov_b32 s67, 0\n"
    "buffer_load_dword v214, s[44:47], s67 offen lds\n"
    "s_mov_b64 exec, -1\n"
    "L_q9_%=:\n"
    "s_cmp_lg_u32 s71, 1\n"
    "s_cbranch_scc1 L_q10_%=\n"
    "s_mov_b32 m0, 162816\n"
    "s_mov_b32 exec_lo, 0xffffffff\n"
    "s_mov_b32 exec_hi, 0x1ff\n"
    "s_mov_b32 s67, 82944\n"
    "buffer_load_dword v212, s[52:55], s67 offen lds\n"
    "s_mov_b32 exec_lo, 0x3f\n"
    "s_mov_b32 exec_hi, 0x0\n"
    "s_mov_b32 s67, 41472\n"
    "buffer_load_dword v214, s[44:47], s67 offen lds\n"
    "s_mov_b64 exec, -1\n"
    "L_q10_%=:\n"
    "s_waitcnt vmcnt(6)\n"
    "s_barrier\n"
    "s_add_u32 m0, s69, 4096\n"
    "s_add_u32 s67, s75, 2654208\n"
    "buffer_load_dwordx4 v254, s[48:51], s67 offen offset:0 lds\n"
    "v_exp_f32 v107, v107\n"
    "v_exp_f32 v108, v108\n"
    "v_exp_f32 v109, v109\n"
    "v_exp_f32 v110, v110\n"
    "v_exp_f32 v111, v111\n"
    "v_cvt_pk_bf16_f32 v116, v104, v105\n"
    "v_cvt_pk_bf16_f32 v117, v106, v107\n"
    "v_cvt_pk_bf16_f32 v118, v108, v109\n"
    "v_cvt_pk_bf16_f32 v119, v110, v111\n"
    "s_add_u32 m0, s69, 8192\n"
    "s_add_u32 s67, s75, 2949120\n"
    "buffer_load_dwordx4 v254, s[48:51], s67 offen offset:0 lds\n"
    "s_setprio 2\n"
    "v_mfma_f32_16x16x32_bf16 v[104:107], v[14:17], v[60:63], v[76:79]\n"
    "ds_read_b64_tr_b16 v[28:29], v202 offset:28672\n"
    "v_exp_f32 v96, v96\n"
    "v_mfma_f32_16x16x32_bf16 v[108:111], v[20:23], v[60:63], v[76:79]\n"
    "ds_read_b64_tr_b16 v[30:31], v202 offset:28680\n"
    "v_exp_f32 v97, v97\n"
    "v_mfma_f32_16x16x32_bf16 v[124:127], v[36:39], v[116:119], v[124:127]\n"
    "v_exp_f32 v98, v98\n"
    "v_mfma_f32_16x16x32_bf16 v[140:143], v[44:47], v[116:119], v[140:143]\n"
    "s_setprio 0\n"
    "v_exp_f32 v99, v99\n"
    "v_exp_f32 v100, v100\n"
    "v_exp_f32 v101, v101\n"
    "v_exp_f32 v102, v102\n"
    "v_exp_f32 v103, v103\n"
    "v_cvt_pk_bf16_f32 v112, v96, v97\n"
    "v_cvt_pk_bf16_f32 v113, v98, v99\n"
    "v_cvt_pk_bf16_f32 v114, v100, v101\n"
    "v_cvt_pk_bf16_f32 v115, v102, v103\n"
    "s_add_u32 m0, s69, 12288\n"
    "s_add_u32 s67, s75, 3244032\n"
    "buffer_load_dwordx4 v254, s[48:51], s67 offen offset:0 lds\n"
    "s_cmp_eq_u32 s83, 1\n"
    "s_cbranch_scc1 L_k1_%=\n"
    "L_kr1_%=:\n"
    "v_sub_u32 v203, v203, v204\n"
    "s_setprio 2\n"
    "s_waitcnt lgkmcnt(7)\n"
    "v_mfma_f32_16x16x32_bf16 v[96:99], v[0:3], v[48:51], v[64:67]\n"
    "ds_read_b64_tr_b16 v[32:33], v203 offset:0\n"
    "v_exp_f32 v104, v104\n"
    "s_waitcnt lgkmcnt(6)\n"
    "v_mfma_f32_16x16x32_bf16 v[100:103], v[6:9], v[48:51], v[64:67]\n"
    "ds_read_b64_tr_b16 v[34:35], v203 offset:8\n"
    "v_exp_f32 v105, v105\n"
    "v_mfma_f32_16x16x32_bf16 v[128:131], v[40:43], v[112:115], v[128:131]\n"
    "v_exp_f32 v106, v106\n"
    "v_mfma_f32_16x16x32_bf16 v[144:147], v[44:47], v[112:115], v[144:147]\n"
    "s_setprio 0\n"
    "v_exp_f32 v107, v107\n"
    "v_exp_f32 v108, v108\n"
    "v_exp_f32 v109, v109\n"
    "v_exp_f32 v110, v110\n"
    "v_exp_f32 v111, v111\n"
    "s_add_u32 m0, s69, 16384\n"
    "s_add_u32 s67, s75, 3538944\n"
    "buffer_load_dwordx4 v254, s[48:51], s67 offen offset:0 lds\n"
    "v_cvt_pk_bf16_f32 v116, v104, v105\n"
    "v_cvt_pk_bf16_f32 v117, v106, v107\n"
    "v_cvt_pk_bf16_f32 v118, v108, v109\n"
    "v_cvt_pk_bf16_f32 v119, v110, v111\n"
    "s_setprio 2\n"
    "v_mfma_f32_16x16x32_bf16 v[104:107], v[0:3], v[56:59], v[68:71]\n"
    "ds_read2st64_b64 v[12:15], v200 offset0:45 offset1:49\n"
    "v_exp_f32 v96, v96\n"
    "v_mfma_f32_16x16x32_bf16 v[108:111], v[6:9], v[56:59], v[68:71]\n"
    "ds_read_b64 v[16:17], v200 offset:24064\n"
    "v_exp_f32 v97, v97\n"
    "v_mfma_f32_16x16x32_bf16 v[132:135], v[40:43], v[116:119], v[132:135]\n"
    "ds_read2st64_b64 v[18:21], v201 offset0:45 offset1:49\n"
    "v_exp_f32 v98, v98\n"
    "v_mfma_f32_16x16x32_bf16 v[148:151], v[44:47], v[116:119], v[148:151]\n"
    "s_setprio 0\n"
    "v_exp_f32 v99, v99\n"
    "v_exp_f32 v100, v100\n"
    "v_exp_f32 v101, v101\n"
    "v_exp_f32 v102, v102\n"
    "s_add_u32 m0, s69, 20480\n"
    "s_add_u32 s67, s75, 3833856\n"
    "buffer_load_dwordx4 v254, s[48:51], s67 offen offset:0 lds\n"
    "v_exp_f32 v103, v103\n"
    "v_cvt_pk_bf16_f32 v112, v96, v97\n"
    "v_cvt_pk_bf16_f32 v113, v98, v99\n"
    "v_cvt_pk_bf16_f32 v114, v100, v101\n"
    "v_cvt_pk_bf16_f32 v115, v102, v103\n"
    "s_setprio 2\n"
    "v_mfma_f32_16x16x32_bf16 v[96:99], v[2:5], v[52:55], v[72:75]\n"
    "ds_read_b64 v[22:23], v200 offset:24072\n"
    "v_exp_f32 v104, v104\n"
    "s_waitcnt lgkmcnt(10)\n"
    "v_mfma_f32_16x16x32_bf16 v[100:103], v[8:11], v[52:55], v[72:75]\n"
    "ds_read_b64_tr_b16 v[36:37], v202 offset:23040\n"
    "v_exp_f32 v105, v105\n"
    "s_waitcnt lgkmcnt(9)\n"
    "v_mfma_f32_16x16x32_bf16 v[120:123], v[24:27], v[112:115], v[120:123]\n"
    "ds_read_b64_tr_b16 v[38:39], v202 offset:23048\n"
    "v_exp_f32 v106, v106\n"
    "s_waitcnt lgkmcnt(6)\n"
    "v_mfma_f32_16x16x32_bf16 v[136:139], v[32:35], v[112:115], v[136:139]\n"
    "s_setprio 0\n"
    "v_exp_f32 v107, v107\n"
    "v_exp_f32 v108, v108\n"
    "v_exp_f32 v109, v109\n"
    "v_exp_f32 v110, v110\n"
    "s_add_u32 m0, s69, 24576\n"
    "s_add_u32 s67, s75, 4128768\n"
    "buffer_load_dwordx4 v254, s[48:51], s67 offen offset:0 lds\n"
    "v_exp_f32 v111, v111\n"
    "v_cvt_pk_bf16_f32 v116, v104, v105\n"
    "v_cvt_pk_bf16_f32 v117, v106, v107\n"
    "v_cvt_pk_bf16_f32 v118, v108, v109\n"
    "v_cvt_pk_bf16_f32 v119, v110, v111\n"
    "s_setprio 2\n"
    "v_mfma_f32_16x16x32_bf16 v[104:107], v[2:5], v[60:63], v[76:79]\n"
    "ds_read_b64_tr_b16 v[40:41], v202 offset:24064\n"
    "v_exp_f32 v96, v96\n"
    "v_mfma_f32_16x16x32_bf16 v[108:111], v[8:11], v[60:63], v[76:79]\n"
    "ds_read_b64_tr_b16 v[42:43], v202 offset:24072\n"
    "v_exp_f32 v97, v97\n"
    "v_mfma_f32_16x16x32_bf16 v[124:127], v[24:27], v[116:119], v[124:127]\n"
    "v_exp_f32 v98, v98\n"
    "v_mfma_f32_16x16x32_bf16 v[140:143], v[32:35], v[116:119], v[140:143]\n"
    "s_setprio 0\n"
    "v_exp_f32 v99, v99\n"
    "v_exp_f32 v100, v100\n"
    "v_exp_f32 v101, v101\n"
    "v_exp_f32 v102, v102\n"
    "s_waitcnt vmcnt(6)\n"
    "s_barrier\n"
    "s_cmp_lg_u32 s66, 0\n"
    "s_cbranch_scc1 L_q2_%=\n"
    "s_mov_b64 exec, 0xffff\n"
    "buffer_store_dwordx2 v[244:245], v237, s[60:63], 0 offen offset:256 sc1\n"
    "s_mov_b64 exec, -1\n"
    "L_q2_%=:\n"
    "s_add_u32 m0, s69, 28672\n"
    "s_add_u32 s67, s75, 4423680\n"
    "buffer_load_dwordx4 v254, s[48:51], s67 offen offset:0 lds\n"
    "v_exp_f32 v103, v103\n"
    "v_cvt_pk_bf16_f32 v112, v96, v97\n"
    "v_cvt_pk_bf16_f32 v113, v98, v99\n"
    "v_cvt_pk_bf16_f32 v114, v100, v101\n"
    "v_cvt_pk_bf16_f32 v115, v102, v103\n"
    "s_cmp_eq_u32 s83, 2\n"
    "s_cbranch_scc1 L_k2_%=\n"
    "L_kr2_%=:\n"
    "v_sub_u32 v203, v203, v204\n"
    "s_setprio 2\n"
    "s_waitcnt lgkmcnt(7)\n"
    "v_mfma_f32_16x16x32_bf16 v[96:99], v[12:15], v[48:51], v[64:67]\n"
    "ds_read_b64_tr_b16 v[44:45], v203 offset:0\n"
    "v_exp_f32 v104, v104\n"
    "s_waitcnt lgkmcnt(6)\n"
    "v_mfma_f32_16x16x32_bf16 v[100:103], v[18:21], v[48:51], v[64:67]\n"
    "ds_read_b64_tr_b16 v[46:47], v203 offset:8\n"
    "v_exp_f32 v105, v105\n"
    "v_mfma_f32_16x16x32_bf16 v[128:131], v[28:31], v[112:115], v[128:131]\n"
    "v_exp_f32 v106, v106\n"
    "v_mfma_f32_16x16x32_bf16 v[144:147], v[32:35], v[112:115], v[144:147]\n"
    "s_setprio 0\n"
    "s_add_u32 m0, s69, 32768\n"
    "s_add_u32 s67, s75, 4718592\n"
    "buffer_load_dwordx4 v254, s[48:51], s67 offen offset:0 lds\n"
    "v_exp_f32 v107, v107\n"
    "v_exp_f32 v108, v108\n"
    "v_exp_f32 v109, v109\n"
    "v_exp_f32 v110, v110\n"
    "v_exp_f32 v111, v111\n"
    "v_cvt_pk_bf16_f32 v116, v104, v105\n"
    "v_cvt_pk_bf16_f32 v117, v106, v107\n"
    "v_cvt_pk_bf16_f32 v118, v108, v109\n"
    "v_cvt_pk_bf16_f32 v119, v110, v111\n"
    "s_setprio 2\n"
    "v_mfma_f32_16x16x32_bf16 v[104:107], v[12:15], v[56:59], v[68:71]\n"
    "ds_read2st64_b64 v[0:3], v200 offset0:36 offset1:40\n"
    "v_exp_f32 v96, v96\n"
    "v_mfma_f32_16x16x32_bf16 v[108:111], v[18:21], v[56:59], v[68:71]\n"
    "ds_read_b64 v[4:5], v200 offset:19456\n"
    "v_exp_f32 v97, v97\n"
    "v_mfma_f32_16x16x32_bf16 v[132:135], v[28:31], v[116:119], v[132:135]\n"
    "ds_read2st64_b64 v[6:9], v201 offset0:36 offset1:40\n"
    "v_exp_f32 v98, v98\n"
    "v_mfma_f32_16x16x32_bf16 v[148:151], v[32:35], v[116:119], v[148:151]\n"
    "s_setprio 0\n"
    "s_add_u32 m0, s69, 36864\n"
    "s_add_u32 s67, s75, 5013504\n"
    "buffer_load_dwordx4 v254, s[48:51], s67 offen offset:0 lds\n"
    "v_exp_f32 v99, v99\n"
    "v_exp_f32 v100, v100\n"
    "v_exp_f32 v101, v101\n"
    "v_exp_f32 v102, v102\n"
    "v_exp_f32 v103, v103\n"
    "v_cvt_pk_bf16_f32 v112, v96, v97\n"
    "v_cvt_pk_bf16_f32 v113, v98, v99\n"
    "v_cvt_pk_bf16_f32 v114, v100, v101\n"
    "v_cvt_pk_bf16_f32 v115, v102, v103\n"
    "s_setprio 2\n"
    "v_mfma_f32_16x16x32_bf16 v[96:99], v[14:17], v[52:55], v[72:75]\n"
    "ds_read_b64 v[10:11], v200 offset:19464\n"
    "v_exp_f32 v104, v104\n"
    "s_waitcnt lgkmcnt(10)\n"
    "v_mfma_f32_16x16x32_bf16 v[100:103], v[20:23], v[52:55], v[72:75]\n"
    "ds_read_b64_tr_b16 v[24:25], v202 offset:18432\n"
    "v_exp_f32 v105, v105\n"
    "s_waitcnt lgkmcnt(9)\n"
    "v_mfma_f32_16x16x32_bf16 v[120:123], v[36:39], v[112:115], v[120:123]\n"
    "ds_read_b64_tr_b16 v[26:27], v202 offset:18440\n"
    "v_exp_f32 v106, v106\n"
    "s_waitcnt lgkmcnt(6)\n"
    "v_mfma_f32_16x16x32_bf16 v[136:139], v[44:47], v[112:115], v[136:139]\n"
    "s_setprio 0\n"
    "s_add_u32 m0, s69, 40960\n"
    "s_add_u32 s67, s76, 0\n"
    "buffer_load_dwordx4 v252, s[48:51], s67 offen offset:0 lds\n"
    "v_exp_f32 v107, v107\n"
    "v_exp_f32 v108, v108\n"
    "v_exp_f32 v109, v109\n"
    "v_exp_f32 v110, v110\n"
    "v_exp_f32 v111, v111\n"
    "v_cvt_pk_bf16_f32 v116, v104, v105\n"
    "v_cvt_pk_bf16_f32 v117, v106, v107\n"
    "v_cvt_pk_bf16_f32 v118, v108, v109\n"
    "v_cvt_pk_bf16_f32 v119, v110, v111\n"
    "s_setprio 2\n"
    "v_mfma_f32_16x16x32_bf16 v[104:107], v[14:17], v[60:63], v[76:79]\n"
    "ds_read_b64_tr_b16 v[28:29], v202 offset:19456\n"
    "v_exp_f32 v96, v96\n"
    "v_mfma_f32_16x16x32_bf16 v[108:111], v[20:23], v[60:63], v[76:79]\n"
    "ds_read_b64_tr_b16 v[30:31], v202 offset:19464\n"
    "v_exp_f32 v97, v97\n"
    "v_mfma_f32_16x16x32_bf16 v[124:127], v[36:39], v[116:119], v[124:127]\n"
    "v_exp_f32 v98, v98\n"
    "v_mfma_f32_16x16x32_bf16 v[140:143], v[44:47], v[116:119], v[140:143]\n"
    "s_setprio 0\n"
    "s_add_u32 m0, s69, 45056\n"
    "s_add_u32 s67, s76, 589824\n"
    "buffer_load_dwordx4 v252, s[48:51], s67 offen offset:0 lds\n"
    "v_exp_f32 v99, v99\n"
    "v_exp_f32 v100, v100\n"
    "v_exp_f32 v101, v101\n"
    "v_exp_f32 v102, v102\n"
    "v_exp_f32 v103, v103\n"
    "v_cvt_pk_bf16_f32 v112, v96, v97\n"
    "v_cvt_pk_bf16_f32 v113, v98, v99\n"
    "v_cvt_pk_bf16_f32 v114, v100, v101\n"
    "v_cvt_pk_bf16_f32 v115, v102, v103\n"
    "s_cmp_eq_u32 s83, 3\n"
    "s_cbranch_scc1 L_k3_%=\n"
    "L_kr3_%=:\n"
    "s_add_u32 m0, s69, 49152\n"
    "s_add_u32 s67, s76, 1179648\n"
    "buffer_load_dwordx4 v252, s[48:51], s67 offen offset:0 lds\n"
    "v_sub_u32 v203, v203, v204\n"
    "s_setprio 2\n"
    "s_waitcnt lgkmcnt(7)\n"
    "v_mfma_f32_16x16x32_bf16 v[96:99], v[0:3], v[48:51], v[64:67]\n"
    "ds_read_b64_tr_b16 v[32:33], v203 offset:0\n"
    "v_exp_f32 v104, v104\n"
    "s_waitcnt lgkmcnt(6)\n"
    "v_mfma_f32_16x16x32_bf16 v[100:103], v[6:9], v[48:51], v[64:67]\n"
    "ds_read_b64_tr_b16 v[34:35], v203 offset:8\n"
    "v_exp_f32 v105, v105\n"
    "v_mfma_f32_16x16x32_bf16 v[128:131], v[40:43], v[112:115], v[128:131]\n"
    "v_exp_f32 v106, v106\n"
    "v_mfma_f32_16x16x32_bf16 v[144:147], v[44:47], v[112:115], v[144:147]\n"
    "s_setprio 0\n"
    "v_exp_f32 v107, v107\n"
    "v_exp_f32 v108, v108\n"
    "v_exp_f32 v109, v109\n"
    "v_exp_f32 v110, v110\n"
    "v_exp_f32 v111, v111\n"
    "v_cvt_pk_bf16_f32 v116, v104, v105\n"
    "v_cvt_pk_bf16_f32 v117, v106, v107\n"
    "v_cvt_pk_bf16_f32 v118, v108, v109\n"
    "v_cvt_pk_bf16_f32 v119, v110, v111\n"
    "s_cmp_lg_u32 s71, 2\n"
    "s_cbranch_scc1 L_q11_%=\n"
    "s_mov_b32 m0, 162816\n"
    "s_mov_b32 exec_lo, 0xffffffff\n"
    "s_mov_b32 exec_hi, 0x1ff\n"
    "s_mov_b32 s67, 165888\n"
    "buffer_load_dword v212, s[52:55], s67 offen lds\n"
    "s_mov_b32 exec_lo, 0x3f\n"
    "s_mov_b32 exec_hi, 0x0\n"
    "s_mov_b32 s67, 82944\n"
    "buffer_load_dword v214, s[44:47], s67 offen lds\n"
    "s_mov_b64 exec, -1\n"
    "L_q11_%=:\n"
    "s_cmp_lg_u32 s71, 3\n"
    "s_cbranch_scc1 L_q12_%=\n"
    "s_mov_b32 m0, 162816\n"
    "s_mov_b32 exec_lo, 0xffffffff\n"
    "s_mov_b32 exec_hi, 0x1ff\n"
    "s_mov_b32 s67, 248832\n"
    "buffer_load_dword v212, s[52:55], s67 offen lds\n"
    "s_mov_b32 exec_lo, 0x3f\n"
    "s_mov_b32 exec_hi, 0x0\n"
    "s_mov_b32 s67, 124416\n"
    "buffer_load_dword v214, s[44:47], s67 offen lds\n"
    "s_mov_b64 exec, -1\n"
    "L_q12_%=:\n"
    "s_waitcnt vmcnt(8)\n"
    "s_barrier\n"
    "s_add_u32 m0, s69, 53248\n"
    "s_add_u32 s67, s76, 1769472\n"
    "buffer_load_dwordx4 v252, s[48:51], s67 offen offset:0 lds\n"
    "s_setprio 2\n"
    "v_mfma_f32_16x16x32_bf16 v[104:107], v[0:3], v[56:59], v[68:71]\n"
    "ds_read2st64_b64 v[12:15], v200 offset0:27 offset1:31\n"
    "v_exp_f32 v96, v96\n"
    "v_mfma_f32_16x16x32_bf16 v[108:111], v[6:9], v[56:59], v[68:71]\n"
    "ds_read_b64 v[16:17], v200 offset:14848\n"
    "v_exp_f32 v97, v97\n"
    "v_mfma_f32_16x16x32_bf16 v[132:135], v[40:43], v[116:119], v[132:135]\n"
    "ds_read2st64_b64 v[18:21], v201 offset0:27 offset1:31\n"
    "v_exp_f32 v98, v98\n"
    "v_mfma_f32_16x16x32_bf16 v[148:151], v[44:47], v[116:119], v[148:151]\n"
    "L_st12_%=:\n"
    "s_cbranch_execz L_bk%=\n"
    "s_setprio 0\n"
    "v_exp_f32 v99, v99\n"
    "v_exp_f32 v100, v100\n"
    "v_exp_f32 v101, v101\n"
    "v_exp_f32 v102, v102\n"
    "v_exp_f32 v103, v103\n"
    "v_cvt_pk_bf16_f32 v112, v96, v97\n"
    "v_cvt_pk_bf16_f32 v113, v98, v99\n"
    "v_cvt_pk_bf16_f32 v114, v100, v101\n"
    "v_cvt_pk_bf16_f32 v115, v102, v103\n"
    "s_setprio 2\n"
    "v_mfma_f32_16x16x32_bf16 v[96:99], v[2:5], v[52:55], v[72:75]\n"
    "ds_read_b64 v[22:23], v200 offset:14856\n"
    "v_exp_f32 v104, v104\n"
    "s_waitcnt lgkmcnt(10)\n"
    "v_mfma_f32_16x16x32_bf16 v[100:103], v[8:11], v[52:55], v[72:75]\n"
    "ds_read_b64_tr_b16 v[36:37], v202 offset:13824\n"
    "v_exp_f32 v105, v105\n"
    "s_waitcnt lgkmcnt(9)\n"
    "v_mfma_f32_16x16x32_bf16 v[120:123], v[24:27], v[112:115], v[120:123]\n"
    "ds_read_b64_tr_b16 v[38:39], v202 offset:13832\n"
    "v_exp_f32 v106, v106\n"
    "s_waitcnt lgkmcnt(6)\n"
    "v_mfma_f32_16x16x32_bf16 v[136:139], v[32:35], v[112:115], v[136:139]\n"
    "s_setprio 0\n"
    "v_exp_f32 v107, v107\n"
    "v_exp_f32 v108, v108\n"
    "v_exp_f32 v109, v109\n"
    "v_exp_f32 v110, v110\n"
    "v_exp_f32 v111, v111\n"
    "v_cvt_pk_bf16_f32 v116, v104, v105\n"
    "v_cvt_pk_bf16_f32 v117, v106, v107\n"
    "v_cvt_pk_bf16_f32 v118, v108, v109\n"
    "v_cvt_pk_bf16_f32 v119, v110, v111\n"
    "s_setprio 2\n"
    "v_mfma_f32_16x16x32_bf16 v[104:107], v[2:5], v[60:63], v[76:79]\n"
    "ds_read_b64_tr_b16 v[40:41], v202 offset:14848\n"
    "v_exp_f32 v96, v96\n"
    "v_mfma_f32_16x16x32_bf16 v[108:111], v[8:11], v[60:63], v[76:79]\n"
    "ds_read_b64_tr_b16 v[42:43], v202 offset:14856\n"
    "v_exp_f32 v97, v97\n"
    "v_mfma_f32_16x16x32_bf16 v[124:127], v[24:27], v[116:119], v[124:127]\n"
    "v_exp_f32 v98, v98\n"
    "v_mfma_f32_16x16x32_bf16 v[140:143], v[32:35], v[116:119], v[140:143]\n"
    "s_setprio 0\n"
    "v_exp_f32 v99, v99\n"
    "v_exp_f32 v100, v100\n"
    "v_exp_f32 v101, v101\n"
    "v_exp_f32 v102, v102\n"
    "v_exp_f32 v103, v103\n"
    "s_add_u32 m0, s69, 57344\n"
    "s_add_u32 s67, s76, 2359296\n"
    "buffer_load_dwordx4 v252, s[48:51], s67 offen offset:0 lds\n"
    "v_cvt_pk_bf16_f32 v112, v96, v97\n"
    "v_cvt_pk_bf16_f32 v113, v98, v99\n"
    "v_cvt_pk_bf16_f32 v114, v100, v101\n"
    "v_cvt_pk_bf16_f32 v115, v102, v103\n"
    "s_cmp_eq_u32 s83, 4\n"
    "s_cbranch_scc1 L_k4_%=\n"
    "L_kr4_%=:\n"
    "v_sub_u32 v203, v203, v204\n"
    "s_setprio 2\n"
    "s_waitcnt lgkmcnt(7)\n"
    "v_mfma_f32_16x16x32_bf16 v[96:99], v[12:15], v[48:51], v[64:67]\n"
    "ds_read_b64_tr_b16 v[44:45], v203 offset:0\n"
    "v_exp_f32 v104, v104\n"
    "s_waitcnt lgkmcnt(6)\n"
    "v_mfma_f32_16x16x32_bf16 v[100:103], v[18:21], v[48:51], v[64:67]\n"
    "ds_read_b64_tr_b16 v[46:47], v203 offset:8\n"
    "v_exp_f32 v105, v105\n"
    "v_mfma_f32_16x16x32_bf16 v[128:131], v[28:31], v[112:115], v[128:131]\n"
    "v_exp_f32 v106, v106\n"
    "v_mfma_f32_16x16x32_bf16 v[144:147], v[32:35], v[112:115], v[144:147]\n"
    "s_setprio 0\n"
    "v_exp_f32 v107, v107\n"
    "v_exp_f32 v108, v108\n"
    "v_exp_f32 v109, v109\n"
    "v_exp_f32 v110, v110\n"
    "v_exp_f32 v111, v111\n"
    "v_cvt_pk_bf16_f32 v116, v104, v105\n"
    "v_cvt_pk_bf16_f32 v117, v106, v107\n"
    "v_cvt_pk_bf16_f32 v118, v108, v109\n"
    "v_cvt_pk_bf16_f32 v119, v110, v111\n"
    "s_setprio 2\n"
    "v_mfma_f32_16x16x32_bf16 v[104:107], v[12:15], v[56:59], v[68:71]\n"
    "ds_read2st64_b64 v[0:3], v200 offset0:18 offset1:22\n"
    "v_exp_f32 v96, v96\n"
    "v_mfma_f32_16x16x32_bf16 v[108:111], v[18:21], v[56:59], v[68:71]\n"
    "ds_read_b64 v[4:5], v200 offset:10240\n"
    "v_exp_f32 v97, v97\n"
    "v_mfma_f32_16x16x32_bf16 v[132:135], v[28:31], v[116:119], v[132:135]\n"
    "ds_read2st64_b64 v[6:9], v201 offset0:18 offset1:22\n"
    "v_exp_f32 v98, v98\n"
    "v_mfma_f32_16x16x32_bf16 v[148:151], v[32:35], v[116:119], v[148:151]\n"
    "s_setprio 0\n"
    "v_exp_f32 v99, v99\n"
    "v_exp_f32 v100, v100\n"
    "v_exp_f32 v101, v101\n"
    "v_exp_f32 v102, v102\n"
    "v_exp_f32 v103, v103\n"
    "v_cvt_pk_bf16_f32 v112, v96, v97\n"
    "v_cvt_pk_bf16_f32 v113, v98, v99\n"
    "v_cvt_pk_bf16_f32 v114, v100, v101\n"
    "v_cvt_pk_bf16_f32 v115, v102, v103\n"
    "s_setprio 2\n"
    "v_mfma_f32_16x16x32_bf16 v[96:99], v[14:17], v[52:55], v[72:75]\n"
    "ds_read_b64 v[10:11], v200 offset:10248\n"
    "v_exp_f32 v104, v104\n"
    "s_waitcnt lgkmcnt(10)\n"
    "v_mfma_f32_16x16x32_bf16 v[100:103], v[20:23], v[52:55], v[72:75]\n"
    "ds_read_b64_tr_b16 v[24:25], v202 offset:9216\n"
    "v_exp_f32 v105, v105\n"
    "s_waitcnt lgkmcnt(9)\n"
    "v_mfma_f32_16x16x32_bf16 v[120:123], v[36:39], v[112:115], v[120:123]\n"
    "ds_read_b64_tr_b16 v[26:27], v202 offset:9224\n"
    "v_exp_f32 v106, v106\n"
    "s_waitcnt lgkmcnt(6)\n"
    "v_mfma_f32_16x16x32_bf16 v[136:139], v[44:47], v[112:115], v[136:139]\n"
    "s_setprio 0\n"
    "v_exp_f32 v107, v107\n"
    "v_exp_f32 v108, v108\n"
    "v_exp_f32 v109, v109\n"
    "v_exp_f32 v110, v110\n"
    "s_waitcnt vmcnt(5)\n"
    "s_barrier\n"
    "s_mov_b32 exec_lo, 0x0\n"
    "s_mov_b32 exec_hi, 0xf\n"
    "buffer_load_dwordx2 v[238:239], v240, s[60:63], 0 offen offset:128 sc1\n"
    "s_mov_b64 exec, -1\n"
    "s_add_u32 m0, s69, 61440\n"
    "s_add_u32 s67, s76, 2949120\n"
    "buffer_load_dwordx4 v252, s[48:51], s67 offen offset:0 lds\n"
    "v_exp_f32 v111, v111\n"
    "v_cvt_pk_bf16_f32 v116, v104, v105\n"
    "v_cvt_pk_bf16_f32 v117, v106, v107\n"
    "v_cvt_pk_bf16_f32 v118, v108, v109\n"
    "v_cvt_pk_bf16_f32 v119, v110, v111\n"
    "s_setprio 2\n"
    "v_mfma_f32_16x16x32_bf16 v[104:107], v[14:17], v[60:63], v[76:79]\n"
    "ds_read_b64_tr_b16 v[28:29], v202 offset:10240\n"
    "v_exp_f32 v96, v96\n"
    "v_mfma_f32_16x16x32_bf16 v[108:111], v[20:23], v[60:63], v[76:79]\n"
    "ds_read_b64_tr_b16 v[30:31], v202 offset:10248\n"
    "v_exp_f32 v97, v97\n"
    "v_mfma_f32_16x16x32_bf16 v[124:127], v[36:39], v[116:119], v[124:127]\n"
    "v_exp_f32 v98, v98\n"
    "v_mfma_f32_16x16x32_bf16 v[140:143], v[44:47], v[116:119], v[140:143]\n"
    "s_setprio 0\n"
    "v_exp_f32 v99, v99\n"
    "v_exp_f32 v100, v100\n"
    "v_exp_f32 v101, v101\n"
    "v_exp_f32 v102, v102\n"
    "v_exp_f32 v103, v103\n"
    "v_cvt_pk_bf16_f32 v112, v96, v97\n"
    "v_cvt_pk_bf16_f32 v113, v98, v99\n"
    "v_cvt_pk_bf16_f32 v114, v100, v101\n"
    "v_cvt_pk_bf16_f32 v115, v102, v103\n"
    "s_add_u32 m0, s69, 65536\n"
    "s_add_u32 s67, s76, 3538944\n"
    "buffer_load_dwordx4 v252, s[48:51], s67 offen offset:0 lds\n"
    "s_cmp_eq_u32 s83, 5\n"
    "s_cbranch_scc1 L_k5_%=\n"
    "L_kr5_%=:\n"
    "v_sub_u32 v203, v203, v204\n"
    "s_setprio 2\n"
    "s_waitcnt lgkmcnt(7)\n"
    "v_mfma_f32_16x16x32_bf16 v[96:99], v[0:3], v[48:51], v[64:67]\n"
    "ds_read_b64_tr_b16 v[32:33], v203 offset:0\n"
    "v_exp_f32 v104, v104\n"
    "s_waitcnt lgkmcnt(6)\n"
    "v_mfma_f32_16x16x32_bf16 v[100:103], v[6:9], v[48:51], v[64:67]\n"
    "ds_read_b64_tr_b16 v[34:35], v203 offset:8\n"
    "v_exp_f32 v105, v105\n"
    "v_mfma_f32_16x16x32_bf16 v[128:131], v[40:43], v[112:115], v[128:131]\n"
    "v_exp_f32 v106, v106\n"
    "v_mfma_f32_16x16x32_bf16 v[144:147], v[44:47], v[112:115], v[144:147]\n"
    "s_setprio 0\n"
    "v_exp_f32 v107, v107\n"
    "v_exp_f32 v108, v108\n"
    "v_exp_f32 v109, v109\n"
    "v_exp_f32 v110, v110\n"
    "v_exp_f32 v111, v111\n"
    "v_cvt_pk_bf16_f32 v116, v104, v105\n"
    "v_cvt_pk_bf16_f32 v117, v106, v107\n"
    "v_cvt_pk_bf16_f32 v118, v108, v109\n"
    "v_cvt_pk_bf16_f32 v119, v110, v111\n"
    "s_setprio 2\n"
    "v_mfma_f32_16x16x32_bf16 v[104:107], v[0:3], v[56:59], v[68:71]\n"
    "ds_read2st64_b64 v[12:15], v200 offset0:9 offset1:13\n"
    "v_exp_f32 v96, v96\n"
    "v_mfma_f32_16x16x32_bf16 v[108:111], v[6:9], v[56:59], v[68:71]\n"
    "ds_read_b64 v[16:17], v200 offset:5632\n"
    "v_exp_f32 v97, v97\n"
    "v_mfma_f32_16x16x32_bf16 v[132:135], v[40:43], v[116:119], v[132:135]\n"
    "ds_read2st64_b64 v[18:21], v201 offset0:9 offset1:13\n"
    "v_exp_f32 v98, v98\n"
    "v_mfma_f32_16x16x32_bf16 v[148:151], v[44:47], v[116:119], v[148:151]\n"
    "s_setprio 0\n"
    "v_exp_f32 v99, v99\n"
    "v_exp_f32 v100, v100\n"
    "s_add_u32 m0, s69, 69632\n"
    "s_add_u32 s67, s76, 4128768\n"
    "buffer_load_dwordx4 v252, s[48:51], s67 offen offset:0 lds\n"
    "v_exp_f32 v101, v101\n"
    "v_exp_f32 v102, v102\n"
    "v_exp_f32 v103, v103\n"
    "v_cvt_pk_bf16_f32 v112, v96, v97\n"
    "v_cvt_pk_bf16_f32 v113, v98, v99\n"
    "v_cvt_pk_bf16_f32 v114, v100, v101\n"
    "v_cvt_pk_bf16_f32 v115, v102, v103\n"
    "s_setprio 2\n"
    "v_mfma_f32_16x16x32_bf16 v[96:99], v[2:5], v[52:55], v[72:75]\n"
    "ds_read_b64 v[22:23], v200 offset:5640\n"
    "v_exp_f32 v104, v104\n"
    "s_waitcnt lgkmcnt(10)\n"
    "v_mfma_f32_16x16x32_bf16 v[100:103], v[8:11], v[52:55], v[72:75]\n"
    "ds_read_b64_tr_b16 v[36:37], v202 offset:4608\n"
    "v_exp_f32 v105, v105\n"
    "s_waitcnt lgkmcnt(9)\n"
    "v_mfma_f32_16x16x32_bf16 v[120:123], v[24:27], v[112:115], v[120:123]\n"
    "ds_read_b64_tr_b16 v[38:39], v202 offset:4616\n"
    "v_exp_f32 v106, v106\n"
    "s_waitcnt lgkmcnt(6)\n"
    "v_mfma_f32_16x16x32_bf16 v[136:139], v[32:35], v[112:115], v[136:139]\n"
    "s_setprio 0\n"
    "v_exp_f32 v107, v107\n"
    "v_exp_f32 v108, v108\n"
    "v_exp_f32 v109, v109\n"
    "v_exp_f32 v110, v110\n"
    "v_exp_f32 v111, v111\n"
    "s_add_u32 m0, s69, 73728\n"
    "s_add_u32 s67, s76, 4718592\n"
    "buffer_load_dwordx4 v252, s[48:51], s67 offen offset:0 lds\n"
    "v_cvt_pk_bf16_f32 v116, v104, v105\n"
    "v_cvt_pk_bf16_f32 v117, v106, v107\n"
    "v_cvt_pk_bf16_f32 v118, v108, v109\n"
    "v_cvt_pk_bf16_f32 v119, v110, v111\n"
    "s_setprio 2\n"
    "v_mfma_f32_16x16x32_bf16 v[104:107], v[2:5], v[60:63], v[76:79]\n"
    "ds_read_b64_tr_b16 v[40:41], v202 offset:5632\n"
    "v_exp_f32 v96, v96\n"
    "v_mfma_f32_16x16x32_bf16 v[108:111], v[8:11], v[60:63], v[76:79]\n"
    "ds_read_b64_tr_b16 v[42:43], v202 offset:5640\n"
    "v_exp_f32 v97, v97\n"
    "v_mfma_f32_16x16x32_bf16 v[124:127], v[24:27], v[116:119], v[124:127]\n"
    "v_exp_f32 v98, v98\n"
    "v_mfma_f32_16x16x32_bf16 v[140:143], v[32:35], v[116:119], v[140:143]\n"
    "s_setprio 0\n"
    "v_exp_f32 v99, v99\n"
    "v_exp_f32 v100, v100\n"
    "v_exp_f32 v101, v101\n"
    "v_exp_f32 v102, v102\n"
    "v_exp_f32 v103, v103\n"
    "v_cvt_pk_bf16_f32 v112, v96, v97\n"
    "v_cvt_pk_bf16_f32 v113, v98, v99\n"
    "v_cvt_pk_bf16_f32 v114, v100, v101\n"
    "v_cvt_pk_bf16_f32 v115, v102, v103\n"
    "s_cmp_eq_u32 s83, 6\n"
    "s_cbranch_scc1 L_k6_%=\n"
    "L_kr6_%=:\n"
    "v_sub_u32 v203, v203, v204\n"
    "s_setprio 2\n"
    "s_waitcnt lgkmcnt(7)\n"
    "v_mfma_f32_16x16x32_bf16 v[96:99], v[12:15], v[48:51], v[64:67]\n"
    "ds_read_b64_tr_b16 v[44:45], v203 offset:0\n"
    "v_exp_f32 v104, v104\n"
    "s_waitcnt lgkmcnt(6)\n"
    "v_mfma_f32_16x16x32_bf16 v[100:103], v[18:21], v[48:51], v[64:67]\n"
    "ds_read_b64_tr_b16 v[46:47], v203 offset:8\n"
    "v_exp_f32 v105, v105\n"
    "v_mfma_f32_16x16x32_bf16 v[128:131], v[28:31], v[112:115], v[128:131]\n"
    "v_exp_f32 v106, v106\n"
    "v_mfma_f32_16x16x32_bf16 v[144:147], v[32:35], v[112:115], v[144:147]\n"
    "s_setprio 0\n"
    "s_waitcnt vmcnt(8)\n"
    "s_barrier\n"
    "s_waitcnt vmcnt(4)\n"
    "v_cmp_ne_u64 vcc, s[98:99], v[238:239]\n"
    "s_mov_b32 s22, 0x0\n"
    "s_mov_b32 s23, 0xf\n"
    "s_and_b64 vcc, vcc, s[22:23]\n"
    "s_cbranch_vccnz L_sl1_%=\n"
    "L_rt1_%=:\n"
    "s_add_u32 m0, s85, 77824\n"
    "s_add_u32 s67, s4, s77\n"
    "buffer_load_dwordx4 v220, s[52:55], s67 offen offset:0 lds\n"
    "s_add_u32 m0, m0, 896\n"
    "s_nop 0\n"
    "buffer_load_dwordx4 v220, s[52:55], s67 offen offset:128 lds\n"
    "v_exp_f32 v107, v107\n"
    "v_exp_f32 v108, v108\n"
    "v_exp_f32 v109, v109\n"
    "v_exp_f32 v110, v110\n"
    "v_exp_f32 v111, v111\n"
    "v_cvt_pk_bf16_f32 v116, v104, v105\n"
    "v_cvt_pk_bf16_f32 v117, v106, v107\n"
    "v_cvt_pk_bf16_f32 v118, v108, v109\n"
    "v_cvt_pk_bf16_f32 v119, v110, v111\n"
    "s_setprio 2\n"
    "v_mfma_f32_16x16x32_bf16 v[104:107], v[12:15], v[56:59], v[68:71]\n"
    "ds_read2st64_b64 v[0:3], v200 offset0:0 offset1:4\n"
    "v_exp_f32 v96, v96\n"
    "v_mfma_f32_16x16x32_bf16 v[108:111], v[18:21], v[56:59], v[68:71]\n"
    "ds_read_b64 v[4:5], v200 offset:1024\n"
    "v_exp_f32 v97, v97\n"
    "v_mfma_f32_16x16x32_bf16 v[132:135], v[28:31], v[116:119], v[132:135]\n"
    "ds_read2st64_b64 v[6:9], v201 offset0:0 offset1:4\n"
    "v_exp_f32 v98, v98\n"
    "v_mfma_f32_16x16x32_bf16 v[148:151], v[32:35], v[116:119], v[148:151]\n"
    "s_setprio 0\n"
    "s_add_u32 m0, s85, 86016\n"
    "s_add_u32 s67, s17, 0\n"
    "buffer_load_dwordx4 v250, s[60:63], s67 offen offset:0 sc1 lds\n"
    "buffer_load_dwordx4 v250, s[60:63], s67 offen offset:1024 sc1 lds\n"
    "v_exp_f32 v99, v99\n"
    "v_exp_f32 v100, v100\n"
    "v_exp_f32 v101, v101\n"
    "v_exp_f32 v102, v102\n"
    "v_exp_f32 v103, v103\n"
    "v_cvt_pk_bf16_f32 v112, v96, v97\n"
    "v_cvt_pk_bf16_f32 v113, v98, v99\n"
    "v_cvt_pk_bf16_f32 v114, v100, v101\n"
    "v_cvt_pk_bf16_f32 v115, v102, v103\n"
    "s_setprio 2\n"
    "v_mfma_f32_16x16x32_bf16 v[96:99], v[14:17], v[52:55], v[72:75]\n"
    "ds_read_b64 v[10:11], v200 offset:1032\n"
    "v_exp_f32 v104, v104\n"
    "s_waitcnt lgkmcnt(10)\n"
    "v_mfma_f32_16x16x32_bf16 v[100:103], v[20:23], v[52:55], v[72:75]\n"
    "ds_read_b64_tr_b16 v[24:25], v202 offset:0\n"
    "v_exp_f32 v105, v105\n"
    "s_waitcnt lgkmcnt(9)\n"
    "v_mfma_f32_16x16x32_bf16 v[120:123], v[36:39], v[112:115], v[120:123]\n"
    "ds_read_b64_tr_b16 v[26:27], v202 offset:8\n"
    "v_exp_f32 v106, v106\n"
    "s_waitcnt lgkmcnt(6)\n"
    "v_mfma_f32_16x16x32_bf16 v[136:139], v[44:47], v[112:115], v[136:139]\n"
    "s_setprio 0\n"
    "s_add_u32 m0, s85, 94208\n"
    "s_add_u32 s67, s5, s77\n"
    "buffer_load_dwordx4 v220, s[52:55], s67 offen offset:0 lds\n"
    "s_add_u32 m0, m0, 896\n"
    "s_nop 0\n"
    "buffer_load_dwordx4 v220, s[52:55], s67 offen offset:128 lds\n"
    "v_exp_f32 v107, v107\n"
    "v_exp_f32 v108, v108\n"
    "v_exp_f32 v109, v109\n"
    "v_exp_f32 v110, v110\n"
    "v_exp_f32 v111, v111\n"
    "v_cvt_pk_bf16_f32 v116, v104, v105\n"
    "v_cvt_pk_bf16_f32 v117, v106, v107\n"
    "v_cvt_pk_bf16_f32 v118, v108, v109\n"
    "v_cvt_pk_bf16_f32 v119, v110, v111\n"
    "s_setprio 2\n"
    "v_mfma_f32_16x16x32_bf16 v[104:107], v[14:17], v[60:63], v[76:79]\n"
    "ds_read_b64_tr_b16 v[28:29], v202 offset:1024\n"
    "v_exp_f32 v96, v96\n"
    "v_mfma_f32_16x16x32_bf16 v[108:111], v[20:23], v[60:63], v[76:79]\n"
    "ds_read_b64_tr_b16 v[30:31], v202 offset:1032\n"
    "v_exp_f32 v97, v97\n"
    "v_mfma_f32_16x16x32_bf16 v[124:127], v[36:39], v[116:119], v[124:127]\n"
    "v_exp_f32 v98, v98\n"
    "v_mfma_f32_16x16x32_bf16 v[140:143], v[44:47], v[116:119], v[140:143]\n"
    "s_setprio 0\n"
    "v_exp_f32 v99, v99\n"
    "v_exp_f32 v100, v100\n"
    "s_add_u32 m0, s85, 144384\n"
    "s_add_u32 s67, s4, s77\n"
    "buffer_load_dwordx4 v221, s[52:55], s67 offen offset:0 lds\n"
    "s_add_u32 s67, s67, 35840\n"
    "buffer_load_dwordx4 v221, s[52:55], s67 offen offset:1024 lds\n"
    "v_exp_f32 v101, v101\n"
    "v_exp_f32 v102, v102\n"
    "v_exp_f32 v103, v103\n"
    "v_cvt_pk_bf16_f32 v112, v96, v97\n"
    "v_cvt_pk_bf16_f32 v113, v98, v99\n"
    "v_cvt_pk_bf16_f32 v114, v100, v101\n"
    "v_cvt_pk_bf16_f32 v115, v102, v103\n"
    "s_cmp_eq_u32 s83, 7\n"
    "s_cbranch_scc1 L_k7_%=\n"
    "L_kr7_%=:\n"
    "v_mov_b32 v247, 0xf149f2ca\n"
    "s_add_u32 m0, s85, 152576\n"
    "s_add_u32 s67, s18, 0\n"
    "buffer_load_dwordx4 v250, s[60:63], s67 offen offset:0 sc1 lds\n"
    "buffer_load_dwordx4 v250, s[60:63], s67 offen offset:1024 sc1 lds\n"
    "v_cndmask_b32 v80, v64, v247, s[88:89]\n"
    "v_cndmask_b32 v81, v64, v247, s[90:91]\n"
    "v_cndmask_b32 v82, v64, v247, s[92:93]\n"
    "v_cndmask_b32 v83, v64, v247, s[94:95]\n"
    "v_cndmask_b32 v84, v68, v247, s[88:89]\n"
    "v_cndmask_b32 v85, v68, v247, s[90:91]\n"
    "v_cndmask_b32 v86, v68, v247, s[92:93]\n"
    "v_cndmask_b32 v87, v68, v247, s[94:95]\n"
    "v_cndmask_b32 v88, v72, v247, s[88:89]\n"
    "v_cndmask_b32 v89, v72, v247, s[90:91]\n"
    "v_cndmask_b32 v90, v72, v247, s[92:93]\n"
    "v_cndmask_b32 v91, v72, v247, s[94:95]\n"
    "v_cndmask_b32 v92, v76, v247, s[88:89]\n"
    "v_cndmask_b32 v93, v76, v247, s[90:91]\n"
    "v_cndmask_b32 v94, v76, v247, s[92:93]\n"
    "v_cndmask_b32 v95, v76, v247, s[94:95]\n"
    "v_sub_u32 v203, v203, v204\n"
    "s_setprio 2\n"
    "s_waitcnt lgkmcnt(7)\n"
    "v_mfma_f32_16x16x32_bf16 v[96:99], v[0:3], v[48:51], v[80:83]\n"
    "ds_read_b64_tr_b16 v[32:33], v203 offset:0\n"
    "v_exp_f32 v104, v104\n"
    "s_waitcnt lgkmcnt(6)\n"
    "v_mfma_f32_16x16x32_bf16 v[100:103], v[6:9], v[48:51], v[64:67]\n"
    "ds_read_b64_tr_b16 v[34:35], v203 offset:8\n"
    "v_exp_f32 v105, v105\n"
    "v_mfma_f32_16x16x32_bf16 v[128:131], v[40:43], v[112:115], v[128:131]\n"
    "v_exp_f32 v106, v106\n"
    "v_mfma_f32_16x16x32_bf16 v[144:147], v[44:47], v[112:115], v[144:147]\n"
    "s_setprio 0\n"
    "v_exp_f32 v107, v107\n"
    "v_exp_f32 v108, v108\n"
    "s_waitcnt vmcnt(13)\n"
    "s_barrier\n"
    "s_cmp_lg_u32 s66, 0\n"
    "s_cbranch_scc1 L_q3_%=\n"
    "s_mov_b64 exec, 0xffff\n"
    "buffer_store_dwordx2 v[244:245], v237, s[60:63], 0 offen offset:288 sc1\n"
    "s_mov_b64 exec, -1\n"
    "L_q3_%=:\n"
    "s_add_u32 m0, s85, 0\n"
    "s_add_u32 s67, s6, s77\n"
    "buffer_load_dwordx4 v220, s[52:55], s67 offen offset:0 lds\n"
    "s_add_u32 m0, m0, 896\n"
    "s_nop 0\n"
    "buffer_load_dwordx4 v220, s[52:55], s67 offen offset:128 lds\n"
    "v_exp_f32 v109, v109\n"
    "v_exp_f32 v110, v110\n"
    "v_exp_f32 v111, v111\n"
    "v_cvt_pk_bf16_f32 v116, v104, v105\n"
    "v_cvt_pk_bf16_f32 v117, v106, v107\n"
    "v_cvt_pk_bf16_f32 v118, v108, v109\n"
    "v_cvt_pk_bf16_f32 v119, v110, v111\n"
    "s_setprio 2\n"
    "v_mfma_f32_16x16x32_bf16 v[108:111], v[6:9], v[56:59], v[84:87]\n"
    "v_exp_f32 v96, v96\n"
    "v_mfma_f32_16x16x32_bf16 v[132:135], v[40:43], v[116:119], v[132:135]\n"
    "v_exp_f32 v97, v97\n"
    "v_mfma_f32_16x16x32_bf16 v[148:151], v[44:47], v[116:119], v[148:151]\n"
    "s_setprio 0\n"
    "s_add_u32 m0, s85, 8192\n"
    "s_add_u32 s67, s4, s78\n"
    "buffer_load_dwordx4 v221, s[52:55], s67 offen offset:0 lds\n"
    "s_add_u32 s67, s67, 35840\n"
    "buffer_load_dwordx4 v221, s[52:55], s67 offen offset:1024 lds\n"
    "v_exp_f32 v98, v98\n"
    "v_exp_f32 v99, v99\n"
    "v_exp_f32 v100, v100\n"
    "v_exp_f32 v101, v101\n"
    "v_exp_f32 v102, v102\n"
    "v_exp_f32 v103, v103\n"
    "v_cvt_pk_bf16_f32 v112, v96, v97\n"
    "v_cvt_pk_bf16_f32 v113, v98, v99\n"
    "v_cvt_pk_bf16_f32 v114, v100, v101\n"
    "v_cvt_pk_bf16_f32 v115, v102, v103\n"
    "s_add_u32 m0, s85, 16384\n"
    "s_add_u32 s67, s19, 0\n"
    "buffer_load_dwordx4 v250, s[60:63], s67 offen offset:0 sc1 lds\n"
    "buffer_load_dwordx4 v250, s[60:63], s67 offen offset:1024 sc1 lds\n"
    "s_setprio 2\n"
    "v_mfma_f32_16x16x32_bf16 v[96:99], v[2:5], v[52:55], v[88:91]\n"
    "v_exp_f32 v108, v108\n"
    "s_waitcnt lgkmcnt(6)\n"
    "v_mfma_f32_16x16x32_bf16 v[100:103], v[8:11], v[52:55], v[72:75]\n"
    "v_exp_f32 v109, v109\n"
    "s_waitcnt lgkmcnt(4)\n"
    "v_mfma_f32_16x16x32_bf16 v[120:123], v[24:27], v[112:115], v[120:123]\n"
    "v_exp_f32 v110, v110\n"
    "s_waitcnt lgkmcnt(0)\n"
    "v_mfma_f32_16x16x32_bf16 v[136:139], v[32:35], v[112:115], v[136:139]\n"
    "s_setprio 0\n"
    "v_exp_f32 v111, v111\n"
    "v_mov_b32 v116, 0\n"
    "v_mov_b32 v117, 0\n"
    "v_cvt_pk_bf16_f32 v118, v108, v109\n"
    "v_cvt_pk_bf16_f32 v119, v110, v111\n"
    "s_setprio 2\n"
    "v_mfma_f32_16x16x32_bf16 v[108:111], v[8:11], v[60:63], v[92:95]\n"
    "v_exp_f32 v96, v96\n"
    "v_mfma_f32_16x16x32_bf16 v[124:127], v[24:27], v[116:119], v[124:127]\n"
    "v_exp_f32 v97, v97\n"
    "v_mfma_f32_16x16x32_bf16 v[140:143], v[32:35], v[116:119], v[140:143]\n"
    "s_setprio 0\n"
    "s_add_u32 m0, s85, 24576\n"
    "s_add_u32 s67, s7, s77\n"
    "buffer_load_dwordx4 v220, s[52:55], s67 offen offset:0 lds\n"
    "s_add_u32 m0, m0, 896\n"
    "s_nop 0\n"
    "buffer_load_dwordx4 v220, s[52:55], s67 offen offset:128 lds\n"
    "v_exp_f32 v98, v98\n"
    "v_exp_f32 v99, v99\n"
    "v_exp_f32 v100, v100\n"
    "v_exp_f32 v101, v101\n"
    "v_exp_f32 v102, v102\n"
    "v_exp_f32 v103, v103\n"
    "v_cvt_pk_bf16_f32 v112, v96, v97\n"
    "v_cvt_pk_bf16_f32 v113, v98, v99\n"
    "v_cvt_pk_bf16_f32 v114, v100, v101\n"
    "v_cvt_pk_bf16_f32 v115, v102, v103\n"
    "s_add_u32 m0, s85, 32768\n"
    "s_add_u32 s67, s4, s79\n"
    "buffer_load_dwordx4 v221, s[52:55], s67 offen offset:0 lds\n"
    "s_add_u32 s67, s67, 35840\n"
    "buffer_load_dwordx4 v221, s[52:55], s67 offen offset:1024 lds\n"
    "s_setprio 2\n"
    "v_mfma_f32_16x16x32_bf16 v[128:131], v[28:31], v[112:115], v[128:131]\n"
    "v_exp_f32 v108, v108\n"
    "v_mfma_f32_16x16x32_bf16 v[144:147], v[32:35], v[112:115], v[144:147]\n"
    "s_setprio 0\n"
    "v_exp_f32 v109, v109\n"
    "v_exp_f32 v110, v110\n"
    "v_exp_f32 v111, v111\n"
    "v_mov_b32 v116, 0\n"
    "v_mov_b32 v117, 0\n"
    "v_cvt_pk_bf16_f32 v118, v108, v109\n"
    "v_cvt_pk_bf16_f32 v119, v110, v111\n"
    "s_waitcnt vmcnt(16)\n"
    "s_barrier\n"
    "s_add_u32 m0, s85, 40960\n"
    "s_add_u32 s67, s17, 2048\n"
    "buffer_load_dwordx4 v250, s[60:63], s67 offen offset:0 sc1 lds\n"
    "buffer_load_dwordx4 v250, s[60:63], s67 offen offset:1024 sc1 lds\n"
    "s_setprio 2\n"
    "v_mfma_f32_16x16x32_bf16 v[132:135], v[28:31], v[116:119], v[132:135]\n"
    "v_mfma_f32_16x16x32_bf16 v[148:151], v[32:35], v[116:119], v[148:151]\n"
    "s_setprio 0\n"
    "v_add_f32 v156, v120, v121\n"
    "v_add_f32 v157, v122, v123\n"
    "v_add_f32 v156, v156, v124\n"
    "v_add_f32 v157, v157, v125\n"
    "v_add_f32 v156, v156, v126\n"
    "v_add_f32 v157, v157, v127\n"
    "v_add_f32 v156, v156, v128\n"
    "v_add_f32 v157, v157, v129\n"
    "v_add_f32 v156, v156, v130\n"
    "v_add_f32 v157, v157, v131\n"
    "v_add_f32 v156, v156, v132\n"
    "v_add_f32 v157, v157, v133\n"
    "v_add_f32 v156, v156, v134\n"
    "s_add_u32 m0, s85, 49152\n"
    "s_add_u32 s67, s5, s77\n"
    "s_add_u32 s67, s67, 36864\n"
    "buffer_load_dwordx4 v220, s[52:55], s67 offen offset:0 lds\n"
    "s_add_u32 m0, m0, 896\n"
    "s_nop 0\n"
    "buffer_load_dwordx4 v220, s[52:55], s67 offen offset:128 lds\n"
    "v_add_f32 v157, v157, v135\n"
    "v_add_f32 v156, v156, v136\n"
    "v_add_f32 v157, v157, v137\n"
    "v_add_f32 v156, v156, v140\n"
    "v_add_f32 v157, v157, v141\n"
    "v_add_f32 v156, v156, v146\n"
    "v_add_f32 v157, v157, v147\n"
    "v_add_f32 v156, v156, v150\n"
    "v_add_f32 v157, v157, v151\n"
    "v_mov_b32 v152, v136\n"
    "v_mov_b32 v160, v136\n"
    "v_mov_b32 v153, v140\n"
    "v_mov_b32 v161, v140\n"
    "v_mov_b32 v154, v144\n"
    "v_mov_b32 v162, v144\n"
    "v_mov_b32 v155, v148\n"
    "v_mov_b32 v163, v148\n"
    "v_permlane16_swap_b32 v160, v152\n"
    "v_permlane16_swap_b32 v161, v153\n"
    "v_permlane16_swap_b32 v162, v154\n"
    "v_permlane16_swap_b32 v163, v155\n"
    "v_add_f32 v156, v156, v157\n"
    "v_add_f32 v157, v152, v153\n"
    "v_add_f32 v158, v154, v155\n"
    "v_add_f32 v157, v157, v158\n"
    "v_fma_f32 v156, v156, 0, v157\n"
    "v_cmp_nge_f32 vcc, 0x71800000, v156\n"
    "s_nop 1\n"
    "s_cbranch_vccnz L_fb%=\n"
    "L_fbr%=:\n"
    "s_waitcnt vmcnt(16)\n"
    "s_barrier\n"
    "s_mov_b32 exec_lo, 0x0\n"
    "s_mov_b32 exec_hi, 0xf0\n"
    "buffer_load_dwordx2 v[238:239], v240, s[60:63], 0 offen offset:128 sc1\n"
    "s_mov_b64 exec, -1\n"
    "v_rcp_f32 v152, v152\n"
    "v_rcp_f32 v153, v153\n"
    "v_rcp_f32 v154, v154\n"
    "v_rcp_f32 v155, v155\n"
    "v_mul_f32 v152, v152, v218\n"
    "v_mul_f32 v153, v153, v219\n"
    "v_mul_f32 v154, v154, v218\n"
    "v_mul_f32 v155, v155, v219\n"
    "s_add_u32 s30, s16, 67584\n"
    "v_mul_f32 v120, v120, v152\n"
    "v_mul_f32 v121, v121, v152\n"
    "v_mul_f32 v122, v122, v152\n"
    "v_mul_f32 v123, v123, v152\n"
    "v_mul_f32 v136, v136, v152\n"
    "v_mul_f32 v137, v137, v152\n"
    "v_cvt_pk_bf16_f32 v96, v120, v121\n"
    "v_cvt_pk_bf16_f32 v97, v122, v123\n"
    "v_cvt_pk_bf16_f32 v104, v136, v137\n"
    "v_mul_f32 v128, v128, v154\n"
    "v_mul_f32 v129, v129, v154\n"
    "v_mul_f32 v130, v130, v154\n"
    "v_mul_f32 v131, v131, v154\n"
    "v_mul_f32 v146, v146, v154\n"
    "v_mul_f32 v147, v147, v154\n"
    "v_cvt_pk_bf16_f32 v98, v128, v129\n"
    "v_cvt_pk_bf16_f32 v99, v130, v131\n"
    "v_cvt_pk_bf16_f32 v105, v146, v147\n"
    "s_waitcnt vmcnt(13)\n"
    "s_barrier\n"
    "s_add_u32 m0, s85, 57344\n"
    "s_add_u32 s67, s4, s80\n"
    "buffer_load_dwordx4 v221, s[52:55], s67 offen offset:0 lds\n"
    "s_add_u32 s67, s67, 35840\n"
    "buffer_load_dwordx4 v221, s[52:55], s67 offen offset:1024 lds\n"
    "v_mul_f32 v124, v124, v153\n"
    "v_mul_f32 v125, v125, v153\n"
    "v_mul_f32 v126, v126, v153\n"
    "v_mul_f32 v127, v127, v153\n"
    "v_mul_f32 v140, v140, v153\n"
    "v_mul_f32 v141, v141, v153\n"
    "v_cvt_pk_bf16_f32 v100, v124, v125\n"
    "v_cvt_pk_bf16_f32 v101, v126, v127\n"
    "v_cvt_pk_bf16_f32 v106, v140, v141\n"
    "s_add_u32 m0, s85, 65536\n"
    "s_add_u32 s67, s18, 2048\n"
    "buffer_load_dwordx4 v250, s[60:63], s67 offen offset:0 sc1 lds\n"
    "buffer_load_dwordx4 v250, s[60:63], s67 offen offset:1024 sc1 lds\n"
    "v_mul_f32 v132, v132, v155\n"
    "v_mul_f32 v133, v133, v155\n"
    "v_mul_f32 v134, v134, v155\n"
    "v_mul_f32 v135, v135, v155\n"
    "v_mul_f32 v150, v150, v155\n"
    "v_mul_f32 v151, v151, v155\n"
    "v_cvt_pk_bf16_f32 v102, v132, v133\n"
    "v_cvt_pk_bf16_f32 v103, v134, v135\n"
    "v_cvt_pk_bf16_f32 v107, v150, v151\n"
    "s_setprio 2\n"
    "buffer_store_dwordx4 v[96:99], v250, s[60:63], s30 offen offset:0 sc1\n"
    "buffer_store_dwordx4 v[100:103], v250, s[60:63], s30 offen offset:1024 sc1\n"
    "s_mov_b64 exec, 0xffff\n"
    "buffer_store_dwordx2 v[104:105], v235, s[60:63], s87 offen offset:0 sc1\n"
    "buffer_store_dwordx2 v[106:107], v235, s[60:63], s87 offen offset:512 sc1\n"
    "s_mov_b64 exec, -1\n"
    "s_setprio 0\n"
    "s_waitcnt vmcnt(15)\n"
    "s_barrier\n"
    "s_add_u32 m0, s85, 73728\n"
    "s_add_u32 s67, s6, s77\n"
    "s_add_u32 s67, s67, 36864\n"
    "buffer_load_dwordx4 v220, s[52:55], s67 offen offset:0 lds\n"
    "s_add_u32 m0, m0, 896\n"
    "s_nop 0\n"
    "buffer_load_dwordx4 v220, s[52:55], s67 offen offset:128 lds\n"
    "s_add_u32 m0, s85, 81920\n"
    "s_mov_b32 s67, s8\n"
    "buffer_load_dwordx4 v221, s[52:55], s67 offen offset:0 lds\n"
    "s_add_u32 m0, s85, 90112\n"
    "s_add_u32 s67, s19, 2048\n"
    "buffer_load_dwordx4 v250, s[60:63], s67 offen offset:0 sc1 lds\n"
    "buffer_load_dwordx4 v250, s[60:63], s67 offen offset:1024 sc1 lds\n"
    "s_waitcnt vmcnt(16)\n"
    "s_barrier\n"
    "s_waitcnt vmcnt(13)\n"
    "v_cmp_ne_u64 vcc, s[98:99], v[238:239]\n"
    "s_mov_b32 s22, 0x0\n"
    "s_mov_b32 s23, 0xf0\n"
    "s_and_b64 vcc, vcc, s[22:23]\n"
    "s_cbranch_vccnz L_sl2_%=\n"
    "L_rt2_%=:\n"
    "s_add_u32 m0, s85, 98304\n"
    "s_add_u32 s67, s7, s77\n"
    "s_add_u32 s67, s67, 36864\n"
    "buffer_load_dwordx4 v220, s[52:55], s67 offen offset:0 lds\n"
    "s_add_u32 m0, m0, 896\n"
    "s_nop 0\n"
    "buffer_load_dwordx4 v220, s[52:55], s67 offen offset:128 lds\n"
    "s_add_u32 m0, s85, 106496\n"
    "s_add_u32 s67, s17, 16384\n"
    "buffer_load_dwordx4 v250, s[60:63], s67 offen offset:0 sc1 lds\n"
    "buffer_load_dwordx4 v250, s[60:63], s67 offen offset:1024 sc1 lds\n"
    "s_add_u32 m0, s85, 114688\n"
    "s_add_u32 s67, s5, s78\n"
    "buffer_load_dwordx4 v220, s[52:55], s67 offen offset:0 lds\n"
    "s_add_u32 m0, m0, 896\n"
    "s_nop 0\n"
    "buffer_load_dwordx4 v220, s[52:55], s67 offen offset:128 lds\n"
    "s_waitcnt vmcnt(17)\n"
    "s_barrier\n"
    "s_add_u32 m0, s85, 122880\n"
    "s_add_u32 s67, s18, 16384\n"
    "buffer_load_dwordx4 v250, s[60:63], s67 offen offset:0 sc1 lds\n"
    "buffer_load_dwordx4 v250, s[60:63], s67 offen offset:1024 sc1 lds\n"
    "s_add_u32 m0, s85, 131072\n"
    "s_add_u32 s67, s6, s78\n"
    "buffer_load_dwordx4 v220, s[52:55], s67 offen offset:0 lds\n"
    "L_st13_%=:\n"
    "s_cbranch_execz L_bk%=\n"
    "s_add_u32 m0, m0, 896\n"
    "s_nop 0\n"
    "buffer_load_dwordx4 v220, s[52:55], s67 offen offset:128 lds\n"
    "s_add_u32 m0, s85, 139264\n"
    "s_add_u32 s67, s19, 16384\n"
    "buffer_load_dwordx4 v250, s[60:63], s67 offen offset:0 sc1 lds\n"
    "buffer_load_dwordx4 v250, s[60:63], s67 offen offset:1024 sc1 lds\n"
    "s_waitcnt vmcnt(15)\n"
    "s_barrier\n"
    "s_add_u32 m0, s85, 147456\n"
    "s_add_u32 s67, s7, s78\n"
    "buffer_load_dwordx4 v220, s[52:55], s67 offen offset:0 lds\n"
    "s_add_u32 m0, m0, 896\n"
    "s_nop 0\n"
    "buffer_load_dwordx4 v220, s[52:55], s67 offen offset:128 lds\n"
    "s_add_u32 m0, s85, 0\n"
    "s_add_u32 s67, s17, 18432\n"
    "buffer_load_dwordx4 v250, s[60:63], s67 offen offset:0 sc1 lds\n"
    "buffer_load_dwordx4 v250, s[60:63], s67 offen offset:1024 sc1 lds\n"
    "s_add_u32 m0, s85, 8192\n"
    "s_add_u32 s67, s5, s78\n"
    "s_add_u32 s67, s67, 36864\n"
    "buffer_load_dwordx4 v220, s[52:55], s67 offen offset:0 lds\n"
    "s_add_u32 m0, m0, 896\n"
    "s_nop 0\n"
    "buffer_load_dwordx4 v220, s[52:55], s67 offen offset:128 lds\n"
    "s_waitcnt vmcnt(14)\n"
    "s_barrier\n"
    "s_add_u32 m0, s85, 16384\n"
    "s_add_u32 s67, s18, 18432\n"
    "buffer_load_dwordx4 v250, s[60:63], s67 offen offset:0 sc1 lds\n"
    "buffer_load_dwordx4 v250, s[60:63], s67 offen offset:1024 sc1 lds\n"
    "s_add_u32 m0, s85, 24576\n"
    "s_add_u32 s67, s6, s78\n"
    "s_add_u32 s67, s67, 36864\n"
    "buffer_load_dwordx4 v220, s[52:55], s67 offen offset:0 lds\n"
    "s_add_u32 m0, m0, 896\n"
    "s_nop 0\n"
    "buffer_load_dwordx4 v220, s[52:55], s67 offen offset:128 lds\n"
    "s_add_u32 m0, s85, 32768\n"
    "s_add_u32 s67, s19, 18432\n"
    "buffer_load_dwordx4 v250, s[60:63], s67 offen offset:0 sc1 lds\n"
    "buffer_load_dwordx4 v250, s[60:63], s67 offen offset:1024 sc1 lds\n"
    "s_waitcnt vmcnt(14)\n"
    "s_barrier\n"
    "s_add_u32 m0, s85, 40960\n"
    "s_add_u32 s67, s7, s78\n"
    "s_add_u32 s67, s67, 36864\n"
    "buffer_load_dwordx4 v220, s[52:55], s67 offen offset:0 lds\n"
    "s_add_u32 m0, m0, 896\n"
    "s_nop 0\n"
    "buffer_load_dwordx4 v220, s[52:55], s67 offen offset:128 lds\n"
    "s_add_u32 m0, s85, 49152\n"
    "s_add_u32 s67, s4, s77\n"
    "s_add_u32 s67, s67, 36864\n"
    "buffer_load_dwordx4 v220, s[52:55], s67 offen offset:0 lds\n"
    "s_add_u32 m0, m0, 896\n"
    "s_nop 0\n"
    "buffer_load_dwordx4 v220, s[52:55], s67 offen offset:128 lds\n"
    "s_add_u32 m0, s85, 57344\n"
    "s_add_u32 s67, s17, 32768\n"
    "buffer_load_dwordx4 v250, s[60:63], s67 offen offset:0 sc1 lds\n"
    "buffer_load_dwordx4 v250, s[60:63], s67 offen offset:1024 sc1 lds\n"
    "s_waitcnt vmcnt(14)\n"
    "s_barrier\n"
    "s_add_u32 m0, s85, 65536\n"
    "s_add_u32 s67, s5, s79\n"
    "buffer_load_dwordx4 v220, s[52:55], s67 offen offset:0 lds\n"
    "s_add_u32 m0, m0, 896\n"
    "s_nop 0\n"
    "buffer_load_dwordx4 v220, s[52:55], s67 offen offset:128 lds\n"
    "s_add_u32 m0, s85, 73728\n"
    "s_add_u32 s67, s18, 32768\n"
    "buffer_load_dwordx4 v250, s[60:63], s67 offen offset:0 sc1 lds\n"
    "buffer_load_dwordx4 v250, s[60:63], s67 offen offset:1024 sc1 lds\n"
    "s_add_u32 m0, s85, 81920\n"
    "s_add_u32 s67, s6, s79\n"
    "buffer_load_dwordx4 v220, s[52:55], s67 offen offset:0 lds\n"
    "s_add_u32 m0, m0, 896\n"
    "s_nop 0\n"
    "buffer_load_dwordx4 v220, s[52:55], s67 offen offset:128 lds\n"
    "s_waitcnt vmcnt(14)\n"
    "s_barrier\n"
    "s_mov_b32 exec_lo, 0x0\n"
    "s_mov_b32 exec_hi, 0xf00\n"
    "buffer_load_dwordx2 v[238:239], v240, s[60:63], 0 offen offset:128 sc1\n"
    "s_mov_b64 exec, -1\n"
    "s_add_u32 m0, s85, 90112\n"
    "s_add_u32 s67, s19, 32768\n"
    "buffer_load_dwordx4 v250, s[60:63], s67 offen offset:0 sc1 lds\n"
    "buffer_load_dwordx4 v250, s[60:63], s67 offen offset:1024 sc1 lds\n"
    "s_add_u32 m0, s85, 98304\n"
    "s_add_u32 s67, s7, s79\n"
    "buffer_load_dwordx4 v220, s[52:55], s67 offen offset:0 lds\n"
    "s_add_u32 m0, m0, 896\n"
    "s_nop 0\n"
    "buffer_load_dwordx4 v220, s[52:55], s67 offen offset:128 lds\n"
    "s_add_u32 m0, s85, 106496\n"
    "s_mov_b32 s67, s8\n"
    "buffer_load_dwordx4 v220, s[52:55], s67 offen offset:0 lds\n"
    "s_add_u32 m0, m0, 896\n"
    "s_nop 0\n"
    "buffer_load_dwordx4 v220, s[52:55], s67 offen offset:128 lds\n"
    "s_waitcnt vmcnt(17)\n"
    "s_barrier\n"
    "s_add_u32 m0, s85, 114688\n"
    "s_add_u32 s67, s17, 34816\n"
    "buffer_load_dwordx4 v250, s[60:63], s67 offen offset:0 sc1 lds\n"
    "buffer_load_dwordx4 v250, s[60:63], s67 offen offset:1024 sc1 lds\n"
    "s_add_u32 m0, s85, 122880\n"
    "s_add_u32 s67, s5, s79\n"
    "s_add_u32 s67, s67, 36864\n"
    "buffer_load_dwordx4 v220, s[52:55], s67 offen offset:0 lds\n"
    "s_add_u32 m0, m0, 896\n"
    "s_nop 0\n"
    "buffer_load_dwordx4 v220, s[52:55], s67 offen offset:128 lds\n"
    "s_add_u32 m0, s85, 131072\n"
    "s_add_u32 s67, s18, 34816\n"
    "buffer_load_dwordx4 v250, s[60:63], s67 offen offset:0 sc1 lds\n"
    "buffer_load_dwordx4 v250, s[60:63], s67 offen offset:1024 sc1 lds\n"
    "s_waitcnt vmcnt(17)\n"
    "s_barrier\n"
    "s_add_u32 m0, s85, 139264\n"
    "s_add_u32 s67, s6, s79\n"
    "s_add_u32 s67, s67, 36864\n"
    "buffer_load_dwordx4 v220, s[52:55], s67 offen offset:0 lds\n"
    "s_add_u32 m0, m0, 896\n"
    "s_nop 0\n"
    "buffer_load_dwordx4 v220, s[52:55], s67 offen offset:128 lds\n"
    "s_add_u32 m0, s85, 147456\n"
    "s_add_u32 s67, s19, 34816\n"
    "buffer_load_dwordx4 v250, s[60:63], s67 offen offset:0 sc1 lds\n"
    "buffer_load_dwordx4 v250, s[60:63], s67 offen offset:1024 sc1 lds\n"
    "s_add_u32 m0, s85, 0\n"
    "s_add_u32 s67, s7, s79\n"
    "s_add_u32 s67, s67, 36864\n"
    "buffer_load_dwordx4 v220, s[52:55], s67 offen offset:0 lds\n"
    "s_add_u32 m0, m0, 896\n"
    "s_nop 0\n"
    "buffer_load_dwordx4 v220, s[52:55], s67 offen offset:128 lds\n"
    "s_waitcnt vmcnt(16)\n"
    "s_barrier\n"
    "v_cmp_ne_u64 vcc, s[98:99], v[238:239]\n"
    "s_mov_b32 s22, 0x0\n"
    "s_mov_b32 s23, 0xf00\n"
    "s_and_b64 vcc, vcc, s[22:23]\n"
    "s_cbranch_vccnz L_sl3_%=\n"
    "L_rt3_%=:\n"
    "s_add_u32 m0, s85, 8192\n"
    "s_add_u32 s67, s17, 49152\n"
    "buffer_load_dwordx4 v250, s[60:63], s67 offen offset:0 sc1 lds\n"
    "buffer_load_dwordx4 v250, s[60:63], s67 offen offset:1024 sc1 lds\n"
    "s_add_u32 m0, s85, 16384\n"
    "s_add_u32 s67, s5, s80\n"
    "buffer_load_dwordx4 v220, s[52:55], s67 offen offset:0 lds\n"
    "s_add_u32 m0, m0, 896\n"
    "s_nop 0\n"
    "buffer_load_dwordx4 v220, s[52:55], s67 offen offset:128 lds\n"
    "s_add_u32 m0, s85, 24576\n"
    "s_add_u32 s67, s18, 49152\n"
    "buffer_load_dwordx4 v250, s[60:63], s67 offen offset:0 sc1 lds\n"
    "buffer_load_dwordx4 v250, s[60:63], s67 offen offset:1024 sc1 lds\n"
    "s_waitcnt vmcnt(18)\n"
    "s_barrier\n"
    "s_add_u32 m0, s85, 32768\n"
    "s_add_u32 s67, s6, s80\n"
    "buffer_load_dwordx4 v220, s[52:55], s67 offen offset:0 lds\n"
    "s_add_u32 m0, m0, 896\n"
    "s_nop 0\n"
    "buffer_load_dwordx4 v220, s[52:55], s67 offen offset:128 lds\n"
    "s_add_u32 m0, s85, 40960\n"
    "s_add_u32 s67, s19, 49152\n"
    "buffer_load_dwordx4 v250, s[60:63], s67 offen offset:0 sc1 lds\n"
    "buffer_load_dwordx4 v250, s[60:63], s67 offen offset:1024 sc1 lds\n"
    "s_add_u32 m0, s85, 49152\n"
    "s_add_u32 s67, s7, s80\n"
    "buffer_load_dwordx4 v220, s[52:55], s67 offen offset:0 lds\n"
    "s_add_u32 m0, m0, 896\n"
    "s_nop 0\n"
    "buffer_load_dwordx4 v220, s[52:55], s67 offen offset:128 lds\n"
    "s_waitcnt vmcnt(18)\n"
    "s_barrier\n"
    "s_add_u32 m0, s85, 57344\n"
    "s_add_u32 s67, s17, 51200\n"
    "buffer_load_dwordx4 v250, s[60:63], s67 offen offset:0 sc1 lds\n"
    "buffer_load_dwordx4 v250, s[60:63], s67 offen offset:1024 sc1 lds\n"
    "s_add_u32 m0, s85, 65536\n"
    "s_add_u32 s67, s5, s80\n"
    "s_add_u32 s67, s67, 36864\n"
    "buffer_load_dwordx4 v220, s[52:55], s67 offen offset:0 lds\n"
    "s_add_u32 m0, m0, 896\n"
    "s_nop 0\n"
    "buffer_load_dwordx4 v220, s[52:55], s67 offen offset:128 lds\n"
    "s_add_u32 m0, s85, 73728\n"
    "s_add_u32 s67, s18, 51200\n"
    "buffer_load_dwordx4 v250, s[60:63], s67 offen offset:0 sc1 lds\n"
    "buffer_load_dwordx4 v250, s[60:63], s67 offen offset:1024 sc1 lds\n"
    "s_waitcnt vmcnt(18)\n"
    "s_barrier\n"
    "s_add_u32 m0, s85, 81920\n"
    "s_add_u32 s67, s6, s80\n"
    "s_add_u32 s67, s67, 36864\n"
    "buffer_load_dwordx4 v220, s[52:55], s67 offen offset:0 lds\n"
    "s_add_u32 m0, m0, 896\n"
    "s_nop 0\n"
    "buffer_load_dwordx4 v220, s[52:55], s67 offen offset:128 lds\n"
    "s_add_u32 m0, s85, 90112\n"
    "s_add_u32 s67, s19, 51200\n"
    "buffer_load_dwordx4 v250, s[60:63], s67 offen offset:0 sc1 lds\n"
    "buffer_load_dwordx4 v250, s[60:63], s67 offen offset:1024 sc1 lds\n"
    "s_add_u32 m0, s85, 98304\n"
    "s_add_u32 s67, s7, s80\n"
    "s_add_u32 s67, s67, 36864\n"
    "buffer_load_dwordx4 v220, s[52:55], s67 offen offset:0 lds\n"
    "s_add_u32 m0, m0, 896\n"
    "s_nop 0\n"
    "buffer_load_dwordx4 v220, s[52:55], s67 offen offset:128 lds\n"
    "s_waitcnt vmcnt(18)\n"
    "s_barrier\n"
    "s_add_u32 m0, s85, 106496\n"
    "s_add_u32 s67, s17, 65536\n"
    "buffer_load_dwordx4 v250, s[60:63], s67 offen offset:0 sc1 lds\n"
    "buffer_load_dwordx4 v250, s[60:63], s67 offen offset:1024 sc1 lds\n"
    "s_add_u32 m0, s85, 114688\n"
    "s_mov_b32 s67, s9\n"
    "buffer_load_dwordx4 v220, s[52:55], s67 offen offset:0 lds\n"
    "s_add_u32 m0, m0, 896\n"
    "s_nop 0\n"
    "buffer_load_dwordx4 v220, s[52:55], s67 offen offset:128 lds\n"
    "s_add_u32 m0, s85, 122880\n"
    "s_add_u32 s67, s18, 65536\n"
    "buffer_load_dwordx4 v250, s[60:63], s67 offen offset:0 sc1 lds\n"
    "buffer_load_dwordx4 v250, s[60:63], s67 offen offset:1024 sc1 lds\n"
    "s_waitcnt vmcnt(18)\n"
    "s_barrier\n"
    "s_add_u32 m0, s85, 131072\n"
    "s_mov_b32 s67, s10\n"
    "buffer_load_dwordx4 v220, s[52:55], s67 offen offset:0 lds\n"
    "s_add_u32 m0, m0, 896\n"
    "s_nop 0\n"
    "buffer_load_dwordx4 v220, s[52:55], s67 offen offset:128 lds\n"
    "s_add_u32 m0, s85, 139264\n"
    "s_add_u32 s67, s19, 65536\n"
    "buffer_load_dwordx4 v250, s[60:63], s67 offen offset:0 sc1 lds\n"
    "buffer_load_dwordx4 v250, s[60:63], s67 offen offset:1024 sc1 lds\n"
    "s_add_u32 m0, s85, 147456\n"
    "s_mov_b32 s67, s11\n"
    "buffer_load_dwordx4 v220, s[52:55], s67 offen offset:0 lds\n"
    "s_add_u32 m0, m0, 896\n"
    "s_nop 0\n"
    "buffer_load_dwordx4 v220, s[52:55], s67 offen offset:128 lds\n"
    "s_waitcnt vmcnt(18)\n"
    "s_barrier\n"
    "s_add_u32 m0, s85, 0\n"
    "s_add_u32 s67, s16, 67584\n"
    "buffer_load_dwordx4 v250, s[60:63], s67 offen offset:0 sc1 lds\n"
    "buffer_load_dwordx4 v250, s[60:63], s67 offen offset:1024 sc1 lds\n"
    "s_lshl_b32 s67, s84, 1\n"
    "s_add_u32 m0, s67, 8192\n"
    "s_mov_b32 s67, s12\n"
    "buffer_load_dwordx4 v222, s[44:47], s67 offen offset:0 lds\n"
    "buffer_load_dwordx4 v223, s[44:47], s67 offen offset:1024 lds\n"
    "s_mov_b64 exec, 0xffff\n"
    "buffer_load_dwordx4 v197, s[44:47], s67 offen offset:2048 lds\n"
    "s_mov_b64 exec, -1\n"
    "s_waitcnt vmcnt(17)\n"
    "s_barrier\n"
    "s_add_u32 m0, s85, 17408\n"
    "s_add_u32 s67, s17, 67584\n"
    "buffer_load_dwordx4 v250, s[60:63], s67 offen offset:0 sc1 lds\n"
    "buffer_load_dwordx4 v250, s[60:63], s67 offen offset:1024 sc1 lds\n"
    "s_lshl_b32 s67, s84, 1\n"
    "s_add_u32 m0, s67, 25600\n"
    "s_mov_b32 s67, s13\n"
    "buffer_load_dwordx4 v222, s[44:47], s67 offen offset:0 lds\n"
    "buffer_load_dwordx4 v223, s[44:47], s67 offen offset:1024 lds\n"
    "s_mov_b64 exec, 0xffff\n"
    "buffer_load_dwordx4 v197, s[44:47], s67 offen offset:2048 lds\n"
    "s_mov_b64 exec, -1\n"
    "s_waitcnt vmcnt(16)\n"
    "s_barrier\n"
    "s_add_u32 m0, s85, 34816\n"
    "s_add_u32 s67, s18, 67584\n"
    "buffer_load_dwordx4 v250, s[60:63], s67 offen offset:0 sc1 lds\n"
    "buffer_load_dwordx4 v250, s[60:63], s67 offen offset:1024 sc1 lds\n"
    "s_lshl_b32 s67, s84, 1\n"
    "s_add_u32 m0, s67, 43008\n"
    "s_mov_b32 s67, s14\n"
    "buffer_load_dwordx4 v222, s[44:47], s67 offen offset:0 lds\n"
    "buffer_load_dwordx4 v223, s[44:47], s67 offen offset:1024 lds\n"
    "s_mov_b64 exec, 0xffff\n"
    "buffer_load_dwordx4 v197, s[44:47], s67 offen offset:2048 lds\n"
    "s_mov_b64 exec, -1\n"
    "s_waitcnt vmcnt(15)\n"
    "s_barrier\n"
    "s_add_u32 m0, s85, 52224\n"
    "s_add_u32 s67, s19, 67584\n"
    "buffer_load_dwordx4 v250, s[60:63], s67 offen offset:0 sc1 lds\n"
    "buffer_load_dwordx4 v250, s[60:63], s67 offen offset:1024 sc1 lds\n"
    "s_lshl_b32 s67, s84, 1\n"
    "s_add_u32 m0, s67, 60416\n"
    "s_mov_b32 s67, s15\n"
    "buffer_load_dwordx4 v222, s[44:47], s67 offen offset:0 lds\n"
    "buffer_load_dwordx4 v223, s[44:47], s67 offen offset:1024 lds\n"
    "s_mov_b64 exec, 0xffff\n"
    "buffer_load_dwordx4 v197, s[44:47], s67 offen offset:2048 lds\n"
    "s_mov_b64 exec, -1\n"
    "s_waitcnt vmcnt(13)\n"
    "s_barrier\n"
    "s_mov_b32 exec_lo, 0xffff0000\n"
    "s_mov_b32 exec_hi, 0x0\n"
    "buffer_load_dwordx2 v[238:239], v240, s[60:63], 0 offen offset:128 sc1\n"
    "s_mov_b64 exec, -1\n"
    "s_add_u32 m0, s69, 69632\n"
    "s_mov_b32 s67, s87\n"
    "buffer_load_dwordx4 v250, s[60:63], s67 offen offset:0 sc1 lds\n"
    "s_add_u32 m0, s84, 73728\n"
    "s_mov_b32 s67, s86\n"
    "buffer_load_dwordx4 v222, s[44:47], s67 offen offset:0 lds\n"
    "s_mov_b64 exec, 0xff\n"
    "buffer_load_dwordx4 v223, s[44:47], s67 offen offset:1024 lds\n"
    "s_mov_b64 exec, -1\n"
    "s_add_u32 m0, s85, 78848\n"
    "s_add_u32 s67, s4, s78\n"
    "buffer_load_dwordx4 v220, s[52:55], s67 offen offset:0 lds\n"
    "s_add_u32 s67, s67, 35840\n"
    "buffer_load_dwordx4 v220, s[52:55], s67 offen offset:1024 lds\n"
    "s_waitcnt vmcnt(11)\n"
    "s_barrier\n"
    "s_add_u32 m0, s85, 87040\n"
    "s_add_u32 s67, s4, s79\n"
    "buffer_load_dwordx4 v220, s[52:55], s67 offen offset:0 lds\n"
    "s_add_u32 s67, s67, 35840\n"
    "buffer_load_dwordx4 v220, s[52:55], s67 offen offset:1024 lds\n"
    "s_add_u32 m0, s85, 95232\n"
    "s_add_u32 s67, s4, s80\n"
    "buffer_load_dwordx4 v220, s[52:55], s67 offen offset:0 lds\n"
    "s_add_u32 s67, s67, 35840\n"
    "buffer_load_dwordx4 v220, s[52:55], s67 offen offset:1024 lds\n"
    "s_add_u32 m0, s85, 103424\n"
    "s_add_u32 s67, s4, s78\n"
    "s_add_u32 s67, s67, 128\n"
    "buffer_load_dwordx4 v220, s[52:55], s67 offen offset:0 lds\n"
    "s_add_u32 s67, s67, 35840\n"
    "buffer_load_dwordx4 v220, s[52:55], s67 offen offset:1024 lds\n"
    "s_waitcnt vmcnt(12)\n"
    "s_barrier\n"
    "s_add_u32 m0, s85, 111616\n"
    "s_add_u32 s67, s4, s79\n"
    "s_add_u32 s67, s67, 128\n"
    "buffer_load_dwordx4 v220, s[52:55], s67 offen offset:0 lds\n"
    "s_add_u32 s67, s67, 35840\n"
    "buffer_load_dwordx4 v220, s[52:55], s67 offen offset:1024 lds\n"
    "s_add_u32 m0, s85, 119808\n"
    "s_add_u32 s67, s4, s80\n"
    "s_add_u32 s67, s67, 128\n"
    "buffer_load_dwordx4 v220, s[52:55], s67 offen offset:0 lds\n"
    "s_add_u32 s67, s67, 35840\n"
    "buffer_load_dwordx4 v220, s[52:55], s67 offen offset:1024 lds\n"
    "s_waitcnt vmcnt(12)\n"
    "s_barrier\n"
    "v_cmp_ne_u64 vcc, s[98:99], v[238:239]\n"
    "s_mov_b32 s22, 0xffff0000\n"
    "s_mov_b32 s23, 0x0\n"
    "s_and_b64 vcc, vcc, s[22:23]\n"
    "s_cbranch_vccnz L_sl4_%=\n"
    "L_rt4_%=:\n"
    "s_cmp_lg_u32 s66, 0\n"
    "s_cbranch_scc1 L_q4_%=\n"
    "s_mov_b64 exec, 0xffff\n"
    "buffer_store_dwordx2 v[244:245], v241, s[60:63], 0 offen offset:0 sc1\n"
    "s_mov_b64 exec, -1\n"
    "L_q4_%=:\n"
    "s_mul_i32 s33, s66, 3072\n"
    "s_add_u32 s33, s33, s82\n"
    "s_add_u32 s34, s33, 12288\n"
    "s_add_u32 s32, s82, 24576\n"
    "s_add_u32 s30, s69, 0x0\n"
    "v_add_u32 v249, s30, v250\n"
    "v_add_u32 v248, 0x1400, v232\n"
    "v_mov_b32 v32, 0\n"
    "v_mov_b32 v33, 0\n"
    "v_mov_b32 v34, 0\n"
    "v_mov_b32 v35, 0\n"
    "s_add_u32 s35, s82, 28672\n"
    "buffer_load_dwordx4 v[36:39], v232, s[60:63], s35 offen sc1\n"
    "s_add_u32 s35, s82, 32768\n"
    "buffer_load_dwordx4 v[40:43], v232, s[60:63], s35 offen sc1\n"
    "s_add_u32 s35, s82, 36864\n"
    "buffer_load_dwordx4 v[44:47], v232, s[60:63], s35 offen sc1\n"
    "s_waitcnt vmcnt(9)\n"
    "s_barrier\n"
    "s_barrier\n"
    "s_waitcnt vmcnt(3)\n"
    "s_barrier\n"
    "s_barrier\n"
    "s_mov_b32 exec_hi, 0xffff\n"
    "buffer_load_dwordx4 v[32:35], v232, s[60:63], s32 offen sc1\n"
    "s_mov_b32 exec_hi, -1\n"
    "buffer_load_dwordx4 v[0:3], v250, s[60:63], s33 offen offset:0 sc1\n"
    "buffer_load_dwordx4 v[4:7], v250, s[60:63], s33 offen offset:1024 sc1\n"
    "buffer_load_dwordx4 v[8:11], v250, s[60:63], s33 offen offset:2048 sc1\n"
    "s_setprio 2\n"
    "s_waitcnt vmcnt(6)\n"
    "v_mfma_f32_16x16x32_bf16 v[96:99], v[192:195], v[36:39], 0\n"
    "s_waitcnt vmcnt(5)\n"
    "v_mfma_f32_16x16x32_bf16 v[96:99], v[192:195], v[40:43], v[96:99]\n"
    "s_waitcnt vmcnt(4)\n"
    "v_mfma_f32_16x16x32_bf16 v[96:99], v[192:195], v[44:47], v[96:99]\n"
    "s_setprio 0\n"
    "v_lshlrev_b32 v120, 16, v180\n"
    "v_and_b32 v121, 0xffff0000, v180\n"
    "v_lshlrev_b32 v122, 16, v181\n"
    "v_and_b32 v123, 0xffff0000, v181\n"
    "v_lshlrev_b32 v124, 16, v182\n"
    "v_and_b32 v125, 0xffff0000, v182\n"
    "v_lshlrev_b32 v126, 16, v183\n"
    "v_and_b32 v127, 0xffff0000, v183\n"
    "v_lshlrev_b32 v128, 16, v184\n"
    "v_and_b32 v129, 0xffff0000, v184\n"
    "v_lshlrev_b32 v130, 16, v185\n"
    "v_and_b32 v131, 0xffff0000, v185\n"
    "v_lshlrev_b32 v132, 16, v186\n"
    "v_and_b32 v133, 0xffff0000, v186\n"
    "v_lshlrev_b32 v134, 16, v187\n"
    "v_and_b32 v135, 0xffff0000, v187\n"
    "s_waitcnt vmcnt(3)\n"
    "v_xor_b32 v80, v228, v32\n"
    "v_xor_b32 v81, v228, v33\n"
    "v_xor_b32 v82, v228, v34\n"
    "v_xor_b32 v83, v228, v35\n"
    "v_min3_u32 v156, v80, v81, v82\n"
    "v_min_u32 v156, v156, v83\n"
    "v_cmp_eq_u32 vcc, 0, v156\n"
    "s_nop 1\n"
    "s_cbranch_vccnz L_sl5_%=\n"
    "L_rt5_%=:\n"
    "s_waitcnt vmcnt(2)\n"
    "v_max3_u32 v156, v0, v1, v2\n"
    "s_waitcnt vmcnt(1)\n"
    "v_max3_u32 v157, v3, v4, v5\n"
    "v_max3_u32 v157, v157, v6, v7\n"
    "s_waitcnt vmcnt(0)\n"
    "v_max3_u32 v156, v156, v8, v9\n"
    "v_max3_u32 v157, v157, v10, v11\n"
    "v_max_u32 v156, v156, v157\n"
    "v_cmp_le_u32 vcc, v228, v156\n"
    "s_nop 1\n"
    "s_cbranch_vccnz L_sl6_%=\n"
    "L_rt6_%=:\n"
    "s_setprio 2\n"
    "v_mfma_f32_16x16x32_bf16 v[48:51], v[166:169], v[0:3], 0\n"
    "v_mfma_f32_16x16x32_bf16 v[52:55], v[164:167], v[0:3], 0\n"
    "v_mfma_f32_16x16x32_bf16 v[48:51], v[166:169], v[4:7], v[48:51]\n"
    "v_mfma_f32_16x16x32_bf16 v[52:55], v[164:167], v[4:7], v[52:55]\n"
    "v_mfma_f32_16x16x32_bf16 v[48:51], v[166:169], v[8:11], v[48:51]\n"
    "v_mfma_f32_16x16x32_bf16 v[52:55], v[164:167], v[8:11], v[52:55]\n"
    "s_setprio 0\n"
    "s_barrier\n"
    "ds_read_b128 v[100:103], v249 offset:0\n"
    "ds_read_b128 v[104:107], v249 offset:4096\n"
    "s_mov_b32 exec_lo, 0\n"
    "s_mov_b32 exec_hi, 0xffff0000\n"
    "ds_read_b128 v[32:35], v248\n"
    "s_mov_b64 exec, -1\n"
    "buffer_load_dwordx4 v[16:19], v250, s[60:63], s34 offen offset:0 sc1\n"
    "buffer_load_dwordx4 v[20:23], v250, s[60:63], s34 offen offset:1024 sc1\n"
    "buffer_load_dwordx4 v[24:27], v250, s[60:63], s34 offen offset:2048 sc1\n"
    "s_waitcnt lgkmcnt(2)\n"
    "v_mfma_f32_16x16x32_bf16 v[48:51], v[166:169], v[100:103], v[48:51]\n"
    "v_mfma_f32_16x16x32_bf16 v[52:55], v[164:167], v[100:103], v[52:55]\n"
    "s_waitcnt lgkmcnt(1)\n"
    "v_mfma_f32_16x16x32_bf16 v[56:59], v[166:169], v[104:107], 0\n"
    "v_mfma_f32_16x16x32_bf16 v[60:63], v[164:167], v[104:107], 0\n"
    "s_waitcnt lgkmcnt(0)\n"
    "v_mfma_f32_16x16x32_bf16 v[96:99], v[192:195], v[32:35], v[96:99]\n"
    "v_lshlrev_b32 v64, 16, v188\n"
    "v_and_b32 v65, 0xffff0000, v188\n"
    "v_lshlrev_b32 v66, 16, v189\n"
    "v_and_b32 v67, 0xffff0000, v189\n"
    "s_nop 3\n"
    "v_fma_f32 v64, v96, v217, v64\n"
    "v_fma_f32 v65, v97, v217, v65\n"
    "v_fma_f32 v66, v98, v217, v66\n"
    "v_fma_f32 v67, v99, v217, v67\n"
    "v_cvt_pk_bf16_f32 v158, v64, v65\n"
    "v_cvt_pk_bf16_f32 v159, v66, v67\n"
    "buffer_store_dwordx2 v[158:159], v233, s[56:59], 0 offen\n"
    "v_permlane16_swap_b32 v48, v52\n"
    "v_permlane16_swap_b32 v49, v53\n"
    "v_permlane16_swap_b32 v50, v54\n"
    "v_permlane16_swap_b32 v51, v55\n"
    "v_fma_f32 v120, v48, v217, v120\n"
    "v_fma_f32 v121, v49, v217, v121\n"
    "v_fma_f32 v122, v50, v217, v122\n"
    "v_fma_f32 v123, v51, v217, v123\n"
    "v_fma_f32 v124, v52, v217, v124\n"
    "v_fma_f32 v125, v53, v217, v125\n"
    "v_fma_f32 v126, v54, v217, v126\n"
    "v_fma_f32 v127, v55, v217, v127\n"
    "v_cvt_pk_bf16_f32 v120, v120, v121\n"
    "v_cvt_pk_bf16_f32 v121, v122, v123\n"
    "v_cvt_pk_bf16_f32 v122, v124, v125\n"
    "v_cvt_pk_bf16_f32 v123, v126, v127\n"
    "buffer_store_dwordx4 v[120:123], v234, s[56:59], 0 offen offset:0\n"
    "s_waitcnt vmcnt(4)\n"
    "v_max3_u32 v156, v16, v17, v18\n"
    "s_waitcnt vmcnt(3)\n"
    "v_max3_u32 v157, v19, v20, v21\n"
    "v_max3_u32 v157, v157, v22, v23\n"
    "s_waitcnt vmcnt(2)\n"
    "v_max3_u32 v156, v156, v24, v25\n"
    "v_max3_u32 v157, v157, v26, v27\n"
    "v_max_u32 v156, v156, v157\n"
    "v_cmp_le_u32 vcc, v228, v156\n"
    "s_nop 1\n"
    "s_cbranch_vccnz L_sl7_%=\n"
    "L_rt7_%=:\n"
    "s_setprio 2\n"
    "v_mfma_f32_16x16x32_bf16 v[56:59], v[166:169], v[16:19], v[56:59]\n"
    "v_mfma_f32_16x16x32_bf16 v[60:63], v[164:167], v[16:19], v[60:63]\n"
    "v_mfma_f32_16x16x32_bf16 v[56:59], v[166:169], v[20:23], v[56:59]\n"
    "v_mfma_f32_16x16x32_bf16 v[60:63], v[164:167], v[20:23], v[60:63]\n"
    "v_mfma_f32_16x16x32_bf16 v[56:59], v[166:169], v[24:27], v[56:59]\n"
    "v_mfma_f32_16x16x32_bf16 v[60:63], v[164:167], v[24:27], v[60:63]\n"
    "s_setprio 0\n"
    "s_nop 6\n"
    "v_permlane16_swap_b32 v56, v60\n"
    "v_permlane16_swap_b32 v57, v61\n"
    "v_permlane16_swap_b32 v58, v62\n"
    "v_permlane16_swap_b32 v59, v63\n"
    "v_fma_f32 v128, v56, v217, v128\n"
    "v_fma_f32 v129, v57, v217, v129\n"
    "v_fma_f32 v130, v58, v217, v130\n"
    "v_fma_f32 v131, v59, v217, v131\n"
    "v_fma_f32 v132, v60, v217, v132\n"
    "v_fma_f32 v133, v61, v217, v133\n"
    "v_fma_f32 v134, v62, v217, v134\n"
    "v_fma_f32 v135, v63, v217, v135\n"
    "v_cvt_pk_bf16_f32 v128, v128, v129\n"
    "v_cvt_pk_bf16_f32 v129, v130, v131\n"
    "v_cvt_pk_bf16_f32 v130, v132, v133\n"
    "v_cvt_pk_bf16_f32 v131, v134, v135\n"
    "buffer_store_dwordx4 v[128:131], v234, s[56:59], 0 offen offset:128\n"
    "s_branch L_end%=\n"
    "L_sl7_%=:\n"
    "s_mov_b32 s20, 65536\n"
    "L_sp7_%=:\n"
    "buffer_load_dwordx4 v[16:19], v250, s[60:63], s34 offen offset:0 sc1\n"
    "buffer_load_dwordx4 v[20:23], v250, s[60:63], s34 offen offset:1024 sc1\n"
    "buffer_load_dwordx4 v[24:27], v250, s[60:63], s34 offen offset:2048 sc1\n"
    "s_waitcnt vmcnt(0)\n"
    "v_xor_b32 v80, v228, v16\n"
    "v_xor_b32 v81, v228, v17\n"
    "v_xor_b32 v82, v228, v18\n"
    "v_xor_b32 v83, v228, v19\n"
    "v_xor_b32 v84, v228, v20\n"
    "v_xor_b32 v85, v228, v21\n"
    "v_xor_b32 v86, v228, v22\n"
    "v_xor_b32 v87, v228, v23\n"
    "v_xor_b32 v88, v228, v24\n"
    "v_xor_b32 v89, v228, v25\n"
    "v_xor_b32 v90, v228, v26\n"
    "v_xor_b32 v91, v228, v27\n"
    "v_min3_u32 v156, v80, v81, v82\n"
    "v_min3_u32 v156, v156, v83, v84\n"
    "v_min3_u32 v156, v156, v85, v86\n"
    "v_min3_u32 v156, v156, v87, v88\n"
    "v_min3_u32 v156, v156, v89, v90\n"
    "v_min_u32 v156, v156, v91\n"
    "v_cmp_eq_u32 vcc, 0, v156\n"
    "s_nop 1\n"
    "s_cbranch_vccz L_rt7_%=\n"
    "s_sub_u32 s20, s20, 1\n"
    "s_cmp_lg_u32 s20, 0\n"
    "s_cbranch_scc1 L_sp7_%=\n"
    "s_branch L_rt7_%=\n"
    "L_sl6_%=:\n"
    "s_mov_b32 s20, 65536\n"
    "L_sp6_%=:\n"
    "buffer_load_dwordx4 v[0:3], v250, s[60:63], s33 offen offset:0 sc1\n"
    "buffer_load_dwordx4 v[4:7], v250, s[60:63], s33 offen offset:1024 sc1\n"
    "buffer_load_dwordx4 v[8:11], v250, s[60:63], s33 offen offset:2048 sc1\n"
    "s_waitcnt vmcnt(0)\n"
    "v_xor_b32 v80, v228, v0\n"
    "v_xor_b32 v81, v228, v1\n"
    "v_xor_b32 v82, v228, v2\n"
    "v_xor_b32 v83, v228, v3\n"
    "v_xor_b32 v84, v228, v4\n"
    "v_xor_b32 v85, v228, v5\n"
    "v_xor_b32 v86, v228, v6\n"
    "v_xor_b32 v87, v228, v7\n"
    "v_xor_b32 v88, v228, v8\n"
    "v_xor_b32 v89, v228, v9\n"
    "v_xor_b32 v90, v228, v10\n"
    "v_xor_b32 v91, v228, v11\n"
    "v_min3_u32 v156, v80, v81, v82\n"
    "v_min3_u32 v156, v156, v83, v84\n"
    "v_min3_u32 v156, v156, v85, v86\n"
    "v_min3_u32 v156, v156, v87, v88\n"
    "v_min3_u32 v156, v156, v89, v90\n"
    "v_min_u32 v156, v156, v91\n"
    "v_cmp_eq_u32 vcc, 0, v156\n"
    "s_nop 1\n"
    "s_cbranch_vccz L_rt6_%=\n"
    "s_sub_u32 s20, s20, 1\n"
    "s_cmp_lg_u32 s20, 0\n"
    "s_cbranch_scc1 L_sp6_%=\n"
    "s_branch L_rt6_%=\n"
    "L_sl5_%=:\n"
    "s_mov_b32 s20, 65536\n"
    "L_sp5_%=:\n"
    "s_mov_b32 exec_hi, 0xffff\n"
    "buffer_load_dwordx4 v[32:35], v232, s[60:63], s32 offen sc1\n"
    "s_mov_b32 exec_hi, -1\n"
    "s_waitcnt vmcnt(0)\n"
    "v_xor_b32 v80, v228, v32\n"
    "v_xor_b32 v81, v228, v33\n"
    "v_xor_b32 v82, v228, v34\n"
    "v_xor_b32 v83, v228, v35\n"
    "v_min3_u32 v156, v80, v81, v82\n"
    "v_min_u32 v156, v156, v83\n"
    "v_cmp_eq_u32 vcc, 0, v156\n"
    "s_nop 1\n"
    "s_cbranch_vccz L_rt5_%=\n"
    "s_sub_u32 s20, s20, 1\n"
    "s_cmp_lg_u32 s20, 0\n"
    "s_cbranch_scc1 L_sp5_%=\n"
    "s_branch L_rt5_%=\n"
    "L_sl4_%=:\n"
    "s_mov_b32 s20, 65536\n"
    "L_sp4_%=:\n"
    "s_mov_b32 exec_lo, 0xffff0000\n"
    "s_mov_b32 exec_hi, 0x0\n"
    "buffer_load_dwordx2 v[238:239], v240, s[60:63], 0 offen offset:128 sc1\n"
    "s_mov_b64 exec, -1\n"
    "s_waitcnt vmcnt(0)\n"
    "v_cmp_ne_u64 vcc, s[98:99], v[238:239]\n"
    "s_mov_b32 s22, 0xffff0000\n"
    "s_mov_b32 s23, 0x0\n"
    "s_and_b64 vcc, vcc, s[22:23]\n"
    "s_cbranch_vccz L_rt4_%=\n"
    "s_sleep 1\n"
    "s_sub_u32 s20, s20, 1\n"
    "s_cmp_lg_u32 s20, 0\n"
    "s_cbranch_scc1 L_sp4_%=\n"
    "s_branch L_rt4_%=\n"
    "L_sl3_%=:\n"
    "s_mov_b32 s20, 65536\n"
    "L_sp3_%=:\n"
    "s_mov_b32 exec_lo, 0x0\n"
    "s_mov_b32 exec_hi, 0xf00\n"
    "buffer_load_dwordx2 v[238:239], v240, s[60:63], 0 offen offset:128 sc1\n"
    "s_mov_b64 exec, -1\n"
    "s_waitcnt vmcnt(0)\n"
    "v_cmp_ne_u64 vcc, s[98:99], v[238:239]\n"
    "s_mov_b32 s22, 0x0\n"
    "s_mov_b32 s23, 0xf00\n"
    "s_and_b64 vcc, vcc, s[22:23]\n"
    "s_cbranch_vccz L_rt3_%=\n"
    "s_sleep 1\n"
    "s_sub_u32 s20, s20, 1\n"
    "s_cmp_lg_u32 s20, 0\n"
    "s_cbranch_scc1 L_sp3_%=\n"
    "s_branch L_rt3_%=\n"
    "L_sl2_%=:\n"
    "s_mov_b32 s20, 65536\n"
    "L_sp2_%=:\n"
    "L_st14_%=:\n"
    "s_cbranch_execz L_bk%=\n"
    "s_mov_b32 exec_lo, 0x0\n"
    "s_mov_b32 exec_hi, 0xf0\n"
    "buffer_load_dwordx2 v[238:239], v240, s[60:63], 0 offen offset:128 sc1\n"
    "s_mov_b64 exec, -1\n"
    "s_waitcnt vmcnt(0)\n"
    "v_cmp_ne_u64 vcc, s[98:99], v[238:239]\n"
    "s_mov_b32 s22, 0x0\n"
    "s_mov_b32 s23, 0xf0\n"
    "s_and_b64 vcc, vcc, s[22:23]\n"
    "s_cbranch_vccz L_rt2_%=\n"
    "s_sleep 1\n"
    "s_sub_u32 s20, s20, 1\n"
    "s_cmp_lg_u32 s20, 0\n"
    "s_cbranch_scc1 L_sp2_%=\n"
    "s_branch L_rt2_%=\n"
    "L_sl1_%=:\n"
    "s_mov_b32 s20, 65536\n"
    "L_sp1_%=:\n"
    "s_mov_b32 exec_lo, 0x0\n"
    "s_mov_b32 exec_hi, 0xf\n"
    "buffer_load_dwordx2 v[238:239], v240, s[60:63], 0 offen offset:128 sc1\n"
    "s_mov_b64 exec, -1\n"
    "s_waitcnt vmcnt(0)\n"
    "v_cmp_ne_u64 vcc, s[98:99], v[238:239]\n"
    "s_mov_b32 s22, 0x0\n"
    "s_mov_b32 s23, 0xf\n"
    "s_and_b64 vcc, vcc, s[22:23]\n"
    "s_cbranch_vccz L_rt1_%=\n"
    "s_sleep 1\n"
    "s_sub_u32 s20, s20, 1\n"
    "s_cmp_lg_u32 s20, 0\n"
    "s_cbranch_scc1 L_sp1_%=\n"
    "s_branch L_rt1_%=\n"
    "L_k7_%=:\n"
    "s_nop 3\n"
    "v_mov_b32 v64, 0xf149f2ca\n"
    "v_mov_b32 v65, 0xf149f2ca\n"
    "v_mov_b32 v66, 0xf149f2ca\n"
    "v_mov_b32 v67, 0xf149f2ca\n"
    "v_mov_b32 v68, 0xf149f2ca\n"
    "v_mov_b32 v69, 0xf149f2ca\n"
    "v_mov_b32 v70, 0xf149f2ca\n"
    "v_mov_b32 v71, 0xf149f2ca\n"
    "v_mov_b32 v72, 0xf149f2ca\n"
    "v_mov_b32 v73, 0xf149f2ca\n"
    "v_mov_b32 v74, 0xf149f2ca\n"
    "v_mov_b32 v75, 0xf149f2ca\n"
    "v_mov_b32 v76, 0xf149f2ca\n"
    "v_mov_b32 v77, 0xf149f2ca\n"
    "v_mov_b32 v78, 0xf149f2ca\n"
    "v_mov_b32 v79, 0xf149f2ca\n"
    "s_nop 1\n"
    "s_branch L_kr7_%=\n"
    "L_k6_%=:\n"
    "s_nop 3\n"
    "v_mov_b32 v64, 0xf149f2ca\n"
    "v_mov_b32 v65, 0xf149f2ca\n"
    "v_mov_b32 v66, 0xf149f2ca\n"
    "v_mov_b32 v67, 0xf149f2ca\n"
    "v_mov_b32 v68, 0xf149f2ca\n"
    "v_mov_b32 v69, 0xf149f2ca\n"
    "v_mov_b32 v70, 0xf149f2ca\n"
    "v_mov_b32 v71, 0xf149f2ca\n"
    "v_mov_b32 v72, 0xf149f2ca\n"
    "v_mov_b32 v73, 0xf149f2ca\n"
    "v_mov_b32 v74, 0xf149f2ca\n"
    "v_mov_b32 v75, 0xf149f2ca\n"
    "v_mov_b32 v76, 0xf149f2ca\n"
    "v_mov_b32 v77, 0xf149f2ca\n"
    "v_mov_b32 v78, 0xf149f2ca\n"
    "v_mov_b32 v79, 0xf149f2ca\n"
    "s_nop 1\n"
    "s_branch L_kr6_%=\n"
    "L_k5_%=:\n"
    "s_nop 3\n"
    "v_mov_b32 v64, 0xf149f2ca\n"
    "v_mov_b32 v65, 0xf149f2ca\n"
    "v_mov_b32 v66, 0xf149f2ca\n"
    "v_mov_b32 v67, 0xf149f2ca\n"
    "v_mov_b32 v68, 0xf149f2ca\n"
    "v_mov_b32 v69, 0xf149f2ca\n"
    "v_mov_b32 v70, 0xf149f2ca\n"
    "v_mov_b32 v71, 0xf149f2ca\n"
    "v_mov_b32 v72, 0xf149f2ca\n"
    "v_mov_b32 v73, 0xf149f2ca\n"
    "v_mov_b32 v74, 0xf149f2ca\n"
    "v_mov_b32 v75, 0xf149f2ca\n"
    "v_mov_b32 v76, 0xf149f2ca\n"
    "v_mov_b32 v77, 0xf149f2ca\n"
    "v_mov_b32 v78, 0xf149f2ca\n"
    "v_mov_b32 v79, 0xf149f2ca\n"
    "s_nop 1\n"
    "s_branch L_kr5_%=\n"
    "L_k4_%=:\n"
    "s_nop 3\n"
    "v_mov_b32 v64, 0xf149f2ca\n"
    "v_mov_b32 v65, 0xf149f2ca\n"
    "v_mov_b32 v66, 0xf149f2ca\n"
    "v_mov_b32 v67, 0xf149f2ca\n"
    "v_mov_b32 v68, 0xf149f2ca\n"
    "v_mov_b32 v69, 0xf149f2ca\n"
    "v_mov_b32 v70, 0xf149f2ca\n"
    "v_mov_b32 v71, 0xf149f2ca\n"
    "v_mov_b32 v72, 0xf149f2ca\n"
    "v_mov_b32 v73, 0xf149f2ca\n"
    "v_mov_b32 v74, 0xf149f2ca\n"
    "v_mov_b32 v75, 0xf149f2ca\n"
    "v_mov_b32 v76, 0xf149f2ca\n"
    "v_mov_b32 v77, 0xf149f2ca\n"
    "v_mov_b32 v78, 0xf149f2ca\n"
    "v_mov_b32 v79, 0xf149f2ca\n"
    "s_nop 1\n"
    "s_branch L_kr4_%=\n"
    "L_k3_%=:\n"
    "s_nop 3\n"
    "v_mov_b32 v64, 0xf149f2ca\n"
    "v_mov_b32 v65, 0xf149f2ca\n"
    "v_mov_b32 v66, 0xf149f2ca\n"
    "v_mov_b32 v67, 0xf149f2ca\n"
    "v_mov_b32 v68, 0xf149f2ca\n"
    "v_mov_b32 v69, 0xf149f2ca\n"
    "v_mov_b32 v70, 0xf149f2ca\n"
    "v_mov_b32 v71, 0xf149f2ca\n"
    "v_mov_b32 v72, 0xf149f2ca\n"
    "v_mov_b32 v73, 0xf149f2ca\n"
    "v_mov_b32 v74, 0xf149f2ca\n"
    "v_mov_b32 v75, 0xf149f2ca\n"
    "v_mov_b32 v76, 0xf149f2ca\n"
    "v_mov_b32 v77, 0xf149f2ca\n"
    "v_mov_b32 v78, 0xf149f2ca\n"
    "v_mov_b32 v79, 0xf149f2ca\n"
    "s_nop 1\n"
    "s_branch L_kr3_%=\n"
    "L_k2_%=:\n"
    "s_nop 3\n"
    "v_mov_b32 v64, 0xf149f2ca\n"
    "v_mov_b32 v65, 0xf149f2ca\n"
    "v_mov_b32 v66, 0xf149f2ca\n"
    "v_mov_b32 v67, 0xf149f2ca\n"
    "v_mov_b32 v68, 0xf149f2ca\n"
    "v_mov_b32 v69, 0xf149f2ca\n"
    "v_mov_b32 v70, 0xf149f2ca\n"
    "v_mov_b32 v71, 0xf149f2ca\n"
    "v_mov_b32 v72, 0xf149f2ca\n"
    "v_mov_b32 v73, 0xf149f2ca\n"
    "v_mov_b32 v74, 0xf149f2ca\n"
    "v_mov_b32 v75, 0xf149f2ca\n"
    "v_mov_b32 v76, 0xf149f2ca\n"
    "v_mov_b32 v77, 0xf149f2ca\n"
    "v_mov_b32 v78, 0xf149f2ca\n"
    "v_mov_b32 v79, 0xf149f2ca\n"
    "s_nop 1\n"
    "s_branch L_kr2_%=\n"
    "L_k1_%=:\n"
    "s_nop 3\n"
    "v_mov_b32 v64, 0xf149f2ca\n"
    "v_mov_b32 v65, 0xf149f2ca\n"
    "v_mov_b32 v66, 0xf149f2ca\n"
    "v_mov_b32 v67, 0xf149f2ca\n"
    "v_mov_b32 v68, 0xf149f2ca\n"
    "v_mov_b32 v69, 0xf149f2ca\n"
    "v_mov_b32 v70, 0xf149f2ca\n"
    "v_mov_b32 v71, 0xf149f2ca\n"
    "v_mov_b32 v72, 0xf149f2ca\n"
    "v_mov_b32 v73, 0xf149f2ca\n"
    "v_mov_b32 v74, 0xf149f2ca\n"
    "v_mov_b32 v75, 0xf149f2ca\n"
    "v_mov_b32 v76, 0xf149f2ca\n"
    "v_mov_b32 v77, 0xf149f2ca\n"
    "v_mov_b32 v78, 0xf149f2ca\n"
    "v_mov_b32 v79, 0xf149f2ca\n"
    "s_nop 1\n"
    "s_branch L_kr1_%=\n"
    "L_k0_%=:\n"
    "s_nop 3\n"
    "v_mov_b32 v64, 0xf149f2ca\n"
    "v_mov_b32 v65, 0xf149f2ca\n"
    "v_mov_b32 v66, 0xf149f2ca\n"
    "v_mov_b32 v67, 0xf149f2ca\n"
    "v_mov_b32 v68, 0xf149f2ca\n"
    "v_mov_b32 v69, 0xf149f2ca\n"
    "v_mov_b32 v70, 0xf149f2ca\n"
    "v_mov_b32 v71, 0xf149f2ca\n"
    "v_mov_b32 v72, 0xf149f2ca\n"
    "v_mov_b32 v73, 0xf149f2ca\n"
    "v_mov_b32 v74, 0xf149f2ca\n"
    "v_mov_b32 v75, 0xf149f2ca\n"
    "v_mov_b32 v76, 0xf149f2ca\n"
    "v_mov_b32 v77, 0xf149f2ca\n"
    "v_mov_b32 v78, 0xf149f2ca\n"
    "v_mov_b32 v79, 0xf149f2ca\n"
    "s_nop 1\n"
    "s_branch L_kr0_%=\n"
    "L_sl0_%=:\n"
    "s_mov_b32 s20, 65536\n"
    "L_sp0_%=:\n"
    "s_mov_b64 exec, 0xfff\n"
    "buffer_load_dwordx2 v[238:239], v240, s[60:63], 0 offen offset:128 sc1\n"
    "s_mov_b64 exec, -1\n"
    "s_waitcnt vmcnt(0)\n"
    "v_cmp_ne_u64 vcc, s[98:99], v[238:239]\n"
    "s_nop 1\n"
    "s_and_b64 vcc, vcc, s[96:97]\n"
    "s_cbranch_vccz L_rt0_%=\n"
    "s_sleep 1\n"
    "s_sub_u32 s20, s20, 1\n"
    "s_cmp_lg_u32 s20, 0\n"
    "s_cbranch_scc1 L_sp0_%=\n"
    "s_branch L_rt0_%=\n"
    "L_fb%=:\n"
    "s_setprio 2\n"
    "s_nop 7\n"
    "s_nop 7\n"
    "s_waitcnt vmcnt(0) lgkmcnt(0)\n"
    "s_mov_b32 s21, 0\n"
    "v_mov_b32 v247, 0xf149f2ca\n"
    "v_mov_b32 v152, v247\n"
    "v_mov_b32 v64, 0\n"
    "v_mov_b32 v153, v247\n"
    "v_mov_b32 v65, 0\n"
    "v_mov_b32 v154, v247\n"
    "v_mov_b32 v66, 0\n"
    "v_mov_b32 v155, v247\n"
    "v_mov_b32 v67, 0\n"
    "v_mov_b32 v120, 0\n"
    "v_mov_b32 v121, 0\n"
    "v_mov_b32 v122, 0\n"
    "v_mov_b32 v123, 0\n"
    "v_mov_b32 v124, 0\n"
    "v_mov_b32 v125, 0\n"
    "v_mov_b32 v126, 0\n"
    "v_mov_b32 v127, 0\n"
    "v_mov_b32 v128, 0\n"
    "v_mov_b32 v129, 0\n"
    "v_mov_b32 v130, 0\n"
    "v_mov_b32 v131, 0\n"
    "v_mov_b32 v132, 0\n"
    "v_mov_b32 v133, 0\n"
    "v_mov_b32 v134, 0\n"
    "v_mov_b32 v135, 0\n"
    "v_mov_b32 v136, 0\n"
    "v_mov_b32 v137, 0\n"
    "v_mov_b32 v138, 0\n"
    "v_mov_b32 v139, 0\n"
    "v_mov_b32 v140, 0\n"
    "v_mov_b32 v141, 0\n"
    "v_mov_b32 v142, 0\n"
    "v_mov_b32 v143, 0\n"
    "v_mov_b32 v144, 0\n"
    "v_mov_b32 v145, 0\n"
    "v_mov_b32 v146, 0\n"
    "v_mov_b32 v147, 0\n"
    "v_mov_b32 v148, 0\n"
    "v_mov_b32 v149, 0\n"
    "v_mov_b32 v150, 0\n"
    "v_mov_b32 v151, 0\n"
    "L_fp%=:\n"
    "s_mov_b32 s20, 0\n"
    "L_fl%=:\n"
    "s_mul_i32 s31, s20, 4608\n"
    "s_sub_u32 s30, s81, s31\n"
    "s_add_u32 m0, s70, 107520\n"
    "s_nop 0\n"
    "buffer_load_dwordx4 v250, s[60:63], s30 offen offset:0 sc1 lds\n"
    "buffer_load_dwordx4 v250, s[60:63], s30 offen offset:1024 sc1 lds\n"
    "buffer_load_dwordx4 v250, s[60:63], s30 offen offset:2048 sc1 lds\n"
    "buffer_load_dwordx4 v250, s[60:63], s30 offen offset:3072 sc1 lds\n"
    "s_add_u32 m0, m0, 4096\n"
    "s_add_u32 s30, s30, 4096\n"
    "s_mov_b32 exec_hi, 0\n"
    "buffer_load_dwordx4 v250, s[60:63], s30 offen sc1 lds\n"
    "s_mov_b32 exec_hi, -1\n"
    "s_waitcnt vmcnt(0)\n"
    "ds_read2st64_b64 v[0:3], v200 offset0:0 offset1:4\n"
    "ds_read_b64 v[4:5], v200 offset:1024\n"
    "ds_read2st64_b64 v[6:9], v201 offset0:0 offset1:4\n"
    "ds_read_b64 v[10:11], v200 offset:1032\n"
    "ds_read_b64_tr_b16 v[24:25], v202 offset:0\n"
    "ds_read_b64_tr_b16 v[26:27], v202 offset:8\n"
    "ds_read_b64_tr_b16 v[28:29], v202 offset:1024\n"
    "ds_read_b64_tr_b16 v[30:31], v202 offset:1032\n"
    "ds_read_b64_tr_b16 v[32:33], v203 offset:0\n"
    "ds_read_b64_tr_b16 v[34:35], v203 offset:8\n"
    "s_waitcnt lgkmcnt(0)\n"
    "s_cmp_eq_u32 s20, 0\n"
    "s_cselect_b64 s[22:23], s[88:89], -1\n"
    "s_not_b64 s[32:33], s[88:89]\n"
    "s_cmp_eq_u32 s20, 8\n"
    "s_cselect_b64 s[22:23], s[32:33], s[22:23]\n"
    "s_cmp_eq_u32 s20, 0\n"
    "s_cselect_b64 s[24:25], s[90:91], -1\n"
    "s_not_b64 s[32:33], s[90:91]\n"
    "s_cmp_eq_u32 s20, 8\n"
    "s_cselect_b64 s[24:25], s[32:33], s[24:25]\n"
    "s_cmp_eq_u32 s20, 0\n"
    "s_cselect_b64 s[26:27], s[92:93], -1\n"
    "s_not_b64 s[32:33], s[92:93]\n"
    "s_cmp_eq_u32 s20, 8\n"
    "s_cselect_b64 s[26:27], s[32:33], s[26:27]\n"
    "s_cmp_eq_u32 s20, 0\n"
    "s_cselect_b64 s[28:29], s[94:95], -1\n"
    "s_not_b64 s[32:33], s[94:95]\n"
    "s_cmp_eq_u32 s20, 8\n"
    "s_cselect_b64 s[28:29], s[32:33], s[28:29]\n"
    "s_waitcnt vmcnt(0)\n"
    "s_cselect_b64 s[30:31], 0, -1\n"
    "s_cmp_eq_u32 s20, 0\n"
    "s_cselect_b64 s[34:35], 0, -1\n"
    "v_cndmask_b32 v96, v247, v64, s[22:23]\n"
    "v_cndmask_b32 v97, v247, v64, s[24:25]\n"
    "v_cndmask_b32 v98, v247, v64, s[26:27]\n"
    "v_cndmask_b32 v99, v247, v64, s[28:29]\n"
    "v_cndmask_b32 v100, v247, v64, s[34:35]\n"
    "v_cndmask_b32 v101, v247, v64, s[34:35]\n"
    "v_cndmask_b32 v102, v247, v64, s[34:35]\n"
    "v_cndmask_b32 v103, v247, v64, s[34:35]\n"
    "v_mfma_f32_16x16x32_bf16 v[96:99], v[0:3], v[48:51], v[96:99]\n"
    "s_nop 0\n"
    "v_mfma_f32_16x16x32_bf16 v[100:103], v[6:9], v[48:51], v[100:103]\n"
    "s_nop 5\n"
    "v_max3_f32 v152, v152, v96, v97\n"
    "v_max3_f32 v152, v152, v98, v99\n"
    "v_max3_f32 v152, v152, v100, v101\n"
    "v_max3_f32 v152, v152, v102, v103\n"
    "s_cmp_eq_u32 s21, 0\n"
    "s_cbranch_scc1 L_fs0_%=\n"
    "v_exp_f32 v96, v96\n"
    "v_exp_f32 v97, v97\n"
    "v_exp_f32 v98, v98\n"
    "v_exp_f32 v99, v99\n"
    "v_exp_f32 v100, v100\n"
    "v_exp_f32 v101, v101\n"
    "v_exp_f32 v102, v102\n"
    "v_exp_f32 v103, v103\n"
    "v_cvt_pk_bf16_f32 v112, v96, v97\n"
    "v_cvt_pk_bf16_f32 v113, v98, v99\n"
    "v_cvt_pk_bf16_f32 v114, v100, v101\n"
    "v_cvt_pk_bf16_f32 v115, v102, v103\n"
    "s_nop 1\n"
    "v_mfma_f32_16x16x32_bf16 v[120:123], v[24:27], v[112:115], v[120:123]\n"
    "v_mfma_f32_16x16x32_bf16 v[136:139], v[32:35], v[112:115], v[136:139]\n"
    "L_fs0_%=:\n"
    "s_nop 7\n"
    "v_cndmask_b32 v96, v247, v65, s[30:31]\n"
    "v_cndmask_b32 v97, v247, v65, s[30:31]\n"
    "v_cndmask_b32 v98, v247, v65, s[30:31]\n"
    "v_cndmask_b32 v99, v247, v65, s[30:31]\n"
    "v_cndmask_b32 v100, v247, v65, s[22:23]\n"
    "v_cndmask_b32 v101, v247, v65, s[24:25]\n"
    "v_cndmask_b32 v102, v247, v65, s[26:27]\n"
    "v_cndmask_b32 v103, v247, v65, s[28:29]\n"
    "v_mfma_f32_16x16x32_bf16 v[96:99], v[0:3], v[56:59], v[96:99]\n"
    "s_nop 0\n"
    "v_mfma_f32_16x16x32_bf16 v[100:103], v[6:9], v[56:59], v[100:103]\n"
    "s_nop 5\n"
    "v_max3_f32 v153, v153, v96, v97\n"
    "v_max3_f32 v153, v153, v98, v99\n"
    "v_max3_f32 v153, v153, v100, v101\n"
    "v_max3_f32 v153, v153, v102, v103\n"
    "s_cmp_eq_u32 s21, 0\n"
    "s_cbranch_scc1 L_fs1_%=\n"
    "v_exp_f32 v96, v96\n"
    "v_exp_f32 v97, v97\n"
    "v_exp_f32 v98, v98\n"
    "v_exp_f32 v99, v99\n"
    "v_exp_f32 v100, v100\n"
    "v_exp_f32 v101, v101\n"
    "v_exp_f32 v102, v102\n"
    "v_exp_f32 v103, v103\n"
    "v_cvt_pk_bf16_f32 v112, v96, v97\n"
    "v_cvt_pk_bf16_f32 v113, v98, v99\n"
    "v_cvt_pk_bf16_f32 v114, v100, v101\n"
    "v_cvt_pk_bf16_f32 v115, v102, v103\n"
    "s_nop 1\n"
    "v_mfma_f32_16x16x32_bf16 v[124:127], v[24:27], v[112:115], v[124:127]\n"
    "v_mfma_f32_16x16x32_bf16 v[140:143], v[32:35], v[112:115], v[140:143]\n"
    "L_fs1_%=:\n"
    "s_nop 7\n"
    "v_cndmask_b32 v96, v247, v66, s[22:23]\n"
    "v_cndmask_b32 v97, v247, v66, s[24:25]\n"
    "v_cndmask_b32 v98, v247, v66, s[26:27]\n"
    "v_cndmask_b32 v99, v247, v66, s[28:29]\n"
    "v_cndmask_b32 v100, v247, v66, s[34:35]\n"
    "v_cndmask_b32 v101, v247, v66, s[34:35]\n"
    "v_cndmask_b32 v102, v247, v66, s[34:35]\n"
    "v_cndmask_b32 v103, v247, v66, s[34:35]\n"
    "v_mfma_f32_16x16x32_bf16 v[96:99], v[2:5], v[52:55], v[96:99]\n"
    "s_nop 0\n"
    "v_mfma_f32_16x16x32_bf16 v[100:103], v[8:11], v[52:55], v[100:103]\n"
    "s_nop 5\n"
    "v_max3_f32 v154, v154, v96, v97\n"
    "v_max3_f32 v154, v154, v98, v99\n"
    "v_max3_f32 v154, v154, v100, v101\n"
    "v_max3_f32 v154, v154, v102, v103\n"
    "s_cmp_eq_u32 s21, 0\n"
    "s_cbranch_scc1 L_fs2_%=\n"
    "v_exp_f32 v96, v96\n"
    "v_exp_f32 v97, v97\n"
    "v_exp_f32 v98, v98\n"
    "v_exp_f32 v99, v99\n"
    "v_exp_f32 v100, v100\n"
    "v_exp_f32 v101, v101\n"
    "v_exp_f32 v102, v102\n"
    "v_exp_f32 v103, v103\n"
    "v_cvt_pk_bf16_f32 v112, v96, v97\n"
    "v_cvt_pk_bf16_f32 v113, v98, v99\n"
    "v_cvt_pk_bf16_f32 v114, v100, v101\n"
    "v_cvt_pk_bf16_f32 v115, v102, v103\n"
    "s_nop 1\n"
    "v_mfma_f32_16x16x32_bf16 v[128:131], v[28:31], v[112:115], v[128:131]\n"
    "v_mfma_f32_16x16x32_bf16 v[144:147], v[32:35], v[112:115], v[144:147]\n"
    "L_fs2_%=:\n"
    "s_nop 7\n"
    "v_cndmask_b32 v96, v247, v67, s[30:31]\n"
    "v_cndmask_b32 v97, v247, v67, s[30:31]\n"
    "v_cndmask_b32 v98, v247, v67, s[30:31]\n"
    "v_cndmask_b32 v99, v247, v67, s[30:31]\n"
    "v_cndmask_b32 v100, v247, v67, s[22:23]\n"
    "v_cndmask_b32 v101, v247, v67, s[24:25]\n"
    "v_cndmask_b32 v102, v247, v67, s[26:27]\n"
    "v_cndmask_b32 v103, v247, v67, s[28:29]\n"
    "v_mfma_f32_16x16x32_bf16 v[96:99], v[2:5], v[60:63], v[96:99]\n"
    "s_nop 0\n"
    "v_mfma_f32_16x16x32_bf16 v[100:103], v[8:11], v[60:63], v[100:103]\n"
    "s_nop 5\n"
    "v_max3_f32 v155, v155, v96, v97\n"
    "v_max3_f32 v155, v155, v98, v99\n"
    "v_max3_f32 v155, v155, v100, v101\n"
    "v_max3_f32 v155, v155, v102, v103\n"
    "s_cmp_eq_u32 s21, 0\n"
    "s_cbranch_scc1 L_fs3_%=\n"
    "v_exp_f32 v96, v96\n"
    "v_exp_f32 v97, v97\n"
    "v_exp_f32 v98, v98\n"
    "v_exp_f32 v99, v99\n"
    "v_exp_f32 v100, v100\n"
    "v_exp_f32 v101, v101\n"
    "v_exp_f32 v102, v102\n"
    "v_exp_f32 v103, v103\n"
    "v_cvt_pk_bf16_f32 v112, v96, v97\n"
    "v_cvt_pk_bf16_f32 v113, v98, v99\n"
    "v_cvt_pk_bf16_f32 v114, v100, v101\n"
    "v_cvt_pk_bf16_f32 v115, v102, v103\n"
    "s_nop 1\n"
    "v_mfma_f32_16x16x32_bf16 v[132:135], v[28:31], v[112:115], v[132:135]\n"
    "v_mfma_f32_16x16x32_bf16 v[148:151], v[32:35], v[112:115], v[148:151]\n"
    "L_fs3_%=:\n"
    "s_nop 7\n"
    "s_cmp_eq_u32 s20, s83\n"
    "s_cbranch_scc1 L_fd%=\n"
    "s_add_u32 s20, s20, 1\n"
    "s_cmp_le_u32 s20, 8\n"
    "s_cbranch_scc1 L_fl%=\n"
    "L_fd%=:\n"
    "s_nop 7\n"
    "s_cmp_eq_u32 s21, 1\n"
    "s_cbranch_scc1 L_fe%=\n"
    "v_mov_b32 v156, v152\n"
    "v_mov_b32 v157, v153\n"
    "v_mov_b32 v158, v154\n"
    "v_mov_b32 v159, v155\n"
    "v_permlane32_swap_b32 v152, v156\n"
    "v_permlane32_swap_b32 v153, v157\n"
    "v_permlane32_swap_b32 v154, v158\n"
    "v_permlane32_swap_b32 v155, v159\n"
    "v_max_f32 v152, v152, v156\n"
    "v_max_f32 v153, v153, v157\n"
    "v_max_f32 v154, v154, v158\n"
    "v_max_f32 v155, v155, v159\n"
    "v_mov_b32 v156, v152\n"
    "v_mov_b32 v157, v153\n"
    "v_mov_b32 v158, v154\n"
    "v_mov_b32 v159, v155\n"
    "v_permlane16_swap_b32 v152, v156\n"
    "v_permlane16_swap_b32 v153, v157\n"
    "v_permlane16_swap_b32 v154, v158\n"
    "v_permlane16_swap_b32 v155, v159\n"
    "v_max_f32 v152, v152, v156\n"
    "v_max_f32 v153, v153, v157\n"
    "v_max_f32 v154, v154, v158\n"
    "v_max_f32 v155, v155, v159\n"
    "v_sub_f32 v64, 0, v152\n"
    "v_sub_f32 v65, 0, v153\n"
    "v_sub_f32 v66, 0, v154\n"
    "v_sub_f32 v67, 0, v155\n"
    "s_mov_b32 s21, 1\n"
    "s_branch L_fp%=\n"
    "L_fe%=:\n"
    "v_mov_b32 v152, v136\n"
    "v_mov_b32 v160, v136\n"
    "v_mov_b32 v153, v140\n"
    "v_mov_b32 v161, v140\n"
    "v_mov_b32 v154, v144\n"
    "v_mov_b32 v162, v144\n"
    "v_mov_b32 v155, v148\n"
    "v_mov_b32 v163, v148\n"
    "v_permlane16_swap_b32 v160, v152\n"
    "v_permlane16_swap_b32 v161, v153\n"
    "v_permlane16_swap_b32 v162, v154\n"
    "v_permlane16_swap_b32 v163, v155\n"
    "s_setprio 0\n"
    "s_nop 1\n"
    "s_branch L_fbr%=\n"
    "L_end%=:\n"
    ".if L_st2_%=-L_st1_%= != 4096\n.error \"stub spacing\"\n.endif\n"
    ".if L_st3_%=-L_st1_%= != 8192\n.error \"stub spacing\"\n.endif\n"
    ".if L_st4_%=-L_st1_%= != 12288\n.error \"stub spacing\"\n.endif\n"
    ".if L_st5_%=-L_st1_%= != 16384\n.error \"stub spacing\"\n.endif\n"
    ".if L_st6_%=-L_st1_%= != 20480\n.error \"stub spacing\"\n.endif\n"
    ".if L_st7_%=-L_st1_%= != 24576\n.error \"stub spacing\"\n.endif\n"
    ".if L_st8_%=-L_st1_%= != 28672\n.error \"stub spacing\"\n.endif\n"
    ".if L_st9_%=-L_st1_%= != 32768\n.error \"stub spacing\"\n.endif\n"
    ".if L_st10_%=-L_st1_%= != 36864\n.error \"stub spacing\"\n.endif\n"
    ".if L_st11_%=-L_st1_%= != 40960\n.error \"stub spacing\"\n.endif\n"
    ".if L_st12_%=-L_st1_%= != 45056\n.error \"stub spacing\"\n.endif\n"
    ".if L_st13_%=-L_st1_%= != 49152\n.error \"stub spacing\"\n.endif\n"
    ".if L_st14_%=-L_st1_%= != 53248\n.error \"stub spacing\"\n.endif\n"
    :
    : "s"(__builtin_amdgcn_kernarg_segment_ptr()), "s"(blockIdx.x), "s"(wid)
    : "v0", "v1", "v2", "v3", "v4", "v5", "v6", "v7", "v8", "v9", "v10", "v11", "v12", "v13", "v14", "v15", "v16", "v17", "v18", "v19", "v20", "v21", "v22", "v23", "v24", "v25", "v26", "v27", "v28", "v29", "v30", "v31", "v32", "v33", "v34", "v35", "v36", "v37", "v38", "v39", "v40", "v41", "v42", "v43", "v44", "v45", "v46", "v47", "v48", "v49", "v50", "v51", "v52", "v53", "v54", "v55", "v56", "v57", "v58", "v59", "v60", "v61", "v62", "v63", "v64", "v65", "v66", "v67", "v68", "v69", "v70", "v71", "v72", "v73", "v74", "v75", "v76", "v77", "v78", "v79", "v80", "v81", "v82", "v83", "v84", "v85", "v86", "v87", "v88", "v89", "v90", "v91", "v92", "v93", "v94", "v95", "v96", "v97", "v98", "v99", "v100", "v101", "v102", "v103", "v104", "v105", "v106", "v107", "v108", "v109", "v110", "v111", "v112", "v113", "v114", "v115", "v116", "v117", "v118", "v119", "v120", "v121", "v122", "v123", "v124", "v125", "v126", "v127", "v128", "v129", "v130", "v131", "v132", "v133", "v134", "v135", "v136", "v137", "v138", "v139", "v140", "v141", "v142", "v143", "v144", "v145", "v146", "v147", "v148", "v149", "v150", "v151", "v152", "v153", "v154", "v155", "v156", "v157", "v158", "v159", "v160", "v161", "v162", "v163", "v164", "v165", "v166", "v167", "v168", "v169", "v170", "v171", "v172", "v173", "v174", "v175", "v176", "v177", "v178", "v179", "v180", "v181", "v182", "v183", "v184", "v185", "v186", "v187", "v188", "v189", "v190", "v191", "v192", "v193", "v194", "v195", "v196", "v197", "v198", "v199", "v200", "v201", "v202", "v203", "v204", "v205", "v206", "v207", "v208", "v209", "v210", "v211", "v212", "v213", "v214", "v215", "v216", "v217", "v218", "v219", "v220", "v221", "v222", "v223", "v224", "v225", "v226", "v227", "v228", "v229", "v230", "v231", "v232", "v233", "v234", "v235", "v236", "v237", "v238", "v239", "v240", "v241", "v242", "v243", "v244", "v245", "v246", "v247", "v248", "v249", "v250", "v251", "v252", "v253", "v254", "v255",
      "s4", "s5", "s6", "s7", "s8", "s9", "s10", "s11", "s12", "s13", "s14", "s15", "s16", "s17", "s18", "s19", "s20", "s21", "s22", "s23", "s24", "s25", "s26", "s27", "s28", "s29", "s30", "s31", "s32", "s33", "s34", "s35", "s36", "s37", "s38", "s39", "s40", "s41", "s42", "s43", "s44", "s45", "s46", "s47", "s48", "s49", "s50", "s51", "s52", "s53", "s54", "s55", "s56", "s57", "s58", "s59", "s60", "s61", "s62", "s63", "s64", "s65", "s66", "s67", "s68", "s69", "s70", "s71", "s72", "s73", "s74", "s75", "s76", "s77", "s78", "s79", "s80", "s81", "s82", "s83", "s84", "s85", "s86", "s87", "s88", "s89", "s90", "s91", "s92", "s93", "s94", "s95", "s96", "s97", "s98", "s99", "vcc", "scc", "memory");
}

extern "C" void kernel_launch(void* const* d_in, const int* in_sizes, int n_in, void* d_out, int out_size, void* d_ws,
                              size_t ws_size, hipStream_t stream) {
  parallel_sum__sliding_block__dense_activation_relu__prompt_kernel<<<256, 512, 0, stream>>>(d_in[0], d_in[1], d_in[2], d_in[4], d_in[5], d_out, d_ws);
}
```
